```python
import torch

def _softmax(x, axis=-1):
    return torch.softmax(x, dim=axis)
def _gelu(x):
    return torch.nn.functional.gelu(x, approximate="tanh")
def _silu(x):
    return torch.nn.functional.silu(x)
def _sigmoid(x):
    return torch.sigmoid(x)
def _layernorm(x, axis=-1, eps=1e-5):
    m = x.mean(dim=axis, keepdim=True)
    v = x.var(dim=axis, keepdim=True, unbiased=False)
    return (x - m) / torch.sqrt(v + eps)
def _scatter_add(tgt, idx, src, axis=0):
    return tgt.clone().index_add_(axis, idx, src)
def _segment_sum(src, seg, n):
    out = torch.zeros((n,) + src.shape[1:], dtype=src.dtype)
    return out.index_add_(0, seg, src)
def _pad(x, pw):
    flat = tuple(v for pair in reversed(pw) for v in pair)
    return torch.nn.functional.pad(x, flat)

kernel_name = 'concat_heads__gqa_block_cached__mha_block_cached__near_ridge'

def setup_inputs():

    _w = (lambda a: (lambda t: t.to(torch.bfloat16) if t.is_floating_point() else t)(torch.as_tensor(a)))
    def _A_setup():
        B, Sq, D = 8, 112, 640
        Hq, Hkv, Sk, Dh = 32, 32, 512, 192
        assert Hq % Hkv == 0, (Hq, Hkv)
        return {
            "x": _w((lambda *s: torch.randn(*s, dtype=torch.bfloat16) * 0.8)(B, Sq, D)),
            "k_cache": _w((lambda *s: torch.randn(*s, dtype=torch.bfloat16) * 0.8)(B, Hkv, Sk, Dh)),
            "v_cache": _w((lambda *s: torch.randn(*s, dtype=torch.bfloat16) * 0.05)(B, Hkv, Sk, Dh)),
            "wq": _w((lambda *s: torch.randn(*s, dtype=torch.bfloat16) * 0.8)(D, Hq * Dh) * (D ** -0.5)),
            "wo": _w((lambda *s: torch.randn(*s, dtype=torch.bfloat16) * 0.05)(Hq * Dh, D) * ((Hq * Dh) ** -0.5)),
        }
    _A_in = _A_setup()

    def _B_setup():
        B, Sq, D = 8, 112, 640
        H, Sk, Dh = 32, 512, 192
        return {
            "x": _w((lambda *s: torch.randn(*s, dtype=torch.bfloat16) * 0.8)(B, Sq, D)),
            "k_cache": _w((lambda *s: torch.randn(*s, dtype=torch.bfloat16) * 0.8)(B, H, Sk, Dh)),
            "v_cache": _w((lambda *s: torch.randn(*s, dtype=torch.bfloat16) * 0.05)(B, H, Sk, Dh)),
            "wq": _w((lambda *s: torch.randn(*s, dtype=torch.bfloat16) * 0.8)(D, H * Dh) * (D ** -0.5)),
            "wo": _w((lambda *s: torch.randn(*s, dtype=torch.bfloat16) * 0.05)(H * Dh, D) * ((H * Dh) ** -0.5)),
        }
    _B_in = _B_setup()

    return {
        "A_x": _A_in["x"],
        "A_k_cache": _A_in["k_cache"],
        "A_v_cache": _A_in["v_cache"],
        "A_wq": _A_in["wq"],
        "A_wo": _A_in["wo"],
        "B_x": _B_in["x"],
        "B_k_cache": _B_in["k_cache"],
        "B_v_cache": _B_in["v_cache"],
        "B_wq": _B_in["wq"],
        "B_wo": _B_in["wo"],
    }

def reference(A_x, A_k_cache, A_v_cache, A_wq, A_wo, B_x, B_k_cache, B_v_cache, B_wq, B_wo):
    def _A_ref(x, k_cache, v_cache, wq, wo):
        B, Sq, D = x.shape
        Hq, Dh = 32, 192
        Hkv = k_cache.shape[1]
        rep = Hq // Hkv
        q = (lambda x, *d: x.permute(*d))((x @ wq).reshape(B, Sq, Hq, Dh), 0, 2, 1, 3)
        k = (lambda x, n, axis: torch.repeat_interleave(x, n, dim=axis))(k_cache, rep, axis=1)
        v = (lambda x, n, axis: torch.repeat_interleave(x, n, dim=axis))(v_cache, rep, axis=1)
        scores = torch.einsum("bhqd,bhkd->bhqk", q, k) * (Dh ** -0.5)
        p = _softmax(scores, axis=-1)
        o = torch.einsum("bhqk,bhkd->bhqd", p, v)
        return (lambda x, *d: x.permute(*d))(o, 0, 2, 1, 3).reshape(B, Sq, Hq * Dh) @ wo

    def _B_ref(x, k_cache, v_cache, wq, wo):
        B, Sq, D = x.shape
        H, Dh = 32, 192
        q = (lambda x, *d: x.permute(*d))((x @ wq).reshape(B, Sq, H, Dh), 0, 2, 1, 3)
        scores = torch.einsum("bhqd,bhkd->bhqk", q, k_cache) * (Dh ** -0.5)
        p = _softmax(scores, axis=-1)
        o = torch.einsum("bhqk,bhkd->bhqd", p, v_cache)
        return (lambda x, *d: x.permute(*d))(o, 0, 2, 1, 3).reshape(B, Sq, H * Dh) @ wo

    a = _A_ref(A_x, A_k_cache, A_v_cache, A_wq, A_wo)
    b = _B_ref(B_x, B_k_cache, B_v_cache, B_wq, B_wo)
    return torch.concatenate([a, b], axis=-1)
```

```cpp
#include <hip/hip_runtime.h>
#include <hip/hip_bf16.h>
#include <stdint.h>

typedef unsigned short u16;
typedef short bf16x8 __attribute__((ext_vector_type(8)));
typedef short s16x4 __attribute__((ext_vector_type(4)));
typedef float f32x16 __attribute__((ext_vector_type(16)));
typedef float f32x4 __attribute__((ext_vector_type(4)));
typedef float f32x2 __attribute__((ext_vector_type(2)));
typedef __bf16 bf16x2_t __attribute__((ext_vector_type(2)));
typedef unsigned u32x4 __attribute__((ext_vector_type(4)));
#define LAS __attribute__((address_space(3)))
#define DI __device__ __forceinline__
#define MFMA(a, b, c) __builtin_amdgcn_mfma_f32_32x32x16_bf16(a, b, c, 0, 0, 0)

constexpr int NB = 8, SQ = 112, DM = 640, NH = 32, SK = 512, DH = 192, HD = NH * DH;
constexpr int SLOT = 24576;
constexpr int LDS1 = 3 * SLOT;
constexpr int SLOT2 = 20480;
constexpr int LDS2 = 3 * SLOT2;
constexpr size_t OWS_BYTES = (size_t)2 * 28 * 384 * 1024;
constexpr size_t SLAB_ONE = (size_t)2 * 28 * 20 * 4096;
constexpr float QSCALE = 0.07216878364870322f * 1.4426950408889634f;

DI unsigned rfl(unsigned x) { return __builtin_amdgcn_readfirstlane(x); }
DI unsigned cvtpk(float lo, float hi) { f32x2 v = {lo, hi}; bf16x2_t b = __builtin_convertvector(v, bf16x2_t); return __builtin_bit_cast(unsigned, b); }
DI s16x4 trd(unsigned a) { return __builtin_bit_cast(s16x4, __builtin_amdgcn_ds_read_tr16_b64_v4i16((LAS s16x4*)a)); }
DI bf16x8 cat8(s16x4 lo, s16x4 hi) { return (bf16x8){lo[0], lo[1], lo[2], lo[3], hi[0], hi[1], hi[2], hi[3]}; }
DI bf16x8 ldsr128(unsigned a) { return *(const LAS bf16x8*)a; }
DI bf16x8 pack8(const f32x16& v, int o, float s) {
  u32x4 w = {cvtpk(v[o] * s, v[o + 1] * s), cvtpk(v[o + 2] * s, v[o + 3] * s), cvtpk(v[o + 4] * s, v[o + 5] * s), cvtpk(v[o + 6] * s, v[o + 7] * s)};
  return __builtin_bit_cast(bf16x8, w);
}
DI bf16x8 pack8n(const f32x16& v, int o) {
  u32x4 w = {cvtpk(v[o], v[o + 1]), cvtpk(v[o + 2], v[o + 3]), cvtpk(v[o + 4], v[o + 5]), cvtpk(v[o + 6], v[o + 7])};
  return __builtin_bit_cast(bf16x8, w);
}
DI int fswz(int row16) { return (((row16 >> 1) & 1) << 2) | ((row16 >> 2) & 3); }

DI void dma3(unsigned m0v, unsigned v0, unsigned v1, unsigned v2, __amdgpu_buffer_rsrc_t srd, unsigned soff) {
  asm volatile(
      "s_mov_b32 m0, %0\n\ts_nop 3\n\t"
      "buffer_load_dwordx4 %1, %4, %5 offen lds\n\t"
      "buffer_load_dwordx4 %2, %4, %5 offen offset:1024 lds\n\t"
      "buffer_load_dwordx4 %3, %4, %5 offen offset:2048 lds"
      :: "s"(m0v), "v"(v0), "v"(v1), "v"(v2), "s"(srd), "s"(soff) : "m0", "memory");
}
DI void dma1(unsigned m0v, unsigned v0, __amdgpu_buffer_rsrc_t srd, unsigned soff) {
  asm volatile("s_mov_b32 m0, %0\n\ts_nop 3\n\tbuffer_load_dwordx4 %1, %2, %3 offen lds"
               :: "s"(m0v), "v"(v0), "s"(srd), "s"(soff) : "m0", "memory");
}
template <int OFF> DI void gld(bf16x8& d, const void* p) {
  asm volatile("global_load_dwordx4 %0, %1, off offset:%2" : "=v"(d) : "v"(p), "i"(OFF) : "memory");
}
#define WAITBAR(N) asm volatile("s_waitcnt vmcnt(" #N ") lgkmcnt(0)\n\ts_barrier" ::: "memory")
#define WAITBAR_X(N, X) asm volatile("s_waitcnt vmcnt(" #N ") lgkmcnt(0)\n\ts_barrier" : "+v"(X[0]), "+v"(X[1]), "+v"(X[2]), "+v"(X[3]) :: "memory")

__global__ __launch_bounds__(256, 2) void attn_kernel(const u16* __restrict__ xA, const u16* __restrict__ kA, const u16* __restrict__ vA, const u16* __restrict__ wqA,
                                                      const u16* __restrict__ xB, const u16* __restrict__ kB, const u16* __restrict__ vB, const u16* __restrict__ wqB,
                                                      char* __restrict__ ows) {
  extern __shared__ __attribute__((aligned(16))) char lds[];
  const int tid = threadIdx.x, lane = tid & 63;
  const int wid = rfl(tid >> 6);
  const int r32 = lane & 31, hi = lane >> 5;
  const int xcd = blockIdx.x & 7, idx = blockIdx.x >> 3;
  const int h = 4 * xcd + (idx & 3), b = (idx >> 2) & 7, br = idx >> 5;
  const u16* x = br ? xB : xA; const u16* kc = br ? kB : kA; const u16* vc = br ? vB : vA; const u16* wq = br ? wqB : wqA;
  const size_t kvoff = (size_t)(b * NH + h) * SK * DH;
  __amdgpu_buffer_rsrc_t srdW = __builtin_amdgcn_make_buffer_rsrc((void*)(wq + h * DH), (short)0, (DM * HD - h * DH) * 2, 0x00020000);
  __amdgpu_buffer_rsrc_t srdK = __builtin_amdgcn_make_buffer_rsrc((void*)(kc + kvoff), (short)0, SK * DH * 2, 0x00020000);
  __amdgpu_buffer_rsrc_t srdV = __builtin_amdgcn_make_buffer_rsrc((void*)(vc + kvoff), (short)0, SK * DH * 2, 0x00020000);
  const unsigned lds0 = (unsigned)(uintptr_t)lds;
  const int half = wid >> 1, g0 = 2 * (wid & 1);
  unsigned vkv[3], vw[3];
#pragma unroll
  for (int k = 0; k < 3; ++k) {
    const int id = 64 * k + lane, rl = id / 24, rem = id % 24, f = fswz(8 * half + rl);
    const int ch = (rem & 7) ^ f;
    vkv[k] = (unsigned)(((lane & ~7) | ch) << 4);
    vw[k] = (unsigned)(rl * (HD * 2) + (rem >> 3) * 128 + ch * 16 - 1024 * k);
  }
  const unsigned wbase = (unsigned)(g0 * 6144 + half * 3072);
  const unsigned wrow = (unsigned)(16 * g0 + 8 * half);
  auto issueW = [&](int t, int slot) {
    const unsigned m = lds0 + slot * SLOT + wbase, so = (64 * t + wrow) * (HD * 2);
    dma3(m, vw[0], vw[1], vw[2], srdW, so);
    dma3(m + 6144, vw[0], vw[1], vw[2], srdW, so + 16 * HD * 2);
  };
  auto issueKV = [&](__amdgpu_buffer_rsrc_t srd, unsigned tile, unsigned slotoff) {
    const unsigned m = lds0 + slotoff + wbase, so = tile * SLOT + wbase;
    dma3(m, vkv[0], vkv[1], vkv[2], srd, so);
    dma3(m + 6144, vkv[0], vkv[1], vkv[2], srd, so + 6144);
  };
  const int q = wid * 32 + r32, qc = q < SQ ? q : SQ - 1;
  const u16* xp = x + (size_t)(b * SQ + qc) * DM + 8 * hi;
  const int dhalf = (lane >> 4) & 1, tq = (lane & 15) >> 2, tp = lane & 3;
  unsigned aW[2][2], aV[2][2], aK[4];
#pragma unroll
  for (int a = 0; a < 2; ++a)
#pragma unroll
    for (int sec = 0; sec < 2; ++sec) {
      const int ch = 4 * a + 2 * dhalf + (tp & 1);
      const int rw = 8 * hi + 4 * sec + tq, rv = 4 * hi + 8 * sec + tq;
      aW[a][sec] = lds0 + rw * 384 + ((ch ^ fswz(rw)) << 4) + 8 * (tp >> 1);
      aV[a][sec] = lds0 + rv * 384 + ((ch ^ fswz(rv)) << 4) + 8 * (tp >> 1);
    }
#pragma unroll
  for (int m = 0; m < 4; ++m) aK[m] = lds0 + r32 * 384 + (((2 * m + hi) ^ fswz(r32 & 15)) << 4);

  bf16x8 xf[3][4];
#define XLOAD(t) do { gld<128 * (t)>(xf[(t) % 3][0], xp); gld<128 * (t) + 32>(xf[(t) % 3][1], xp); gld<128 * (t) + 64>(xf[(t) % 3][2], xp); gld<128 * (t) + 96>(xf[(t) % 3][3], xp); } while (0)
  f32x16 acc[6];
#pragma unroll
  for (int i = 0; i < 6; ++i) acc[i] = f32x16{};
  issueW(0, 0); XLOAD(0);
  issueW(1, 1); XLOAD(1);
#define G1STEP(t, N, NEXT) do { \
    WAITBAR_X(N, xf[(t) % 3]); \
    NEXT; \
    _Pragma("unroll") for (int ks = 0; ks < 4; ++ks) \
    _Pragma("unroll") for (int dt = 0; dt < 6; ++dt) { \
      const unsigned o_ = ((t) % 3) * SLOT + ks * 6144 + (dt >> 1) * 128; \
      acc[dt] = MFMA(cat8(trd(aW[dt & 1][0] + o_), trd(aW[dt & 1][1] + o_)), xf[(t) % 3][ks], acc[dt]); } \
  } while (0)
  G1STEP(0, 10, issueW(2, 2); XLOAD(2));
  G1STEP(1, 10, issueW(3, 0); XLOAD(3));
  G1STEP(2, 10, issueW(4, 1); XLOAD(4));
  G1STEP(3, 10, issueW(5, 2); XLOAD(5));
  G1STEP(4, 10, issueW(6, 0); XLOAD(6));
  G1STEP(5, 10, issueW(7, 1); XLOAD(7));
  G1STEP(6, 10, issueW(8, 2); XLOAD(8));
  G1STEP(7, 10, issueW(9, 0); XLOAD(9));
  G1STEP(8, 10, issueKV(srdK, 0, 1 * SLOT));
  G1STEP(9, 6, issueKV(srdV, 0, 2 * SLOT));
  bf16x8 qf[12];
#pragma unroll
  for (int dt = 0; dt < 6; ++dt) { qf[2 * dt] = pack8(acc[dt], 0, QSCALE); qf[2 * dt + 1] = pack8(acc[dt], 8, QSCALE); }
#pragma unroll
  for (int i = 0; i < 6; ++i) acc[i] = f32x16{};
  float m_run = -1e30f, l_run = 0.f;
  unsigned s0 = 1 * SLOT, s1 = 2 * SLOT, s2 = 0;
  for (int u = 0; u < 8; ++u) {
    WAITBAR(6);
    issueKV(srdK, u + 1, s2);
    f32x16 p0 = f32x16{}, p1 = f32x16{};
#pragma unroll
    for (int s = 0; s < 12; ++s) {
      const unsigned a = aK[s & 3] + s0 + (s >> 2) * 128;
      p0 = MFMA(ldsr128(a), qf[s], p0);
      p1 = MFMA(ldsr128(a + 12288), qf[s], p1);
    }
    float mx = fmaxf(p0[0], p1[0]);
#pragma unroll
    for (int r = 1; r < 16; ++r) mx = fmaxf(mx, fmaxf(p0[r], p1[r]));
    { auto rr = __builtin_amdgcn_permlane32_swap(__float_as_uint(mx), __float_as_uint(mx), false, false);
      mx = fmaxf(__uint_as_float(rr[0]), __uint_as_float(rr[1])); }
    const float mn = fmaxf(m_run, mx);
    const float alpha = __builtin_amdgcn_exp2f(m_run - mn);
    m_run = mn;
    float ps = 0.f;
#pragma unroll
    for (int r = 0; r < 16; ++r) { p0[r] = __builtin_amdgcn_exp2f(p0[r] - mn); p1[r] = __builtin_amdgcn_exp2f(p1[r] - mn); ps += p0[r] + p1[r]; }
    l_run = l_run * alpha + ps;
    bf16x8 pf[4] = {pack8n(p0, 0), pack8n(p0, 8), pack8n(p1, 0), pack8n(p1, 8)};
    if (__any(alpha < 1.f)) {
#pragma unroll
      for (int dt = 0; dt < 6; ++dt)
#pragma unroll
        for (int r = 0; r < 16; ++r) acc[dt][r] *= alpha;
    }
    WAITBAR(6);
    issueKV(srdV, u + 1, s0);
#pragma unroll
    for (int ks = 0; ks < 4; ++ks)
#pragma unroll
      for (int dt = 0; dt < 6; ++dt) {
        const unsigned o_ = s1 + ks * 6144 + (dt >> 1) * 128;
        acc[dt] = MFMA(cat8(trd(aV[dt & 1][0] + o_), trd(aV[dt & 1][1] + o_)), pf[ks], acc[dt]);
      }
    const unsigned t_ = s0; s0 = s2; s2 = s1; s1 = t_;
  }
  { auto rr = __builtin_amdgcn_permlane32_swap(__float_as_uint(l_run), __float_as_uint(l_run), false, false);
    l_run = __uint_as_float(rr[0]) + __uint_as_float(rr[1]); }
  const float inv = 1.f / l_run;
  if (q < SQ) {
    const int R = SQ * b + q;
    char* op = ows + ((size_t)((br * 28 + (R >> 5)) * 384 + h * 12) * 64 + (R & 31) + 32 * hi) * 16;
#pragma unroll
    for (int dt = 0; dt < 6; ++dt) {
      *(bf16x8*)(op + (2 * dt) * 1024) = pack8(acc[dt], 0, inv);
      *(bf16x8*)(op + (2 * dt + 1) * 1024) = pack8(acc[dt], 8, inv);
    }
  }
  asm volatile("s_waitcnt vmcnt(0)" ::: "memory");
}

__global__ __launch_bounds__(256, 2) void oproj_kernel(const u16* __restrict__ woA, const u16* __restrict__ woB, const char* __restrict__ ows, char* __restrict__ slab) {
  extern __shared__ __attribute__((aligned(16))) char lds[];
  const int tid = threadIdx.x, lane = tid & 63;
  const int wid = rfl(tid >> 6);
  const int hi = lane >> 5;
  const int ks8 = blockIdx.x & 7, idx = blockIdx.x >> 3;
  const int mg = idx % 7, ng = (idx / 7) & 3, br = idx / 28;
  const u16* wo = br ? woB : woA;
  const size_t wofs = (size_t)ks8 * 768 * DM + ng * 160;
  __amdgpu_buffer_rsrc_t srd = __builtin_amdgcn_make_buffer_rsrc((void*)(wo + wofs), (short)0, 768 * DM * 2 - ng * 320, 0x00020000);
  const unsigned lds0 = (unsigned)(uintptr_t)lds;
  unsigned vo[5];
#pragma unroll
  for (int k = 0; k < 5; ++k) { const int id = 64 * k + lane; vo[k] = (unsigned)((id / 20) * (DM * 2) + (id % 20) * 16); }
  auto issue = [&](int t, int slot) {
    const unsigned m = lds0 + slot * SLOT2 + wid * 5120, so = (64 * t + 16 * wid) * (DM * 2);
#pragma unroll
    for (int k = 0; k < 5; ++k) dma1(m + 1024 * k, vo[k], srd, so);
  };
  const int T = 4 * mg + wid;
  const char* op = ows + ((size_t)((br * 28 + T) * 384 + 48 * ks8) * 64 + lane) * 16;
  const int dhalf = (lane >> 4) & 1, tq = (lane & 15) >> 2, tp = lane & 3;
  const unsigned a0 = lds0 + (8 * hi + tq) * 320 + dhalf * 32 + (tp & 1) * 16 + (tp >> 1) * 8;
  bf16x8 of[3][4];
#define OLOAD(t) do { const char* p_ = op + (t) * 4096; gld<0>(of[(t) % 3][0], p_); gld<1024>(of[(t) % 3][1], p_); gld<2048>(of[(t) % 3][2], p_); gld<3072>(of[(t) % 3][3], p_); } while (0)
  f32x16 acc[5];
#pragma unroll
  for (int i = 0; i < 5; ++i) acc[i] = f32x16{};
  issue(0, 0); OLOAD(0);
  issue(1, 1); OLOAD(1);
#define G4STEP(t, N, NEXT) do { \
    WAITBAR_X(N, of[(t) % 3]); \
    NEXT; \
    _Pragma("unroll") for (int ks = 0; ks < 4; ++ks) \
    _Pragma("unroll") for (int nt = 0; nt < 5; ++nt) { \
      const unsigned o_ = a0 + ((t) % 3) * SLOT2 + ks * 16 * 320 + nt * 64; \
      acc[nt] = MFMA(cat8(trd(o_), trd(o_ + 4 * 320)), of[(t) % 3][ks], acc[nt]); } \
  } while (0)
  G4STEP(0, 9, issue(2, 2); OLOAD(2));
  G4STEP(1, 9, issue(3, 0); OLOAD(3));
  G4STEP(2, 9, issue(4, 1); OLOAD(4));
  G4STEP(3, 9, issue(5, 2); OLOAD(5));
  G4STEP(4, 9, issue(6, 0); OLOAD(6));
  G4STEP(5, 9, issue(7, 1); OLOAD(7));
  G4STEP(6, 9, issue(8, 2); OLOAD(8));
  G4STEP(7, 9, issue(9, 0); OLOAD(9));
  G4STEP(8, 9, issue(10, 1); OLOAD(10));
  G4STEP(9, 9, issue(11, 2); OLOAD(11));
  G4STEP(10, 9, );
  G4STEP(11, 0, );
  char* sp = slab + ks8 * SLAB_ONE + ((size_t)((br * 28 + T) * 20 + 5 * ng) * 4096) + lane * 16;
#pragma unroll
  for (int nt = 0; nt < 5; ++nt)
#pragma unroll
    for (int rq = 0; rq < 4; ++rq)
      *(f32x4*)(sp + nt * 4096 + rq * 1024) = (f32x4){acc[nt][4 * rq], acc[nt][4 * rq + 1], acc[nt][4 * rq + 2], acc[nt][4 * rq + 3]};
}

__global__ __launch_bounds__(256) void reduce_kernel(const char* __restrict__ slab, u16* __restrict__ out) {
  const int gw = blockIdx.x * 4 + (threadIdx.x >> 6), lane = threadIdx.x & 63;
  const int g = gw % 10, rg = (gw / 10) % 112, br = gw / 1120;
  const int qs = lane & 7, c8 = lane >> 3;
  const int R = rg * 8 + qs, T = R >> 5;
  const int nt = 2 * g + (c8 >> 2), s = (c8 >> 1) & 1, hh = c8 & 1;
  const char* sp = slab + ((size_t)((br * 28 + T) * 20 + nt) * 4096) + (2 * s) * 1024 + (32 * hh + (R & 31)) * 16;
  f32x4 a = {0, 0, 0, 0}, c = {0, 0, 0, 0};
#pragma unroll
  for (int k = 0; k < 8; ++k) { a += *(const f32x4*)(sp + k * SLAB_ONE); c += *(const f32x4*)(sp + k * SLAB_ONE + 1024); }
  u32x4 w = {cvtpk(a[0], a[1]), cvtpk(a[2], a[3]), cvtpk(c[0], c[1]), cvtpk(c[2], c[3])};
  *(u32x4*)(out + (size_t)R * (2 * DM) + br * DM + 64 * g + 8 * c8) = w;
}

extern "C" void kernel_launch(void* const* d_in, const int* in_sizes, int n_in, void* d_out, int out_size, void* d_ws, size_t ws_size, hipStream_t stream) {
  (void)in_sizes; (void)n_in; (void)out_size; (void)ws_size;
  hipFuncSetAttribute((const void*)attn_kernel, hipFuncAttributeMaxDynamicSharedMemorySize, LDS1);
  hipFuncSetAttribute((const void*)oproj_kernel, hipFuncAttributeMaxDynamicSharedMemorySize, LDS2);
  char* ows = (char*)d_ws;
  char* slab = ows + OWS_BYTES;
  attn_kernel<<<512, 256, LDS1, stream>>>((const u16*)d_in[0], (const u16*)d_in[1], (const u16*)d_in[2], (const u16*)d_in[3],
                                          (const u16*)d_in[5], (const u16*)d_in[6], (const u16*)d_in[7], (const u16*)d_in[8], ows);
  oproj_kernel<<<448, 256, LDS2, stream>>>((const u16*)d_in[4], (const u16*)d_in[9], ows, slab);
  reduce_kernel<<<560, 256, 0, stream>>>(slab, (u16*)d_out);
}
```

```cpp
#include <hip/hip_runtime.h>
#include <hip/hip_bf16.h>
#include <stdint.h>

typedef unsigned short u16;
typedef short bf16x8 __attribute__((ext_vector_type(8)));
typedef short s16x4 __attribute__((ext_vector_type(4)));
typedef float f32x4 __attribute__((ext_vector_type(4)));
typedef float f32x2 __attribute__((ext_vector_type(2)));
typedef __bf16 bf16x2_t __attribute__((ext_vector_type(2)));
typedef unsigned u32x4 __attribute__((ext_vector_type(4)));
typedef unsigned u32x2 __attribute__((ext_vector_type(2)));
typedef __amdgpu_buffer_rsrc_t rsrc_t;
#define LAS __attribute__((address_space(3)))
#define DI __device__ __forceinline__
#define MFMA(a, b, c) __builtin_amdgcn_mfma_f32_16x16x32_bf16(a, b, c, 0, 0, 0)

constexpr int SQ = 112, DM = 640, NH = 32, SK = 512, DH = 192, HD = NH * DH;
constexpr int STG = 24576;
constexpr int XOFF = 49152, XSLOT = 14336;
constexpr int LDS_BYTES = XOFF + 2 * XSLOT;
constexpr unsigned FLAG_BYTES = 16384;
constexpr unsigned OBLK = 7 * 6 * 1024;
constexpr unsigned PBLK = 7 * 5 * 1024;
constexpr size_t WS_O = FLAG_BYTES;
constexpr size_t WS_P = WS_O + (size_t)512 * OBLK;
constexpr size_t WS_END = WS_P + (size_t)512 * PBLK;
constexpr float QSCALE = 0.07216878364870322f * 1.4426950408889634f;
constexpr float RESC_THR = 8.0f;

DI unsigned rfl(unsigned x) { return __builtin_amdgcn_readfirstlane(x); }
DI unsigned cvtpk(float lo, float hi) { f32x2 v = {lo, hi}; bf16x2_t b = __builtin_convertvector(v, bf16x2_t); return __builtin_bit_cast(unsigned, b); }
DI s16x4 trd(unsigned a) { return __builtin_bit_cast(s16x4, __builtin_amdgcn_ds_read_tr16_b64_v4i16((LAS s16x4*)a)); }
DI bf16x8 trd2(unsigned a, unsigned d) { s16x4 lo = trd(a), hi = trd(a + d); return (bf16x8){lo[0], lo[1], lo[2], lo[3], hi[0], hi[1], hi[2], hi[3]}; }
DI bf16x8 ldsr128(unsigned a) { return *(const LAS bf16x8*)a; }
DI bf16x8 pack2(const f32x4& a, const f32x4& b) {
  u32x4 w = {cvtpk(a[0], a[1]), cvtpk(a[2], a[3]), cvtpk(b[0], b[1]), cvtpk(b[2], b[3])};
  return __builtin_bit_cast(bf16x8, w);
}
DI float bflo(unsigned w) { return __uint_as_float(w << 16); }
DI float bfhi(unsigned w) { return __uint_as_float(w & 0xFFFF0000u); }
DI float xg_max(float v) { v = fmaxf(v, __shfl_xor(v, 16)); return fmaxf(v, __shfl_xor(v, 32)); }
DI float xg_sum(float v) { v += __shfl_xor(v, 16); return v + __shfl_xor(v, 32); }

DI void dma1(unsigned m0v, unsigned v0, rsrc_t srd, unsigned soff) {
  asm volatile("s_mov_b32 m0, %0\n\ts_nop 0\n\tbuffer_load_dwordx4 %1, %2, %3 offen lds"
               :: "s"(m0v), "v"(v0), "s"(srd), "s"(soff) : "m0", "memory");
}
DI void dma2(unsigned m0v, unsigned v0, unsigned v1, rsrc_t srd, unsigned soff) {
  asm volatile("s_mov_b32 m0, %0\n\ts_nop 0\n\t"
               "buffer_load_dwordx4 %1, %3, %4 offen lds\n\t"
               "buffer_load_dwordx4 %2, %3, %4 offen offset:1024 lds"
               :: "s"(m0v), "v"(v0), "v"(v1), "s"(srd), "s"(soff) : "m0", "memory");
}
DI void dma3(unsigned m0v, unsigned v0, unsigned v1, unsigned v2, rsrc_t srd, unsigned soff) {
  asm volatile("s_mov_b32 m0, %0\n\ts_nop 0\n\t"
               "buffer_load_dwordx4 %1, %4, %5 offen lds\n\t"
               "buffer_load_dwordx4 %2, %4, %5 offen offset:1024 lds\n\t"
               "buffer_load_dwordx4 %3, %4, %5 offen offset:2048 lds"
               :: "s"(m0v), "v"(v0), "v"(v1), "v"(v2), "s"(srd), "s"(soff) : "m0", "memory");
}
template <int OFF> DI void bload(u32x4& d, unsigned v, rsrc_t srd, unsigned soff) {
  asm volatile("buffer_load_dwordx4 %0, %1, %2, %3 offen offset:%4" : "=v"(d) : "v"(v), "s"(srd), "s"(soff), "i"(OFF) : "memory");
}
template <int OFF> DI void bload_sc1(bf16x8& d, unsigned v, rsrc_t srd, unsigned soff) {
  asm volatile("buffer_load_dwordx4 %0, %1, %2, %3 offen offset:%4 sc1" : "=v"(d) : "v"(v), "s"(srd), "s"(soff), "i"(OFF) : "memory");
}
template <int OFF> DI void bstore_sc1(const bf16x8& d, unsigned v, rsrc_t srd, unsigned soff) {
  asm volatile("buffer_store_dwordx4 %0, %1, %2, %3 offen offset:%4 sc1\n\ts_nop 1" :: "v"(d), "v"(v), "s"(srd), "s"(soff), "i"(OFF) : "memory");
}
template <int N> DI void waitbar() { asm volatile("s_waitcnt vmcnt(%0) lgkmcnt(0)\n\ts_barrier" :: "i"(N) : "memory"); }
template <int N> DI void waitvm() { asm volatile("s_waitcnt vmcnt(%0)" :: "i"(N) : "memory"); }
template <typename T> DI void pin(T& x) { asm volatile("" : "+v"(x)); }

constexpr int STAMP_LDS = 0;
#define STAMP(i)
#define CSTAMP(i)

constexpr int g1_step_ops(int t, int NQ) { return (t + 3 < 20 ? 3 : 0) + (((t & 1) && (t + 3) / 2 < 10) ? 2 * NQ : 0) + (t < 15 ? 1 : 0); }
constexpr int g1_total_before(int t, int NQ) { int n = 9 + 4 * NQ; for (int i = 0; i < t; ++i) n += g1_step_ops(i, NQ); return n; }
constexpr int g1_pos_w(int c, int NQ) { return c == 0 ? 2 * NQ + 3 : c == 1 ? 2 * NQ + 6 : c == 2 ? 4 * NQ + 9 : g1_total_before(c - 3, NQ) + 3; }
constexpr int g1_pos_x(int j, int NQ) { return j == 0 ? 2 * NQ : j == 1 ? 4 * NQ + 6 : g1_total_before(2 * j - 3, NQ) + 3 + 2 * NQ; }
constexpr int g1_wait(int t, int NQ) {
  int need = g1_pos_w(t, NQ);
  if (!(t & 1) && g1_pos_x(t / 2, NQ) > need) need = g1_pos_x(t / 2, NQ);
  return g1_total_before(t, NQ) - need;
}

struct Ctx {
  rsrc_t srdX, srdW, srdK, srdV, srdWo, srdWs;
  unsigned lds0;
  int lane, wid, br, b, h;
};

DI bool poll_flags(rsrc_t srdWs, unsigned off, int lane, int nflag) {
  const unsigned vo = off + (unsigned)(lane < nflag ? lane : 0) * 4u;
  for (unsigned spins = 0; spins < (1u << 22); ++spins) {
    unsigned f;
    asm volatile("buffer_load_dword %0, %1, %2, 0 offen sc1\n\ts_waitcnt vmcnt(0)" : "=v"(f) : "v"(vo), "s"(srdWs) : "memory");
    if (__all(f == 1u)) return true;
    __builtin_amdgcn_s_sleep(2);
  }
  return false;
}
DI void set_flag(rsrc_t srdWs, unsigned off, int lane) {
  if (lane == 0) { const unsigned one = 1u; asm volatile("buffer_store_dword %0, %1, %2, 0 offen sc1\n\ts_nop 1" :: "v"(one), "v"(off), "s"(srdWs) : "memory"); }
}

template <int NQ, int T> struct G1Steps {
  template <typename F> static DI void run(F& f) { G1Steps<NQ, T - 1>::run(f); f.template step<T - 1>(); }
};
template <int NQ> struct G1Steps<NQ, 0> { template <typename F> static DI void run(F&) {} };

template <int NQ> struct G1 {
  const Ctx& c;
  unsigned vw[3], vx[2], vkv[3];
  unsigned aW[2][2], aX[2];
  unsigned mW, mX, soW, soX, soKV;
  f32x4 acc[12][NQ];
  bf16x8 xf[NQ][2];
  u32x4 kv[15];
  DI G1(const Ctx& c_) : c(c_) {}
  template <int C> DI void issueW() { dma3(mW + (C & 3) * 12288, vw[0], vw[1], vw[2], c.srdW, soW + C * (32 * HD * 2)); }
  template <int J> DI void issueX() {
#pragma unroll
    for (int i = 0; i < NQ; ++i) dma2(mX + (J & 1) * XSLOT + i * 2048, vx[0], vx[1], c.srdX, soX + i * (16 * DM * 2) + J * 128);
  }
  template <int I> DI void issueKV() {
    constexpr int p = I / 3, k = I % 3;
    constexpr int isv = (p == 2 || p == 4), idx = p == 0 ? 0 : p == 1 ? 1 : p == 2 ? 0 : p == 3 ? 2 : 1;
    bload<1024 * k>(kv[I], vkv[k], isv ? c.srdV : c.srdK, soKV + idx * 12288);
  }
  template <int T> DI void step() {
    waitbar<g1_wait(T, NQ)>();
    if constexpr (!(T & 1)) {
#pragma unroll
      for (int i = 0; i < NQ; ++i) { xf[i][0] = ldsr128(aX[0] + ((T >> 1) & 1) * XSLOT + i * 2048); xf[i][1] = ldsr128(aX[1] + ((T >> 1) & 1) * XSLOT + i * 2048); }
    }
    bf16x8 a0[4];
#pragma unroll
    for (int dt = 0; dt < 4; ++dt) a0[dt] = trd2(aW[(dt >> 1) & 1][dt & 1] + (T & 3) * 12288, 1536);
    if constexpr (T + 3 < 20) issueW<T + 3>();
    if constexpr ((T & 1) && (T + 3) / 2 < 10) issueX<(T + 3) / 2>();
    if constexpr (T < 15) issueKV<T>();
#pragma unroll
    for (int dt = 0; dt < 12; ++dt) {
      const bf16x8 a = dt < 4 ? a0[dt] : trd2(aW[(dt >> 1) & 1][dt & 1] + (T & 3) * 12288 + (dt >> 2) * 128, 1536);
#pragma unroll
      for (int i = 0; i < NQ; ++i) acc[dt][i] = MFMA(a, xf[i][T & 1], acc[dt][i]);
    }
  }
};

template <int NQ>
DI void body(const Ctx& c, u16* __restrict__ out, char* ws) {
  const int lane = c.lane, wid = c.wid;
  const int l15 = lane & 15, g = lane >> 4, tq = l15 >> 2, tp = lane & 3;
  const unsigned lds0 = c.lds0;
  STAMP(0); CSTAMP(32);
  unsigned vkv[3];
#pragma unroll
  for (int k = 0; k < 3; ++k) { const int row = (64 * k + lane) / 24; vkv[k] = (unsigned)(((lane & ~7) | ((lane & 7) ^ (((row >> 1) & 3) << 1))) << 4); }
  const unsigned wpiece = (unsigned)wid * 3072u;

  bf16x8 qf[NQ][6];
  {
    G1<NQ> G(c);
#pragma unroll
    for (int k = 0; k < 3; ++k) {
      const int id = 64 * k + lane, row = id / 24, pos = id % 24;
      const int f = ((row >> 1) & 1) | ((wid & 1) << 2);
      G.vw[k] = (unsigned)(row * (HD * 2) + (pos >> 3) * 128 + (((pos & 7) ^ f) << 4) - 1024 * k);
      G.vkv[k] = vkv[k];
    }
    G.vx[0] = (unsigned)((lane >> 3) * (DM * 2) + (((lane & 7) ^ (((lane >> 4) & 3) << 1)) << 4));
    G.vx[1] = G.vx[0] + 8 * DM * 2 - 1024;
    G.mW = lds0 + wpiece; G.soW = (unsigned)wid * (8 * HD * 2);
    G.mX = lds0 + XOFF + wid * 4096; G.soX = (unsigned)wid * (32 * DM * 2);
    G.soKV = wpiece;
#pragma unroll
    for (int s1 = 0; s1 < 2; ++s1)
#pragma unroll
      for (int T = 0; T < 2; ++T)
        G.aW[s1][T] = lds0 + (8 * g + tq) * 384 + (((4 * s1 + tp) ^ ((tq >> 1) | ((g & 1) << 2))) << 4) + 8 * (T ^ (tp & 1));
    G.aX[0] = lds0 + XOFF + wid * 4096 + l15 * 128 + ((g ^ (((l15 >> 1) & 3) << 1)) << 4);
    G.aX[1] = G.aX[0] ^ 64;
#pragma unroll
    for (int dt = 0; dt < 12; ++dt)
#pragma unroll
      for (int i = 0; i < NQ; ++i) G.acc[dt][i] = f32x4{};
    G.template issueX<0>(); G.template issueW<0>(); G.template issueW<1>(); G.template issueX<1>(); G.template issueW<2>();
    G1Steps<NQ, 20>::run(G);
    const bool odd = g & 1;
#pragma unroll
    for (int i = 0; i < NQ; ++i)
#pragma unroll
      for (int s = 0; s < 6; ++s) {
        const f32x4 t0 = G.acc[2 * s][i] * QSCALE, t1 = G.acc[2 * s + 1][i] * QSCALE;
        const unsigned u0 = cvtpk(t0[0], t0[1]), u1 = cvtpk(t0[2], t0[3]), u2 = cvtpk(t1[0], t1[1]), u3 = cvtpk(t1[2], t1[3]);
        u32x4 w = {odd ? u2 : u0, odd ? u3 : u1, odd ? u0 : u2, odd ? u1 : u3};
        qf[i][s] = __builtin_bit_cast(bf16x8, w);
      }
    asm volatile("s_waitcnt vmcnt(0) lgkmcnt(0)\n\ts_barrier"
                 : "+v"(G.kv[0]), "+v"(G.kv[1]), "+v"(G.kv[2]), "+v"(G.kv[3]), "+v"(G.kv[4]), "+v"(G.kv[5]), "+v"(G.kv[6]), "+v"(G.kv[7]),
                   "+v"(G.kv[8]), "+v"(G.kv[9]), "+v"(G.kv[10]), "+v"(G.kv[11]), "+v"(G.kv[12]), "+v"(G.kv[13]), "+v"(G.kv[14]) :: "memory");
    const unsigned wa = lds0 + wpiece + lane * 16;
#pragma unroll
    for (int I = 0; I < 15; ++I) {
      const int p = I / 3, k = I % 3;
      const unsigned dst = p == 0 ? 0 : (p + 1) * 12288;
      *(LAS u32x4*)(wa + dst + k * 1024) = G.kv[I];
    }
  }
  STAMP(1);

  unsigned aK[2], aV[4];
  aK[0] = lds0 + l15 * 384 + ((g ^ (((l15 >> 1) & 3) << 1)) << 4);
  aK[1] = aK[0] ^ 64;
#pragma unroll
  for (int d4 = 0; d4 < 4; ++d4) aV[d4] = lds0 + 12288 + (4 * g + tq) * 384 + (((2 * d4 + (tp >> 1)) ^ (((2 * g + (tq >> 1)) & 3) << 1)) << 4) + 8 * (tp & 1);
  unsigned vo[5];
#pragma unroll
  for (int k = 0; k < 5; ++k) { const int id = 64 * k + lane, row = id / 20, pos = id % 20; vo[k] = (unsigned)(row * (DM * 2) + ((pos ^ ((row >> 2) & 1)) << 4) - 1024 * (k < 3 ? k : k - 3)); }
  const int hq = c.h & 3;
  auto issueWo = [&](int j, unsigned reg) {
    const unsigned m = lds0 + reg + wid * 5120;
    const unsigned so = (unsigned)((((hq + j / 3) & 3) * DH + (j % 3) * 64 + 16 * wid) * (DM * 2));
    dma3(m, vo[0], vo[1], vo[2], c.srdWo, so);
    dma2(m + 3072, vo[3], vo[4], c.srdWo, so);
  };
  f32x4 o[12][NQ];
#pragma unroll
  for (int dt = 0; dt < 12; ++dt)
#pragma unroll
    for (int i = 0; i < NQ; ++i) o[dt][i] = f32x4{};
  f32x4 negm[NQ];
  float lsum[NQ];
  bf16x8 pf[NQ];
  auto qk = [&](f32x4 (&s)[2][NQ], unsigned reg) {
#pragma unroll
    for (int ks = 0; ks < 6; ++ks)
#pragma unroll
      for (int kt = 0; kt < 2; ++kt) {
        const bf16x8 a = ldsr128(aK[ks & 1] + reg + (ks >> 1) * 128 + kt * 6144);
#pragma unroll
        for (int i = 0; i < NQ; ++i) s[kt][i] = MFMA(a, qf[i][ks], s[kt][i]);
      }
  };
  auto pv = [&](unsigned reg) {
#pragma unroll
    for (int dt = 0; dt < 12; ++dt) {
      const bf16x8 a = trd2(aV[dt & 3] + reg + (dt >> 2) * 128, 6144);
#pragma unroll
      for (int i = 0; i < NQ; ++i) o[dt][i] = MFMA(a, pf[i], o[dt][i]);
    }
  };
  auto lanemax = [&](const f32x4 (&s)[2][NQ], int i) {
    return fmaxf(fmaxf(fmaxf(s[0][i][0], s[0][i][1]), fmaxf(s[0][i][2], s[0][i][3])), fmaxf(fmaxf(s[1][i][0], s[1][i][1]), fmaxf(s[1][i][2], s[1][i][3])));
  };
  {
    waitbar<0>();
    f32x4 s[2][NQ];
#pragma unroll
    for (int i = 0; i < NQ; ++i) { s[0][i] = f32x4{}; s[1][i] = f32x4{}; }
    qk(s, 0);
#pragma unroll
    for (int i = 0; i < NQ; ++i) {
      const float m = xg_max(lanemax(s, i));
      negm[i] = (f32x4){-m, -m, -m, -m};
      float ps = 0.f;
#pragma unroll
      for (int kt = 0; kt < 2; ++kt)
#pragma unroll
        for (int r = 0; r < 4; ++r) { s[kt][i][r] = __builtin_amdgcn_exp2f(s[kt][i][r] - m); ps += s[kt][i][r]; }
      lsum[i] = ps;
      pf[i] = pack2(s[0][i], s[1][i]);
    }
  }
  unsigned rc = STG, rn = 2 * STG, rf = 0;
  for (int st = 1; st < 16; ++st) {
    waitbar<6>();
    if (st < 15) {
      dma3(lds0 + rf + wpiece, vkv[0], vkv[1], vkv[2], c.srdK, (unsigned)(st + 2) * 12288u + wpiece);
      dma3(lds0 + rf + 12288 + wpiece, vkv[0], vkv[1], vkv[2], c.srdV, (unsigned)(st + 1) * 12288u + wpiece);
    } else {
      issueWo(0, rf);
    }
    f32x4 s[2][NQ];
#pragma unroll
    for (int i = 0; i < NQ; ++i) { s[0][i] = negm[i]; s[1][i] = negm[i]; }
    qk(s, rc);
    float mx = lanemax(s, 0);
    if (NQ == 2) mx = fmaxf(mx, lanemax(s, NQ - 1));
    const bool resc = __any(mx > RESC_THR);
    float sc[NQ];
    if (resc) {
#pragma unroll
      for (int i = 0; i < NQ; ++i) {
        const float d = fmaxf(xg_max(lanemax(s, i)), 0.f);
        s[0][i] -= d; s[1][i] -= d; negm[i] -= d;
        sc[i] = __builtin_amdgcn_exp2f(-d);
        lsum[i] *= sc[i];
      }
    }
    pv(rc);
#pragma unroll
    for (int i = 0; i < NQ; ++i) {
      float ps = lsum[i];
#pragma unroll
      for (int kt = 0; kt < 2; ++kt)
#pragma unroll
        for (int r = 0; r < 4; ++r) { s[kt][i][r] = __builtin_amdgcn_exp2f(s[kt][i][r]); ps += s[kt][i][r]; }
      lsum[i] = ps;
      pf[i] = pack2(s[0][i], s[1][i]);
    }
    if (resc) {
#pragma unroll
      for (int dt = 0; dt < 12; ++dt)
#pragma unroll
        for (int i = 0; i < NQ; ++i) o[dt][i] *= sc[i];
    }
    const unsigned t_ = rc; rc = rn; rn = rf; rf = t_;
    STAMP(1 + st);
  }
  waitbar<5>();
  issueWo(1, 0);
  pv(STG);
  STAMP(17);

  const int item = (c.br * 8 + c.b) * 32 + c.h;
  const unsigned vl = (unsigned)lane * 16u;
  bf16x8 oa[NQ][6], ob[NQ][6];
#pragma unroll
  for (int i = 0; i < NQ; ++i) {
    const float inv = 1.f / xg_sum(lsum[i]);
#pragma unroll
    for (int s = 0; s < 6; ++s) oa[i][s] = pack2(o[2 * s][i] * inv, o[2 * s + 1][i] * inv);
  }
  const unsigned owave = (unsigned)WS_O + (unsigned)wid * 12288u;
  {
    const unsigned so = owave + (unsigned)item * OBLK;
#pragma unroll
    for (int i = 0; i < NQ; ++i) {
      bstore_sc1<0>(oa[i][0], vl, c.srdWs, so + i * 6144); bstore_sc1<1024>(oa[i][1], vl, c.srdWs, so + i * 6144); bstore_sc1<2048>(oa[i][2], vl, c.srdWs, so + i * 6144);
      bstore_sc1<0>(oa[i][3], vl, c.srdWs, so + i * 6144 + 3072); bstore_sc1<1024>(oa[i][4], vl, c.srdWs, so + i * 6144 + 3072); bstore_sc1<2048>(oa[i][5], vl, c.srdWs, so + i * 6144 + 3072);
    }
  }
  auto loadO = [&](bf16x8 (&d)[NQ][6], int rel) {
    const unsigned so = owave + (unsigned)((item & ~3) | ((hq + rel) & 3)) * OBLK;
#pragma unroll
    for (int i = 0; i < NQ; ++i) {
      bload_sc1<0>(d[i][0], vl, c.srdWs, so + i * 6144); bload_sc1<1024>(d[i][1], vl, c.srdWs, so + i * 6144); bload_sc1<2048>(d[i][2], vl, c.srdWs, so + i * 6144);
      bload_sc1<0>(d[i][3], vl, c.srdWs, so + i * 6144 + 3072); bload_sc1<1024>(d[i][4], vl, c.srdWs, so + i * 6144 + 3072); bload_sc1<2048>(d[i][5], vl, c.srdWs, so + i * 6144 + 3072);
    }
  };
  auto pinO = [&](bf16x8 (&d)[NQ][6]) {
#pragma unroll
    for (int i = 0; i < NQ; ++i)
#pragma unroll
      for (int s = 0; s < 6; ++s) pin(d[i][s]);
  };

  unsigned aO[2];
  aO[0] = lds0 + (4 * g + tq) * 320 + ((tp ^ (g & 1)) << 4) + 8 * (tp & 1);
  aO[1] = aO[0] ^ 8;
  f32x4 y[5][2][NQ];
#pragma unroll
  for (int P = 0; P < 5; ++P)
#pragma unroll
    for (int T = 0; T < 2; ++T)
#pragma unroll
      for (int i = 0; i < NQ; ++i) y[P][T][i] = f32x4{};
  auto g4 = [&](const bf16x8 (&of)[NQ][6], int j) {
    const unsigned reg = ((j + 2) % 3) * STG;
#pragma unroll
    for (int ks = 0; ks < 2; ++ks)
#pragma unroll
      for (int P = 0; P < 5; ++P)
#pragma unroll
        for (int T = 0; T < 2; ++T) {
          const bf16x8 a = trd2(aO[T] + reg + ks * 10240 + P * 64, 5120);
#pragma unroll
          for (int i = 0; i < NQ; ++i) y[P][T][i] = MFMA(a, of[i][2 * (j % 3) + ks], y[P][T][i]);
        }
  };
  const unsigned flagO = (unsigned)(item >> 2) * 16u;
  constexpr int NO = 6 * NQ;
  waitbar<5 + NO>(); issueWo(2, 1 * STG); g4(oa, 0);
  waitbar<5>();
  if (wid == 0) set_flag(c.srdWs, flagO + hq * 4, lane);
  issueWo(3, 2 * STG); g4(oa, 1);
  STAMP(18);
  poll_flags(c.srdWs, flagO, lane, 4);
  STAMP(19);
  loadO(ob, 1);
  waitbar<NO>(); issueWo(4, 0 * STG); g4(oa, 2);
  loadO(oa, 2);
  waitbar<5 + NO>(); pinO(ob); issueWo(5, 1 * STG); g4(ob, 3);
  waitbar<NO + 5>(); issueWo(6, 2 * STG); g4(ob, 4);
  waitbar<5>(); issueWo(7, 0 * STG); g4(ob, 5);
  loadO(ob, 3);
  waitbar<5 + NO>(); pinO(oa); issueWo(8, 1 * STG); g4(oa, 6);
  waitbar<NO + 5>(); issueWo(9, 2 * STG); g4(oa, 7);
  waitbar<5>(); issueWo(10, 0 * STG); g4(oa, 8);
  waitbar<5>(); pinO(ob); issueWo(11, 1 * STG); g4(ob, 9);
  waitbar<5>(); g4(ob, 10);
  waitbar<0>(); g4(ob, 11);
  STAMP(20);

  const int grp = (c.br * 8 + c.b) * 4 + hq, ks8 = c.h >> 2;
  {
    const unsigned so = (unsigned)WS_P + (unsigned)(grp * 8 + ks8) * PBLK + (unsigned)wid * 10240u;
#pragma unroll
    for (int i = 0; i < NQ; ++i) {
      bstore_sc1<0>(pack2(y[0][0][i], y[0][1][i]), vl, c.srdWs, so + i * 5120); bstore_sc1<1024>(pack2(y[1][0][i], y[1][1][i]), vl, c.srdWs, so + i * 5120);
      bstore_sc1<2048>(pack2(y[2][0][i], y[2][1][i]), vl, c.srdWs, so + i * 5120); bstore_sc1<3072>(pack2(y[3][0][i], y[3][1][i]), vl, c.srdWs, so + i * 5120);
      bstore_sc1<0>(pack2(y[4][0][i], y[4][1][i]), vl, c.srdWs, so + i * 5120 + 4096);
    }
  }
  waitbar<0>();
  const unsigned flagP = 4096u + (unsigned)grp * 32u;
  if (wid == 0) set_flag(c.srdWs, flagP + ks8 * 4, lane);
  STAMP(21);
  if (ks8 < 7) {
    poll_flags(c.srdWs, flagP, lane, 8);
    STAMP(22);
    constexpr int NP = NQ == 2 ? 1 : 2;
    bf16x8 pr[NP][8];
    const unsigned so = (unsigned)WS_P + (unsigned)(grp * 8) * PBLK + (unsigned)ks8 * 5120u + (unsigned)wid * 1024u;
#pragma unroll
    for (int k = 0; k < 8; ++k) {
      bload_sc1<0>(pr[0][k], vl, c.srdWs, so + k * PBLK);
      if (NP == 2) bload_sc1<1024>(pr[NP - 1][k], vl, c.srdWs, so + k * PBLK);
    }
    waitvm<0>();
#pragma unroll
    for (int p = 0; p < NP; ++p) {
      float a[8] = {0.f, 0.f, 0.f, 0.f, 0.f, 0.f, 0.f, 0.f};
#pragma unroll
      for (int k = 0; k < 8; ++k) {
        pin(pr[p][k]);
        const u32x4 w = __builtin_bit_cast(u32x4, pr[p][k]);
#pragma unroll
        for (int e = 0; e < 4; ++e) { a[2 * e] += bflo(w[e]); a[2 * e + 1] += bfhi(w[e]); }
      }
      const unsigned u0 = cvtpk(a[0], a[1]), u1 = cvtpk(a[2], a[3]), u2 = cvtpk(a[4], a[5]), u3 = cvtpk(a[6], a[7]);
      const bool odd = g & 1;
      const u32x4 w = {odd ? u2 : u0, odd ? u3 : u1, odd ? u0 : u2, odd ? u1 : u3};
      *(u32x4*)(out + (size_t)(c.b * SQ + ks8 * 16 + l15) * (2 * DM) + c.br * DM + hq * 160 + (wid + p) * 32 + 8 * g) = w;
    }
  }
}

__global__ __launch_bounds__(256, 2) void fused_kernel(const u16* __restrict__ xA, const u16* __restrict__ kA, const u16* __restrict__ vA, const u16* __restrict__ wqA, const u16* __restrict__ woA,
                                                       const u16* __restrict__ xB, const u16* __restrict__ kB, const u16* __restrict__ vB, const u16* __restrict__ wqB, const u16* __restrict__ woB,
                                                       u16* __restrict__ out, char* ws) {
  extern __shared__ __attribute__((aligned(16))) char lds[];
  Ctx c;
  c.lane = threadIdx.x & 63;
  c.wid = rfl(threadIdx.x >> 6);
  const int xcd = blockIdx.x & 7, idx = blockIdx.x >> 3;
  c.h = 4 * xcd + (idx & 3); c.b = (idx >> 2) & 7; c.br = idx >> 5;
  const u16* x = c.br ? xB : xA; const u16* kc = c.br ? kB : kA; const u16* vc = c.br ? vB : vA; const u16* wq = c.br ? wqB : wqA; const u16* wo = c.br ? woB : woA;
  const size_t kvoff = (size_t)(c.b * NH + c.h) * SK * DH;
  c.srdX = __builtin_amdgcn_make_buffer_rsrc((void*)(x + (size_t)c.b * SQ * DM), (short)0, SQ * DM * 2, 0x00020000);
  c.srdW = __builtin_amdgcn_make_buffer_rsrc((void*)(wq + c.h * DH), (short)0, (DM * HD - c.h * DH) * 2, 0x00020000);
  c.srdK = __builtin_amdgcn_make_buffer_rsrc((void*)(kc + kvoff), (short)0, SK * DH * 2, 0x00020000);
  c.srdV = __builtin_amdgcn_make_buffer_rsrc((void*)(vc + kvoff), (short)0, SK * DH * 2, 0x00020000);
  c.srdWo = __builtin_amdgcn_make_buffer_rsrc((void*)(wo + (size_t)(c.h >> 2) * 4 * DH * DM + (c.h & 3) * 160), (short)0, (4 * DH * DM - (c.h & 3) * 160) * 2, 0x00020000);
  c.srdWs = __builtin_amdgcn_make_buffer_rsrc((void*)ws, (short)0, (int)WS_END, 0x00020000);
  c.lds0 = (unsigned)(uintptr_t)lds;
  if (c.wid < 3) body<2>(c, out, ws); else body<1>(c, out, ws);
}

extern "C" void kernel_launch(void* const* d_in, const int* in_sizes, int n_in, void* d_out, int out_size, void* d_ws, size_t ws_size, hipStream_t stream) {
  (void)in_sizes; (void)n_in; (void)out_size; (void)ws_size;
  (void)hipFuncSetAttribute((const void*)fused_kernel, hipFuncAttributeMaxDynamicSharedMemorySize, LDS_BYTES + STAMP_LDS);
  (void)hipMemsetAsync(d_ws, 0, FLAG_BYTES, stream);
  fused_kernel<<<512, 256, LDS_BYTES + STAMP_LDS, stream>>>((const u16*)d_in[0], (const u16*)d_in[1], (const u16*)d_in[2], (const u16*)d_in[3], (const u16*)d_in[4],
                                                 (const u16*)d_in[5], (const u16*)d_in[6], (const u16*)d_in[7], (const u16*)d_in[8], (const u16*)d_in[9],
                                                 (u16*)d_out, (char*)d_ws);
}
```

```cpp
#include <hip/hip_runtime.h>
#include <hip/hip_bf16.h>
#include <stdint.h>

typedef unsigned short u16;
typedef short bf16x8 __attribute__((ext_vector_type(8)));
typedef short s16x4 __attribute__((ext_vector_type(4)));
typedef float f32x4 __attribute__((ext_vector_type(4)));
typedef float f32x2 __attribute__((ext_vector_type(2)));
typedef __bf16 bf16x2_t __attribute__((ext_vector_type(2)));
typedef unsigned u32x4 __attribute__((ext_vector_type(4)));
typedef unsigned u32x2 __attribute__((ext_vector_type(2)));
typedef __amdgpu_buffer_rsrc_t rsrc_t;
#define LAS __attribute__((address_space(3)))
#define DI __device__ __forceinline__
#define MFMA(a, b, c) __builtin_amdgcn_mfma_f32_16x16x32_bf16(a, b, c, 0, 0, 0)
#define NWG 512

constexpr int SQ = 112, DM = 640, NH = 32, SK = 512, DH = 192, HD = NH * DH;
constexpr int STG = 24576;
constexpr int XOFF = 49152, XSLOT = 15360;
constexpr int WSLOT = 20480;
constexpr int LDS_BYTES = 4 * WSLOT;
constexpr unsigned FLAG_BYTES = 16384;
constexpr unsigned OBLK = 7 * 6 * 1024;
constexpr unsigned PBLK = 7 * 5 * 1024;
constexpr size_t WS_O = FLAG_BYTES;
constexpr size_t WS_P = WS_O + (size_t)512 * OBLK;
constexpr size_t WS_END = WS_P + (size_t)512 * PBLK;
constexpr float QSCALE = 0.07216878364870322f * 1.4426950408889634f;
constexpr int JUNK = XOFF + XSLOT + 14336;
#define KV_T0 4
constexpr float RESC_THR = 8.0f;

DI unsigned rfl(unsigned x) { return __builtin_amdgcn_readfirstlane(x); }
DI unsigned cvtpk(float lo, float hi) { f32x2 v = {lo, hi}; bf16x2_t b = __builtin_convertvector(v, bf16x2_t); return __builtin_bit_cast(unsigned, b); }
DI s16x4 trd(unsigned a) { return __builtin_bit_cast(s16x4, __builtin_amdgcn_ds_read_tr16_b64_v4i16((LAS s16x4*)a)); }
DI bf16x8 trd2(unsigned a, unsigned d) { s16x4 lo = trd(a), hi = trd(a + d); return (bf16x8){lo[0], lo[1], lo[2], lo[3], hi[0], hi[1], hi[2], hi[3]}; }
DI bf16x8 ldsr128(unsigned a) { return *(const LAS bf16x8*)a; }
DI bf16x8 pack2(const f32x4& a, const f32x4& b) {
  u32x4 w = {cvtpk(a[0], a[1]), cvtpk(a[2], a[3]), cvtpk(b[0], b[1]), cvtpk(b[2], b[3])};
  return __builtin_bit_cast(bf16x8, w);
}
DI float bflo(unsigned w) { return __uint_as_float(w << 16); }
DI float bfhi(unsigned w) { return __uint_as_float(w & 0xFFFF0000u); }
DI float xg_max(float v) { v = fmaxf(v, __shfl_xor(v, 16)); return fmaxf(v, __shfl_xor(v, 32)); }
DI float xg_sum(float v) { v += __shfl_xor(v, 16); return v + __shfl_xor(v, 32); }

DI void dma1(unsigned m0v, unsigned v0, rsrc_t srd, unsigned soff) {
  asm volatile("s_mov_b32 m0, %0\n\ts_nop 0\n\tbuffer_load_dwordx4 %1, %2, %3 offen lds"
               :: "s"(m0v), "v"(v0), "s"(srd), "s"(soff) : "m0", "memory");
}
DI void dma2(unsigned m0v, unsigned v0, unsigned v1, rsrc_t srd, unsigned soff) {
  asm volatile("s_mov_b32 m0, %0\n\ts_nop 0\n\t"
               "buffer_load_dwordx4 %1, %3, %4 offen lds\n\t"
               "buffer_load_dwordx4 %2, %3, %4 offen offset:1024 lds"
               :: "s"(m0v), "v"(v0), "v"(v1), "s"(srd), "s"(soff) : "m0", "memory");
}
DI void dma3(unsigned m0v, unsigned v0, unsigned v1, unsigned v2, rsrc_t srd, unsigned soff) {
  asm volatile("s_mov_b32 m0, %0\n\ts_nop 0\n\t"
               "buffer_load_dwordx4 %1, %4, %5 offen lds\n\t"
               "buffer_load_dwordx4 %2, %4, %5 offen offset:1024 lds\n\t"
               "buffer_load_dwordx4 %3, %4, %5 offen offset:2048 lds"
               :: "s"(m0v), "v"(v0), "v"(v1), "v"(v2), "s"(srd), "s"(soff) : "m0", "memory");
}
DI void dma4(unsigned m0v, unsigned v0, unsigned v1, unsigned v2, unsigned v3, rsrc_t srd, unsigned soff) {
  asm volatile("s_mov_b32 m0, %0\n\ts_nop 0\n\t"
               "buffer_load_dwordx4 %1, %5, %6 offen lds\n\t"
               "buffer_load_dwordx4 %2, %5, %6 offen offset:1024 lds\n\t"
               "buffer_load_dwordx4 %3, %5, %6 offen offset:2048 lds\n\t"
               "buffer_load_dwordx4 %4, %5, %6 offen offset:3072 lds"
               :: "s"(m0v), "v"(v0), "v"(v1), "v"(v2), "v"(v3), "s"(srd), "s"(soff) : "m0", "memory");
}
template <int OFF> DI void dmao(unsigned m0v, unsigned v0, rsrc_t srd, unsigned soff) {
  asm volatile("s_mov_b32 m0, %0\n\ts_nop 0\n\tbuffer_load_dwordx4 %1, %2, %3 offen offset:%4 lds"
               :: "s"(m0v), "v"(v0), "s"(srd), "s"(soff), "i"(OFF) : "m0", "memory");
}
#define FENCE() __builtin_amdgcn_sched_barrier(0)
template <int OFF> DI void bload(u32x4& d, unsigned v, rsrc_t srd, unsigned soff) {
  asm volatile("buffer_load_dwordx4 %0, %1, %2, %3 offen offset:%4" : "=v"(d) : "v"(v), "s"(srd), "s"(soff), "i"(OFF) : "memory");
}
template <int OFF> DI void bload_sc1(bf16x8& d, unsigned v, rsrc_t srd, unsigned soff) {
  asm volatile("buffer_load_dwordx4 %0, %1, %2, %3 offen offset:%4 sc1" : "=v"(d) : "v"(v), "s"(srd), "s"(soff), "i"(OFF) : "memory");
}
template <int OFF> DI void bstore_sc1(const bf16x8& d, unsigned v, rsrc_t srd, unsigned soff) {
  asm volatile("buffer_store_dwordx4 %0, %1, %2, %3 offen offset:%4 sc1\n\ts_nop 1" :: "v"(d), "v"(v), "s"(srd), "s"(soff), "i"(OFF) : "memory");
}
template <int N> DI void waitbar() { asm volatile("s_waitcnt vmcnt(%0) lgkmcnt(0)\n\ts_barrier" :: "i"(N) : "memory"); }
DI void bar() { asm volatile("s_waitcnt lgkmcnt(0)\n\ts_barrier" ::: "memory"); }
template <int N> DI void vmbar() { asm volatile("s_waitcnt vmcnt(%0)\n\ts_barrier" :: "i"(N) : "memory"); }
template <int N> DI void waitvm() { asm volatile("s_waitcnt vmcnt(%0)" :: "i"(N) : "memory"); }
template <typename T> DI void pin(T& x) { asm volatile("" : "+v"(x)); }

constexpr int STAMP_LDS = 0;
#define STAMP(i)
#define CSTAMP(i)

constexpr int g1_xops(int t) { return (t & 1) ? ((t + 3) / 2 < 10 ? 3 : 0) : (t / 2 + 1 < 10 ? 2 : 0); }
constexpr int g1_before(int t) { int n = 20; for (int i = 0; i < t; ++i) n += g1_xops(i) + 4; return n; }
constexpr int g1_pos_w(int c) { return c == 0 ? 4 : c == 1 ? 13 : c == 2 ? 20 : g1_before(c - 3) + g1_xops(c - 3) + 4; }
constexpr int g1_pos_x(int j) { return j == 0 ? 9 : g1_before(2 * j - 2) + 2; }
constexpr int g1_wait(int t) {
  int need = g1_pos_w(t);
  if (!(t & 1) && g1_pos_x(t / 2) > need) need = g1_pos_x(t / 2);
  return g1_before(t) - need;
}

struct Ctx {
  rsrc_t srdX, srdW, srdK, srdV, srdWo, srdWs;
  unsigned lds0;
  int lane, wid, br, b, h, sec;
};

DI bool poll_flags(rsrc_t srdWs, unsigned off, int lane, int nflag) {
  const unsigned vo = off + (unsigned)(lane < nflag ? lane : 0) * 4u;
  for (unsigned spins = 0; spins < (1u << 22); ++spins) {
    unsigned f;
    asm volatile("buffer_load_dword %0, %1, %2, 0 offen sc1\n\ts_waitcnt vmcnt(0)" : "=v"(f) : "v"(vo), "s"(srdWs) : "memory");
    if (__all(f == 1u)) return true;
    __builtin_amdgcn_s_sleep(2);
  }
  return false;
}
DI void set_flag(rsrc_t srdWs, unsigned off, int lane) {
  if (lane == 0) { const unsigned one = 1u; asm volatile("buffer_store_dword %0, %1, %2, 0 offen sc1\n\ts_nop 1" :: "v"(one), "v"(off), "s"(srdWs) : "memory"); }
}

DI void issue_piece(unsigned m, const unsigned (&v)[4], rsrc_t srd, unsigned so) {
#pragma unroll
  for (int q = 0; q < 4; ++q) dma3(m + 3072 * q, v[0], v[1], v[2], srd, so + 3072 * q);
}

DI void touch(unsigned m0v, unsigned v, rsrc_t srd, unsigned soff) {
  asm volatile("s_mov_b32 m0, %0\n\ts_nop 0\n\tbuffer_load_dword %1, %2, %3 offen lds" :: "s"(m0v), "v"(v), "s"(srd), "s"(soff) : "m0", "memory");
}

struct G1Addr { unsigned aW[2][2], aX[2]; };

struct G1H {
  const Ctx& c; G1Addr A;
  unsigned vw[4], vx[3], vkv[4];
  unsigned soW, soX;
  f32x4 acc[12][2];
  bf16x8 xf[2][2], a[8];
  DI G1H(const Ctx& c_) : c(c_) {}
  template <int S> DI void issueW() { dma4(c.lds0 + S * 12288 + c.wid * 4096, vw[0], vw[1], vw[2], vw[3], c.srdW, soW); soW += 32 * HD * 2; }
  template <int S, int H> DI void issueX() {
    const unsigned m = c.lds0 + XOFF + S * XSLOT + c.wid * 5120;
    if (H == 0) dma3(m, vx[0], vx[1], vx[2], c.srdX, soX);
    else { dma2(m + 3072, vx[0], vx[1], c.srdX, soX + 24 * DM * 2); soX += 128; }
  }
  template <int T, int K> DI void rd() { a[(12 * T + K) & 7] = trd2(A.aW[(K >> 1) & 1][K & 1] + T * 12288 + (K >> 2) * 128, 1536); }
  template <int T, int K> DI void mf() {
#pragma unroll
    for (int i = 0; i < 2; ++i) acc[K][i] = MFMA(a[(12 * T + K) & 7], xf[i][T & 1], acc[K][i]);
  }
  template <int T, bool LAST, int I> DI void dm() {
    constexpr bool ring = T == 0 || !LAST;
    constexpr int nx = ring ? ((T & 1) ? 3 : 2) : 0;
    if constexpr (I < nx) {
      constexpr int S = (T & 1) ? ((T + 3) / 2) & 1 : (T / 2 + 1) & 1;
      const unsigned m = c.lds0 + XOFF + S * XSLOT + c.wid * 5120;
      if constexpr (T & 1) dmao<1024 * I>(m, vx[I], c.srdX, soX);
      else { dmao<1024 * I>(m + 3072, vx[I], c.srdX, soX + 24 * DM * 2); if (I == 1) soX += 128; }
    } else if constexpr (I < nx + 4) {
      constexpr int k = I - nx;
      if constexpr (ring) { dmao<1024 * k>(c.lds0 + ((T + 3) & 3) * 12288 + c.wid * 4096, vw[k], c.srdW, soW); if (k == 3) soW += 32 * HD * 2; }
      else dmao<1024 * k>(c.lds0 + (T == 1 ? 2 * STG : T == 2 ? 12288 : 2 * STG + 12288) + c.wid * 4096, vkv[k], T == 1 ? c.srdK : c.srdV,
                          (T == 3 ? 12288u : 24576u) + c.wid * 4096);
    }
  }
  template <int T, bool LAST> DI void step() {
    static_assert(g1_wait(T) == g1_wait(T + 12), "steady state");
    constexpr int P = (T + 3) & 3;
    waitbar<g1_wait(LAST ? T + 16 : T)>();
    if constexpr (!(T & 1)) { xf[0][0] = ldsr128(A.aX[0] + (T >> 1) * XSLOT); xf[1][0] = ldsr128(A.aX[0] + (T >> 1) * XSLOT + 2048); }
    rd<T, 0>(); rd<T, 1>(); rd<T, 2>(); FENCE();
    mf<P, 7>(); rd<T, 3>(); dm<T, LAST, 0>(); FENCE();
    mf<P, 8>(); rd<T, 4>(); dm<T, LAST, 1>(); FENCE();
    mf<P, 9>(); rd<T, 5>(); dm<T, LAST, 2>(); FENCE();
    mf<P, 10>(); rd<T, 6>(); dm<T, LAST, 3>(); FENCE();
    mf<P, 11>(); rd<T, 7>(); dm<T, LAST, 4>(); FENCE();
    mf<T, 0>(); rd<T, 8>();
    if constexpr (!(T & 1)) { xf[0][1] = ldsr128(A.aX[1] + (T >> 1) * XSLOT); xf[1][1] = ldsr128(A.aX[1] + (T >> 1) * XSLOT + 2048); }
    dm<T, LAST, 5>(); FENCE();
    mf<T, 1>(); rd<T, 9>(); dm<T, LAST, 6>(); FENCE();
    mf<T, 2>(); rd<T, 10>(); FENCE();
    mf<T, 3>(); rd<T, 11>(); FENCE();
    mf<T, 4>(); mf<T, 5>(); mf<T, 6>(); FENCE();
  }
  template <bool LAST> DI void iter() { step<0, LAST>(); step<1, LAST>(); step<2, LAST>(); step<3, LAST>(); }
};

struct G1L {
  const Ctx& c; G1Addr A;
  unsigned vkv[4], vtw, vtx, wa;
  f32x4 acc[12][1];
  bf16x8 xf[2];
  u32x4 kv[36];
  DI G1L(const Ctx& c_) : c(c_) {}
  template <int J> DI void touchW() { touch(c.lds0 + JUNK, vtw, c.srdW, J * (160 * HD * 2)); }
  template <int J> DI void touchX() { touch(c.lds0 + JUNK, vtx, c.srdX, J * 256); }
  template <int I> DI void issueKV() {
    constexpr int p = I / 12, q = (I % 12) / 3, k = I % 3;
    bload<1024 * k>(kv[I], vkv[k], p == 2 ? c.srdV : c.srdK, (p == 1 ? 12288u : 0u) + q * 3072u);
  }
  template <int P> DI void putKV() {
    asm volatile("s_waitcnt vmcnt(%12)"
                 : "+v"(kv[12 * P]), "+v"(kv[12 * P + 1]), "+v"(kv[12 * P + 2]), "+v"(kv[12 * P + 3]), "+v"(kv[12 * P + 4]), "+v"(kv[12 * P + 5]),
                   "+v"(kv[12 * P + 6]), "+v"(kv[12 * P + 7]), "+v"(kv[12 * P + 8]), "+v"(kv[12 * P + 9]), "+v"(kv[12 * P + 10]), "+v"(kv[12 * P + 11])
                 : "i"(24 - 12 * P) : "memory");
#pragma unroll
    for (int k = 0; k < 12; ++k) *(LAS u32x4*)(wa + (P ? (P + 1) * 12288 : 0) + k * 1024) = kv[12 * P + k];
  }
  template <int T> DI void step() {
    bar();
    if constexpr (!(T & 1)) { xf[0] = ldsr128(A.aX[0] + ((T >> 1) & 1) * XSLOT); xf[1] = ldsr128(A.aX[1] + ((T >> 1) & 1) * XSLOT); }
    bf16x8 a[12];
#pragma unroll
    for (int dt = 0; dt < 12; ++dt) a[dt] = trd2(A.aW[(dt >> 1) & 1][dt & 1] + (T & 3) * 12288 + (dt >> 2) * 128, 1536);
    if constexpr (T == 0) touchX<2>();
    if constexpr (T == 1) touchW<2>();
    if constexpr (T == 2) touchX<3>();
    if constexpr (T == 3) { touchW<3>(); touchX<4>(); }
    if constexpr (T >= KV_T0 && T < KV_T0 + 12) { issueKV<3 * (T - KV_T0)>(); issueKV<3 * (T - KV_T0) + 1>(); issueKV<3 * (T - KV_T0) + 2>(); }
#pragma unroll
    for (int dt = 0; dt < 12; ++dt) acc[dt][0] = MFMA(a[dt], xf[T & 1], acc[dt][0]);
    if constexpr (T == 18) putKV<0>();
    if constexpr (T == 19) putKV<1>();
    if constexpr (T < 19) step<T + 1>();
  }
};

template <int NQ> DI void pack_q(bf16x8 (&qf)[NQ][6], const f32x4 (&acc)[12][NQ], bool odd) {
#pragma unroll
  for (int i = 0; i < NQ; ++i)
#pragma unroll
    for (int s = 0; s < 6; ++s) {
      const f32x4 t0 = acc[2 * s][i] * QSCALE, t1 = acc[2 * s + 1][i] * QSCALE;
      const unsigned u0 = cvtpk(t0[0], t0[1]), u1 = cvtpk(t0[2], t0[3]), u2 = cvtpk(t1[0], t1[1]), u3 = cvtpk(t1[2], t1[3]);
      u32x4 w = {odd ? u2 : u0, odd ? u3 : u1, odd ? u0 : u2, odd ? u1 : u3};
      qf[i][s] = __builtin_bit_cast(bf16x8, w);
    }
}

template <int NQ>
DI void body(const Ctx& c, u16* __restrict__ out, char* ws) {
  const int lane = c.lane, wid = c.wid;
  const int l15 = lane & 15, g = lane >> 4, tq = l15 >> 2, tp = lane & 3;
  const unsigned lds0 = c.lds0;
  STAMP(0); CSTAMP(32);
  constexpr bool LD = NQ == 1;
  unsigned vkv[4];
#pragma unroll
  for (int k = 0; k < 4; ++k) { const int row = ((LD ? 0 : 256 * wid) + 64 * k + lane) / 24; vkv[k] = (unsigned)(((lane & ~7) | ((lane & 7) ^ (((row >> 1) & 3) << 1))) << 4); }
  const int hq = c.h & 3;

  bf16x8 qf[NQ][6];
  {
    G1Addr A;
#pragma unroll
    for (int s1 = 0; s1 < 2; ++s1)
#pragma unroll
      for (int T = 0; T < 2; ++T)
        A.aW[s1][T] = lds0 + (8 * g + tq) * 384 + (((4 * s1 + tp) ^ ((tq >> 1) | ((g & 1) << 2))) << 4) + 8 * (T ^ (tp & 1));
    A.aX[0] = lds0 + XOFF + wid * 4096 + l15 * 128 + ((g ^ (((l15 >> 1) & 3) << 1)) << 4);
    A.aX[1] = A.aX[0] ^ 64;
    if constexpr (LD) {
      G1L G(c); G.A = A;
#pragma unroll
      for (int k = 0; k < 4; ++k) G.vkv[k] = vkv[k];
      G.vtw = (unsigned)((8 * (lane / 3) + c.b) * (HD * 2) + (lane % 3) * 128);
      G.vtx = (unsigned)((4 * (lane >> 1) + hq) * (DM * 2) + (lane & 1) * 128);
      G.wa = lds0 + lane * 16;
#pragma unroll
      for (int dt = 0; dt < 12; ++dt) G.acc[dt][0] = f32x4{};
      G.template touchW<0>(); G.template touchX<1>(); G.template touchW<1>();
      G.template step<0>();
      bar();
      G.template putKV<2>();
      pack_q<1>(qf, G.acc, g & 1);
    } else {
      G1H G(c); G.A = A;
#pragma unroll
      for (int k = 0; k < 4; ++k) {
        const int id = 256 * wid + 64 * k + lane, row = id / 24, pos = id % 24;
        G.vw[k] = (unsigned)(row * (HD * 2) + (pos >> 3) * 128 + (((pos & 7) ^ (((row >> 1) & 1) | (((row >> 3) & 1) << 2))) << 4) - 1024 * k);
        G.vkv[k] = vkv[k];
      }
#pragma unroll
      for (int k = 0; k < 3; ++k) G.vx[k] = (unsigned)((lane >> 3) * (DM * 2) + (((lane & 7) ^ (((lane >> 4) & 3) << 1)) << 4) + k * (8 * DM * 2 - 1024));
      G.soW = 0; G.soX = (unsigned)wid * (40 * DM * 2);
#pragma unroll
      for (int dt = 0; dt < 12; ++dt)
#pragma unroll
        for (int i = 0; i < 2; ++i) G.acc[dt][i] = f32x4{};
      G.template issueW<0>(); G.template issueX<0, 0>(); G.template issueX<0, 1>(); G.template issueW<1>(); G.template issueX<1, 0>(); G.template issueW<2>();
#pragma unroll
      for (int k = 0; k < 8; ++k) G.a[k] = bf16x8{};
      G.xf[0][1] = G.xf[1][1] = bf16x8{};
#pragma unroll 1
      for (int it = 0; it < 4; ++it) { G.template iter<false>(); STAMP(24 + it); }
      G.template iter<true>();
      STAMP(28);
      bar();
      G.template mf<3, 7>(); G.template mf<3, 8>(); G.template mf<3, 9>(); G.template mf<3, 10>(); G.template mf<3, 11>();
      pack_q<2>(qf, G.acc, g & 1);
    }
  }
  if (c.sec) __builtin_amdgcn_s_setprio(1);
  STAMP(1);

  unsigned aK[2], aV[4];
  aK[0] = lds0 + l15 * 384 + ((g ^ (((l15 >> 1) & 3) << 1)) << 4);
  aK[1] = aK[0] ^ 64;
#pragma unroll
  for (int d4 = 0; d4 < 4; ++d4) aV[d4] = lds0 + 12288 + (4 * g + tq) * 384 + (((2 * d4 + (tp >> 1)) ^ (((2 * g + (tq >> 1)) & 3) << 1)) << 4) + 8 * (tp & 1);
  unsigned vo[5];
#pragma unroll
  for (int k = 0; k < (LD ? 5 : 3); ++k) {
    const int id = 64 * (LD ? k : 11 + 3 * wid + k) + lane, row = id / 20, pos = id % 20;
    vo[k] = (unsigned)(row * (DM * 2) + ((pos ^ ((row >> 2) & 1)) << 4) - 1024 * (k < 3 ? k : k - 3));
  }
  constexpr int NW = LD ? 11 : 3;
  auto wo1 = [&](int j, int k) {
    const unsigned so = (unsigned)((((hq + j / 3) & 3) * DH + (j % 3) * 64) * (DM * 2)), m = lds0 + ((j + 3) & 3) * WSLOT;
    if constexpr (LD) {
      const int q = k / 5, r = k % 5;
      const unsigned mm = m + q * 5120 + (r < 3 ? 0 : 3072), ss = so + q * (16 * DM * 2);
      if (r == 0 || r == 3) dmao<0>(mm, vo[r], c.srdWo, ss); else if (r == 1 || r == 4) dmao<1024>(mm, vo[r], c.srdWo, ss); else dmao<2048>(mm, vo[r], c.srdWo, ss);
    } else {
      const unsigned mm = m + 11264 + wid * 3072;
      if (k == 0) dmao<0>(mm, vo[0], c.srdWo, so); else if (k == 1) dmao<1024>(mm, vo[1], c.srdWo, so); else dmao<2048>(mm, vo[2], c.srdWo, so);
    }
  };
  auto issueWo = [&](int j) {
#pragma unroll
    for (int k = 0; k < NW; ++k) wo1(j, k);
  };
  f32x4 o[12][NQ];
#pragma unroll
  for (int dt = 0; dt < 12; ++dt)
#pragma unroll
    for (int i = 0; i < NQ; ++i) o[dt][i] = f32x4{};
  f32x4 negm[NQ];
  float lsum[NQ];
  bf16x8 pf[NQ];
  auto qk = [&](f32x4 (&s)[2][NQ], unsigned reg) {
#pragma unroll
    for (int ks = 0; ks < 6; ++ks)
#pragma unroll
      for (int kt = 0; kt < 2; ++kt) {
        const bf16x8 a = ldsr128(aK[ks & 1] + reg + (ks >> 1) * 128 + kt * 6144);
#pragma unroll
        for (int i = 0; i < NQ; ++i) s[kt][i] = MFMA(a, qf[i][ks], s[kt][i]);
      }
  };
  auto pv = [&](unsigned reg) {
#pragma unroll
    for (int dt = 0; dt < 12; ++dt) {
      const bf16x8 a = trd2(aV[dt & 3] + reg + (dt >> 2) * 128, 6144);
#pragma unroll
      for (int i = 0; i < NQ; ++i) o[dt][i] = MFMA(a, pf[i], o[dt][i]);
    }
  };
  auto lanemax = [&](const f32x4 (&s)[2][NQ], int i) {
    return fmaxf(fmaxf(fmaxf(s[0][i][0], s[0][i][1]), fmaxf(s[0][i][2], s[0][i][3])), fmaxf(fmaxf(s[1][i][0], s[1][i][1]), fmaxf(s[1][i][2], s[1][i][3])));
  };
  {
    f32x4 s[2][NQ];
#pragma unroll
    for (int i = 0; i < NQ; ++i) { s[0][i] = f32x4{}; s[1][i] = f32x4{}; }
    qk(s, 0);
#pragma unroll
    for (int i = 0; i < NQ; ++i) {
      const float m = xg_max(lanemax(s, i));
      negm[i] = (f32x4){-m, -m, -m, -m};
      float ps = 0.f;
#pragma unroll
      for (int kt = 0; kt < 2; ++kt)
#pragma unroll
        for (int r = 0; r < 4; ++r) { s[kt][i][r] = __builtin_amdgcn_exp2f(s[kt][i][r] - m); ps += s[kt][i][r]; }
      lsum[i] = ps;
      pf[i] = pack2(s[0][i], s[1][i]);
    }
  }
  unsigned rc = STG, rn = 2 * STG, rf = 0;
#pragma unroll 1
  for (int st = 1; st < 16; ++st) {
    int sq = st; asm volatile("" : "+s"(sq));
    if constexpr (LD) {
      if (sq == 15) waitbar<0>(); else waitbar<12>();
      if (sq < 14) issue_piece(lds0 + rf, vkv, c.srdK, (unsigned)(st + 2) * 12288u);
    } else {
      if (sq == 1) bar(); else if (sq == 2) waitbar<0>(); else waitbar<4>();
      if (sq > 1 && sq < 15) dma4(lds0 + rf + 12288 + wid * 4096, vkv[0], vkv[1], vkv[2], vkv[3], c.srdV, (unsigned)(st + 1) * 12288u + wid * 4096);
    }
    if (sq == 15) issueWo(0);
    f32x4 s[2][NQ];
#pragma unroll
    for (int i = 0; i < NQ; ++i) { s[0][i] = negm[i]; s[1][i] = negm[i]; }
    qk(s, rc);
    float mx = lanemax(s, 0);
    if (NQ == 2) mx = fmaxf(mx, lanemax(s, NQ - 1));
    const bool resc = __any(mx > RESC_THR);
    float sc[NQ];
    if (resc) {
#pragma unroll
      for (int i = 0; i < NQ; ++i) {
        const float d = fmaxf(xg_max(lanemax(s, i)), 0.f);
        s[0][i] -= d; s[1][i] -= d; negm[i] -= d;
        sc[i] = __builtin_amdgcn_exp2f(-d);
        lsum[i] *= sc[i];
      }
    }
    pv(rc);
#pragma unroll
    for (int i = 0; i < NQ; ++i) {
      float ps = lsum[i];
#pragma unroll
      for (int kt = 0; kt < 2; ++kt)
#pragma unroll
        for (int r = 0; r < 4; ++r) { s[kt][i][r] = __builtin_amdgcn_exp2f(s[kt][i][r]); ps += s[kt][i][r]; }
      lsum[i] = ps;
      pf[i] = pack2(s[0][i], s[1][i]);
    }
    if (resc) {
#pragma unroll
      for (int dt = 0; dt < 12; ++dt)
#pragma unroll
        for (int i = 0; i < NQ; ++i) o[dt][i] *= sc[i];
    }
    const unsigned t_ = rc; rc = rn; rn = rf; rf = t_;
    STAMP(1 + st);
  }
  waitbar<NW>();
  issueWo(1);
  pv(STG);
  if (!c.sec) __builtin_amdgcn_s_setprio(0);
  STAMP(17);

  const int item = (c.br * 8 + c.b) * 32 + c.h;
  const unsigned vl = (unsigned)lane * 16u;
  bf16x8 oa[NQ][6], ob[NQ][6];
#pragma unroll
  for (int i = 0; i < NQ; ++i) {
    const float inv = 1.f / xg_sum(lsum[i]);
#pragma unroll
    for (int s = 0; s < 6; ++s) oa[i][s] = pack2(o[2 * s][i] * inv, o[2 * s + 1][i] * inv);
  }
  const unsigned owave = (unsigned)WS_O + (unsigned)wid * 12288u;
  {
    const unsigned so = owave + (unsigned)item * OBLK;
#pragma unroll
    for (int i = 0; i < NQ; ++i) {
      bstore_sc1<0>(oa[i][0], vl, c.srdWs, so + i * 6144); bstore_sc1<1024>(oa[i][1], vl, c.srdWs, so + i * 6144); bstore_sc1<2048>(oa[i][2], vl, c.srdWs, so + i * 6144);
      bstore_sc1<0>(oa[i][3], vl, c.srdWs, so + i * 6144 + 3072); bstore_sc1<1024>(oa[i][4], vl, c.srdWs, so + i * 6144 + 3072); bstore_sc1<2048>(oa[i][5], vl, c.srdWs, so + i * 6144 + 3072);
    }
  }
  auto loadO = [&](bf16x8 (&d)[NQ][6], int rel) {
    const unsigned so = owave + (unsigned)((item & ~3) | ((hq + rel) & 3)) * OBLK;
#pragma unroll
    for (int i = 0; i < NQ; ++i) {
      bload_sc1<0>(d[i][0], vl, c.srdWs, so + i * 6144); bload_sc1<1024>(d[i][1], vl, c.srdWs, so + i * 6144); bload_sc1<2048>(d[i][2], vl, c.srdWs, so + i * 6144);
      bload_sc1<0>(d[i][3], vl, c.srdWs, so + i * 6144 + 3072); bload_sc1<1024>(d[i][4], vl, c.srdWs, so + i * 6144 + 3072); bload_sc1<2048>(d[i][5], vl, c.srdWs, so + i * 6144 + 3072);
    }
  };
  auto loadO2 = [&](bf16x8 (&d)[NQ][6], int rel, int s) {
    const unsigned so = owave + (unsigned)((item & ~3) | ((hq + rel) & 3)) * OBLK + s * 2048;
#pragma unroll
    for (int i = 0; i < NQ; ++i) { bload_sc1<0>(d[i][2 * s], vl, c.srdWs, so + i * 6144); bload_sc1<1024>(d[i][2 * s + 1], vl, c.srdWs, so + i * 6144); }
    FENCE();
  };
  auto pinO = [&](bf16x8 (&d)[NQ][6]) {
#pragma unroll
    for (int i = 0; i < NQ; ++i)
#pragma unroll
      for (int s = 0; s < 6; ++s) pin(d[i][s]);
  };

  unsigned aO[2][2];
  aO[0][0] = lds0 + (4 * g + tq) * 320 + ((tp ^ (g & 1)) << 4) + 8 * (tp & 1);
  aO[0][1] = aO[0][0] ^ 8;
  aO[1][0] = aO[0][0] + 2 * WSLOT; aO[1][1] = aO[0][1] + 2 * WSLOT;
  f32x4 y[5][2][NQ];
#pragma unroll
  for (int P = 0; P < 5; ++P)
#pragma unroll
    for (int T = 0; T < 2; ++T)
#pragma unroll
      for (int i = 0; i < NQ; ++i) y[P][T][i] = f32x4{};
  constexpr int WN = 8;
  bf16x8 wf[WN];
  auto rd = [&](int n) {
    const int j = n / 20, k = n % 20, sl = (j + 3) & 3;
    wf[n % WN] = trd2(aO[sl >> 1][k & 1] + (sl & 1) * WSLOT + (k / 10) * 10240 + ((k % 10) >> 1) * 64, 5120);
  };
  auto mf = [&](int n, const bf16x8 (&of)[NQ][6]) {
    const int j = n / 20, k = n % 20;
#pragma unroll
    for (int i = 0; i < NQ; ++i) y[(k % 10) >> 1][k & 1][i] = MFMA(wf[n % WN], of[i][2 * (j % 3) + k / 10], y[(k % 10) >> 1][k & 1][i]);
  };
  auto run = [&](int n0, int n1, int nr, const bf16x8 (&of)[NQ][6], int ja, int jb, int k0) {
#pragma unroll
    for (int n = n0; n < n1; ++n) {
      mf(n, of);
      if (n + WN < nr) rd(n + WN);
      const int k = k0 + n - n0;
      if (ja >= 0 && k < NW) wo1(ja, k);
      else if (jb >= 0 && k < 2 * NW) wo1(jb, k - NW);
      FENCE();
    }
  };
  const unsigned flagO = (unsigned)(item >> 2) * 16u;
  constexpr int NO = 6 * NQ, B = 20 - WN;
  waitbar<NW + NO>();
#pragma unroll
  for (int n = 0; n < WN; ++n) rd(n);
  if constexpr (LD) {
#pragma unroll
    for (int k = 0; k < 10; ++k) wo1(2, k);
    FENCE(); run(0, B, 60, oa, 2, 3, 10);
  } else { FENCE(); run(0, B, 60, oa, 2, 3, 0); }
  vmbar<NO + 2 * NW>(); run(B, 20 + B, 60, oa, -1, -1, 0);
  vmbar<0>();
  if (wid == 0) set_flag(c.srdWs, flagO + hq * 4, lane);
  run(20 + B, 60, 60, oa, 4, -1, 0);
  STAMP(18);
  poll_flags(c.srdWs, flagO, lane, 4);
  STAMP(19);
  loadO(ob, 1); loadO(oa, 2);
  bar();
#pragma unroll
  for (int n = 60; n < 60 + WN; ++n) rd(n);
  issueWo(5);
  constexpr int NB = 2 * NQ;
  waitvm<NO + NW>(); pinO(ob); FENCE();
  run(60, 60 + B, 240, ob, -1, -1, 0);
  vmbar<63>(); run(60 + B, 84, 240, ob, 6, -1, 0); loadO2(ob, 3, 0); run(84, 80 + B, 240, ob, -1, -1, 0);
  vmbar<NW + NB>(); pinO(oa); run(80 + B, 104, 240, ob, 7, -1, 0); loadO2(ob, 3, 1); run(104, 100 + B, 240, ob, -1, -1, 0);
  vmbar<NW + 2 * NB>(); run(100 + B, 120, 240, ob, 8, -1, 0); run(120, 124, 240, oa, 8, -1, WN); loadO2(ob, 3, 2); run(124, 120 + B, 240, oa, -1, -1, 0);
  vmbar<NW + 2 * NB>(); run(120 + B, 140 + B, 240, oa, 9, -1, 0);
  vmbar<NW + NB>(); run(140 + B, 160 + B, 240, oa, 10, -1, 0);
  vmbar<NW>(); pinO(ob); run(160 + B, 180, 240, oa, 11, -1, 0); run(180, 180 + B, 240, ob, 11, -1, WN);
  vmbar<NW>(); run(180 + B, 200 + B, 240, ob, -1, -1, 0);
  vmbar<0>(); run(200 + B, 240, 240, ob, -1, -1, 0);
  STAMP(20);

  const int grp = (c.br * 8 + c.b) * 4 + hq, ks8 = c.h >> 2;
  {
    const unsigned so = (unsigned)WS_P + (unsigned)(grp * 8 + ks8) * PBLK + (unsigned)wid * 10240u;
#pragma unroll
    for (int i = 0; i < NQ; ++i) {
      bstore_sc1<0>(pack2(y[0][0][i], y[0][1][i]), vl, c.srdWs, so + i * 5120); bstore_sc1<1024>(pack2(y[1][0][i], y[1][1][i]), vl, c.srdWs, so + i * 5120);
      bstore_sc1<2048>(pack2(y[2][0][i], y[2][1][i]), vl, c.srdWs, so + i * 5120); bstore_sc1<3072>(pack2(y[3][0][i], y[3][1][i]), vl, c.srdWs, so + i * 5120);
      bstore_sc1<0>(pack2(y[4][0][i], y[4][1][i]), vl, c.srdWs, so + i * 5120 + 4096);
    }
  }
  waitbar<0>();
  const unsigned flagP = 4096u + (unsigned)grp * 32u;
  if (wid == 0) set_flag(c.srdWs, flagP + ks8 * 4, lane);
  STAMP(21);
  if (ks8 < 7) {
    poll_flags(c.srdWs, flagP, lane, 8);
    STAMP(22);
    constexpr int NP = NQ == 2 ? 1 : 2;
    bf16x8 pr[NP][8];
    const unsigned so = (unsigned)WS_P + (unsigned)(grp * 8) * PBLK + (unsigned)ks8 * 5120u + (unsigned)wid * 1024u;
#pragma unroll
    for (int k = 0; k < 8; ++k) {
      bload_sc1<0>(pr[0][k], vl, c.srdWs, so + k * PBLK);
      if (NP == 2) bload_sc1<1024>(pr[NP - 1][k], vl, c.srdWs, so + k * PBLK);
    }
    waitvm<0>();
#pragma unroll
    for (int p = 0; p < NP; ++p) {
      float a[8] = {0.f, 0.f, 0.f, 0.f, 0.f, 0.f, 0.f, 0.f};
#pragma unroll
      for (int k = 0; k < 8; ++k) {
        pin(pr[p][k]);
        const u32x4 w = __builtin_bit_cast(u32x4, pr[p][k]);
#pragma unroll
        for (int e = 0; e < 4; ++e) { a[2 * e] += bflo(w[e]); a[2 * e + 1] += bfhi(w[e]); }
      }
      const unsigned u0 = cvtpk(a[0], a[1]), u1 = cvtpk(a[2], a[3]), u2 = cvtpk(a[4], a[5]), u3 = cvtpk(a[6], a[7]);
      const bool odd = g & 1;
      const u32x4 w = {odd ? u2 : u0, odd ? u3 : u1, odd ? u0 : u2, odd ? u1 : u3};
      *(u32x4*)(out + (size_t)(c.b * SQ + ks8 * 16 + l15) * (2 * DM) + c.br * DM + hq * 160 + (wid + p) * 32 + 8 * g) = w;
    }
  }
}

__global__ __launch_bounds__(256, 2) void fused_kernel(const u16* __restrict__ xA, const u16* __restrict__ kA, const u16* __restrict__ vA, const u16* __restrict__ wqA, const u16* __restrict__ woA,
                                                       const u16* __restrict__ xB, const u16* __restrict__ kB, const u16* __restrict__ vB, const u16* __restrict__ wqB, const u16* __restrict__ woB,
                                                       u16* __restrict__ out, char* ws) {
  extern __shared__ __attribute__((aligned(16))) char lds[];
  Ctx c;
  c.lane = threadIdx.x & 63;
  c.wid = rfl(threadIdx.x >> 6);
  const int xcd = blockIdx.x & 7, idx = blockIdx.x >> 3;
  c.sec = idx >> 5;
  c.h = 8 * (xcd & 3) + 4 * (idx >> 5) + (idx & 3); c.b = (idx >> 2) & 7; c.br = xcd >> 2;
  const u16* x = c.br ? xB : xA; const u16* kc = c.br ? kB : kA; const u16* vc = c.br ? vB : vA; const u16* wq = c.br ? wqB : wqA; const u16* wo = c.br ? woB : woA;
  const size_t kvoff = (size_t)(c.b * NH + c.h) * SK * DH;
  c.srdX = __builtin_amdgcn_make_buffer_rsrc((void*)(x + (size_t)c.b * SQ * DM), (short)0, SQ * DM * 2, 0x00020000);
  c.srdW = __builtin_amdgcn_make_buffer_rsrc((void*)(wq + c.h * DH), (short)0, (DM * HD - c.h * DH) * 2, 0x00020000);
  c.srdK = __builtin_amdgcn_make_buffer_rsrc((void*)(kc + kvoff), (short)0, SK * DH * 2, 0x00020000);
  c.srdV = __builtin_amdgcn_make_buffer_rsrc((void*)(vc + kvoff), (short)0, SK * DH * 2, 0x00020000);
  c.srdWo = __builtin_amdgcn_make_buffer_rsrc((void*)(wo + (size_t)(c.h >> 2) * 4 * DH * DM + (c.h & 3) * 160), (short)0, (4 * DH * DM - (c.h & 3) * 160) * 2, 0x00020000);
  c.srdWs = __builtin_amdgcn_make_buffer_rsrc((void*)ws, (short)0, (int)WS_END, 0x00020000);
  c.lds0 = (unsigned)(uintptr_t)lds;
  if (!c.sec) __builtin_amdgcn_s_setprio(1);
  if (c.wid < 3) body<2>(c, out, ws); else body<1>(c, out, ws);
}

extern "C" void kernel_launch(void* const* d_in, const int* in_sizes, int n_in, void* d_out, int out_size, void* d_ws, size_t ws_size, hipStream_t stream) {
  (void)in_sizes; (void)n_in; (void)out_size; (void)ws_size;
  (void)hipFuncSetAttribute((const void*)fused_kernel, hipFuncAttributeMaxDynamicSharedMemorySize, LDS_BYTES + STAMP_LDS);
  (void)hipMemsetAsync(d_ws, 0, FLAG_BYTES, stream);
  fused_kernel<<<NWG, 256, LDS_BYTES + STAMP_LDS, stream>>>((const u16*)d_in[0], (const u16*)d_in[1], (const u16*)d_in[2], (const u16*)d_in[3], (const u16*)d_in[4],
                                                 (const u16*)d_in[5], (const u16*)d_in[6], (const u16*)d_in[7], (const u16*)d_in[8], (const u16*)d_in[9],
                                                 (u16*)d_out, (char*)d_ws);
}
```

```cpp
#define DUMP_STAMPS
#include <cstdio>
#include <hip/hip_runtime.h>
#include <hip/hip_bf16.h>
#include <stdint.h>
#include <type_traits>
#pragma clang diagnostic ignored "-Winline-asm"

typedef unsigned short u16;
typedef short bf16x8 __attribute__((ext_vector_type(8)));
typedef short s16x4 __attribute__((ext_vector_type(4)));
typedef float f32x4 __attribute__((ext_vector_type(4)));
typedef float f32x2 __attribute__((ext_vector_type(2)));
typedef __bf16 bf16x2_t __attribute__((ext_vector_type(2)));
typedef unsigned u32x4 __attribute__((ext_vector_type(4)));
typedef unsigned u32x2 __attribute__((ext_vector_type(2)));
typedef __amdgpu_buffer_rsrc_t rsrc_t;
#define LAS __attribute__((address_space(3)))
#define DI __device__ __forceinline__

constexpr int SQ = 112, DM = 640, NH = 32, SK = 512, DH = 192, HD = NH * DH;
constexpr int PIECE = 12288, NP = 13;
constexpr int WOFF = 4 * PIECE;
constexpr int XOFF = 8 * PIECE, XSLOT = 28672;
constexpr int WSLOT = 20480, NWS = 7;
constexpr int CTL = NP * PIECE;
constexpr int C_RDYA = 0, C_RDYK = 256, C_RDYG = 512, C_RDYO = 768, C_DONE = 1024, C_TAG = 2816, C_CNT = 2832, C_JUNK = 3072;
constexpr int LDS_BYTES = 163840;
constexpr unsigned FLAG_BYTES = 8192;
constexpr unsigned OBLK = 7 * 12 * 1024;
constexpr unsigned PBLK = 7 * 10 * 1024;
constexpr size_t WS_O = FLAG_BYTES;
constexpr size_t WS_P = WS_O + (size_t)256 * OBLK;
constexpr size_t WS_END = WS_P + (size_t)256 * PBLK;
constexpr float QSCALE = 0.07216878364870322f * 1.4426950408889634f;
constexpr float RESC_THR = 8.0f;
constexpr unsigned KVITEM = NH * SK * DH * 2;
constexpr int D_ATT = 20, D_G4 = 84;
#define INFL 36

DI void mfma(f32x4& d, const bf16x8& a, const bf16x8& b) { asm("v_mfma_f32_16x16x32_bf16 %0, %1, %2, %0" : "+v"(d) : "v"(a), "v"(b)); }
DI void mfmac(f32x4& d, const bf16x8& a, const bf16x8& b, const f32x4& c) { asm("v_mfma_f32_16x16x32_bf16 %0, %1, %2, %3" : "=v"(d) : "v"(a), "v"(b), "v"(c)); }
DI void mfma0(f32x4& d, const bf16x8& a, const bf16x8& b) { asm("v_mfma_f32_16x16x32_bf16 %0, %1, %2, 0" : "=v"(d) : "v"(a), "v"(b)); }
#define FENCE() __builtin_amdgcn_sched_barrier(0)
template <int N> DI void nops() { FENCE(); asm volatile("s_nop %0" :: "i"(N - 1)); FENCE(); }
DI int lane_id() { int l; asm volatile("v_mbcnt_lo_u32_b32 %0, -1, 0\n\tv_mbcnt_hi_u32_b32 %0, -1, %0" : "=v"(l)); return l; }
DI unsigned rfl(unsigned x) { return __builtin_amdgcn_readfirstlane(x); }
DI unsigned cvtpk(float lo, float hi) { f32x2 v = {lo, hi}; bf16x2_t b = __builtin_convertvector(v, bf16x2_t); return __builtin_bit_cast(unsigned, b); }
DI s16x4 trd(unsigned a) { return __builtin_bit_cast(s16x4, __builtin_amdgcn_ds_read_tr16_b64_v4i16((LAS s16x4*)a)); }
DI void trlo(bf16x8& d, unsigned a) { const s16x4 t = trd(a); d[0] = t[0]; d[1] = t[1]; d[2] = t[2]; d[3] = t[3]; }
DI void trhi(bf16x8& d, unsigned a) { const s16x4 t = trd(a); d[4] = t[0]; d[5] = t[1]; d[6] = t[2]; d[7] = t[3]; }
DI bf16x8 trd2(unsigned a, unsigned d) { s16x4 lo = trd(a), hi = trd(a + d); return (bf16x8){lo[0], lo[1], lo[2], lo[3], hi[0], hi[1], hi[2], hi[3]}; }
DI bf16x8 ldsr128(unsigned a) { return *(const LAS bf16x8*)a; }
DI bf16x8 pack2(const f32x4& a, const f32x4& b) {
  u32x4 w = {cvtpk(a[0], a[1]), cvtpk(a[2], a[3]), cvtpk(b[0], b[1]), cvtpk(b[2], b[3])};
  return __builtin_bit_cast(bf16x8, w);
}
DI float bflo(unsigned w) { return __uint_as_float(w << 16); }
DI float bfhi(unsigned w) { return __uint_as_float(w & 0xFFFF0000u); }
DI float addf(float a, float b) { float d = a + b; asm("" : "+v"(d)); return d; }
DI int max3i(int a, float b, float c) { return max(max(a, __float_as_int(b)), __float_as_int(c)); }
DI f32x2 sw16(float v) { const unsigned u = __float_as_uint(v); const auto r = __builtin_amdgcn_permlane16_swap(u, u, false, false); return f32x2{__uint_as_float(r[0]), __uint_as_float(r[1])}; }
DI f32x2 sw32(float v) { const unsigned u = __float_as_uint(v); const auto r = __builtin_amdgcn_permlane32_swap(u, u, false, false); return f32x2{__uint_as_float(r[0]), __uint_as_float(r[1])}; }
DI float xg_max(float v) { f32x2 r = sw16(v); r = sw32(fmaxf(r[0], r[1])); return fmaxf(r[0], r[1]); }
DI float xg_sum(float v) { f32x2 r = sw16(v); r = sw32(r[0] + r[1]); return r[0] + r[1]; }

#define KVPOL ""
DI void dma4(unsigned m0v, unsigned v0, unsigned v1, unsigned v2, unsigned v3, rsrc_t srd, unsigned soff) {
  asm volatile("s_mov_b32 m0, %0\n\ts_nop 0\n\t"
               "buffer_load_dwordx4 %1, %5, %6 offen lds\n\t"
               "buffer_load_dwordx4 %2, %5, %6 offen offset:1024 lds\n\t"
               "buffer_load_dwordx4 %3, %5, %6 offen offset:2048 lds\n\t"
               "buffer_load_dwordx4 %4, %5, %6 offen offset:3072 lds"
               :: "s"(m0v), "v"(v0), "v"(v1), "v"(v2), "v"(v3), "s"(srd), "s"(soff) : "m0", "memory");
}
DI void dma3k(unsigned m0v, unsigned v0, unsigned v1, unsigned v2, rsrc_t srd, unsigned soff) {
  asm volatile("s_mov_b32 m0, %0\n\ts_nop 0\n\t"
               "buffer_load_dwordx4 %1, %4, %5 offen" KVPOL " lds\n\t"
               "buffer_load_dwordx4 %2, %4, %5 offen offset:1024" KVPOL " lds\n\t"
               "buffer_load_dwordx4 %3, %4, %5 offen offset:2048" KVPOL " lds"
               :: "s"(m0v), "v"(v0), "v"(v1), "v"(v2), "s"(srd), "s"(soff) : "m0", "memory");
}
DI void touch(unsigned m0v, unsigned v, rsrc_t srd, unsigned soff) {
  asm volatile("s_mov_b32 m0, %0\n\ts_nop 0\n\tbuffer_load_dword %1, %2, %3 offen lds" :: "s"(m0v), "v"(v), "s"(srd), "s"(soff) : "m0", "memory");
}
template <int OFF> DI void bload_sc1(bf16x8& d, unsigned v, rsrc_t srd, unsigned soff) {
  asm volatile("buffer_load_dwordx4 %0, %1, %2, %3 offen offset:%4 sc1" : "=v"(d) : "v"(v), "s"(srd), "s"(soff), "i"(OFF) : "memory");
}
template <int OFF> DI void bstore_sc1(const bf16x8& d, unsigned v, rsrc_t srd, unsigned soff) {
  asm volatile("buffer_store_dwordx4 %0, %1, %2, %3 offen offset:%4 sc1\n\ts_nop 1" :: "v"(d), "v"(v), "s"(srd), "s"(soff), "i"(OFF) : "memory");
}
template <int N> DI void waitvm() { asm volatile("s_waitcnt vmcnt(%0)" :: "i"(N) : "memory"); }
template <typename T> DI void pin(T& x) { asm volatile("" : "+v"(x)); }
DI void cbar() { asm volatile("" ::: "memory"); }

#define STAMP_(i, ins) do { unsigned long long t_; asm volatile(ins " %0\n\ts_waitcnt lgkmcnt(0)" : "=s"(t_) :: "memory"); \
  asm volatile("s_mov_b32 m0, %2\n\ts_nop 4\n\tv_writelane_b32 %0, %1, m0" : "+v"(stampv) : "s"((unsigned)t_), "s"(i) : "m0"); } while (0)
#define STAMP(i) STAMP_(i, "s_memrealtime")
#define CSTAMP(i) STAMP_(i, "s_memtime")

struct Ctx {
  rsrc_t srdX, srdW, srdK, srdV, srdWo, srdWs, srdC;
  unsigned lds0, cshare;
  int wid, br, bp, h;
};

DI unsigned cread(unsigned a) { return *(const volatile LAS unsigned*)a; }
DI void cwrite(unsigned a, unsigned v) { *(volatile LAS unsigned*)a = v; }
DI void need(unsigned& have, unsigned a, unsigned n) {
  if (__builtin_expect(have < n, 0)) {
    unsigned spins = 0;
    for (;;) {
      have = rfl(cread(a));
      if (have >= n) break;
      if (++spins > (1u << 20)) { have = ~0u; break; }
      __builtin_amdgcn_s_sleep(1);
    }
  }
  cbar();
}
DI void seen(unsigned& have, unsigned pre) { have = max(have, rfl(pre)); }

struct Tag { unsigned lo, hi; };
DI bool poll_flags(rsrc_t srdWs, unsigned off, unsigned stride, int lane, int nflag, Tag tag) {
  const unsigned vo = off + (unsigned)(lane < nflag ? lane : 0) * stride;
  for (unsigned spins = 0; spins < (1u << 20); ++spins) {
    u32x2 f;
    asm volatile("buffer_load_dwordx2 %0, %1, %2, 0 offen sc1\n\ts_waitcnt vmcnt(0)" : "=v"(f) : "v"(vo), "s"(srdWs) : "memory");
    if (__all(f[0] == tag.lo && f[1] == tag.hi)) return true;
    __builtin_amdgcn_s_sleep(2);
  }
  return false;
}
DI void set_flag(rsrc_t srdWs, unsigned off, int lane, Tag tag) {
  if (lane == 0) { const u32x2 t = {tag.lo, tag.hi}; asm volatile("buffer_store_dwordx2 %0, off, %1, %2 sc1\n\ts_nop 1" :: "v"(t), "s"(srdWs), "s"(off) : "memory"); }
}
DI void arrive(const Ctx& c, int which, unsigned off, int lane) {
  waitvm<0>();
  unsigned old = 0;
  if (lane == 0) old = __hip_atomic_fetch_add((LAS unsigned*)(c.lds0 + CTL + C_CNT + 4 * which), 1u, __ATOMIC_RELAXED, __HIP_MEMORY_SCOPE_WORKGROUP);
  if (rfl(old) == 6) {
    const Tag tag{rfl(cread(c.lds0 + CTL + C_TAG)), rfl(cread(c.lds0 + CTL + C_TAG + 4))};
    set_flag(c.srdWs, off, lane, tag);
  }
}

DI void reduce_store(const bf16x8 (&pr)[8], u16* __restrict__ dst, bool odd) {
  float a[8] = {0.f, 0.f, 0.f, 0.f, 0.f, 0.f, 0.f, 0.f};
#pragma unroll
  for (int k = 0; k < 8; ++k) {
    const u32x4 w = __builtin_bit_cast(u32x4, pr[k]);
#pragma unroll
    for (int e = 0; e < 4; ++e) { a[2 * e] += bflo(w[e]); a[2 * e + 1] += bfhi(w[e]); }
  }
  const unsigned u0 = cvtpk(a[0], a[1]), u1 = cvtpk(a[2], a[3]), u2 = cvtpk(a[4], a[5]), u3 = cvtpk(a[6], a[7]);
  const u32x4 w = {odd ? u2 : u0, odd ? u3 : u1, odd ? u0 : u2, odd ? u1 : u3};
  *(u32x4*)dst = w;
}

DI void loader(const Ctx& c, u16* __restrict__ out, char* ws) {
  const int lane = lane_id();
  const unsigned lds0 = c.lds0, ctl = lds0 + CTL + lane * 4;
  const int hq = c.h & 3;
  unsigned stampv = 0;
  STAMP(0); CSTAMP(32);
  const unsigned flag0 = (unsigned)(c.br * 4 + c.bp) * 1024u;
  u32x2 ep;
  asm volatile("buffer_load_dwordx2 %0, off, %1, %2 sc1" : "=v"(ep) : "s"(c.srdWs), "s"(flag0) : "memory");
  unsigned vw[12], vx[4];
#pragma unroll
  for (int k = 0; k < 12; ++k) {
    const int id = 64 * k + lane, row = id / 24, pos = id % 24;
    vw[k] = (unsigned)(row * (HD * 2) + (pos >> 3) * 128 + (((pos & 7) ^ (((row >> 1) & 1) | (((row >> 3) & 1) << 2))) << 4) - 1024 * (k & 3));
  }
#pragma unroll
  for (int k = 0; k < 4; ++k)
    vx[k] = (unsigned)(((lane >> 3) + 8 * (k & 1) + SQ * (k >> 1)) * (DM * 2) + (((lane & 7) ^ (((lane >> 4) & 3) << 1)) << 4) - 1024 * k);
  const int rot = (c.bp + hq) & 3;
  unsigned cw = (unsigned)(rot * 5 >> 1) * 2u, cx = (unsigned)(rot * 5 >> 1);
  auto issueW = [&](int slot) {
    const unsigned m = lds0 + WOFF + slot * PIECE, so = cw * (32 * HD * 2);
#pragma unroll
    for (int q = 0; q < 3; ++q) dma4(m + 4096 * q, vw[4 * q], vw[4 * q + 1], vw[4 * q + 2], vw[4 * q + 3], c.srdW, so);
    cw = cw == 19 ? 0 : cw + 1;
  };
  auto issueX = [&](int slot) {
    const unsigned m = lds0 + XOFF + slot * XSLOT, so = cx * 128;
#pragma unroll
    for (int w = 0; w < 7; ++w) dma4(m + 4096 * w, vx[0], vx[1], vx[2], vx[3], c.srdX, so + w * (16 * DM * 2));
    cx = cx == 9 ? 0 : cx + 1;
  };
  const unsigned vtc = (unsigned)lane * 4096u;
  auto touchC = [&]() { touch(lds0 + CTL + C_JUNK, vtc, c.srdC, c.cshare); };
  issueW(0); issueX(0); issueW(1);
  cwrite(ctl + C_RDYA, 0); cwrite(ctl + C_RDYK, 0); cwrite(ctl + C_RDYG, 0); cwrite(ctl + C_RDYO, 0); cwrite(ctl + C_TAG, 0);
  asm volatile("s_waitcnt lgkmcnt(0)\n\ts_barrier" ::: "memory");
  unsigned dv = 0;
  const unsigned adone = lds0 + CTL + C_DONE + (lane < 7 ? lane : 0) * 260;
  auto gate = [&](unsigned p) {
    if (!__all(dv >= p)) {
      for (unsigned spins = 0;; ++spins) {
        dv = cread(adone);
        if (__all(dv >= p)) break;
        if (spins > (1u << 20)) { dv = ~0u; break; }
        __builtin_amdgcn_s_sleep(1);
      }
    }
    cbar();
  };
  asm volatile("s_waitcnt vmcnt(12)" : "+v"(ep) :: "memory");
  const unsigned long long tg = (((unsigned long long)rfl(ep[1]) << 32) | rfl(ep[0])) + 1ull;
  const Tag tag{(unsigned)tg, (unsigned)(tg >> 32)};
  if (lane == 0) { cwrite(lds0 + CTL + C_TAG, tag.lo); cwrite(lds0 + CTL + C_TAG + 4, tag.hi); }
  cwrite(ctl + C_RDYA, 1);
  touchC();
  issueW(2); issueX(1);
  waitvm<41>(); cwrite(ctl + C_RDYA, 2);
  issueW(3);
  waitvm<12>(); cwrite(ctl + C_RDYA, 3);
  STAMP(24);
#pragma unroll 1
  for (int t = 0; t < 16; t += 2) {
    gate(t + 1);
    issueW(t & 3); issueX((t >> 1) & 1);
    waitvm<40>(); cwrite(ctl + C_RDYA, t + 4);
    gate(t + 2);
    issueW((t + 1) & 3);
    waitvm<12>(); cwrite(ctl + C_RDYA, t + 5);
  }
  STAMP(1);
  unsigned vb[3];
#pragma unroll
  for (int k = 0; k < 3; ++k) { const int row = (64 * k + lane) / 24; vb[k] = (unsigned)(((lane & ~7) | ((lane & 7) ^ (((row >> 1) & 3) << 1))) << 4); }
  unsigned mslot = lds0;
  auto piece = [&](rsrc_t srd, int j) {
    const unsigned so = (unsigned)(j & 15) * PIECE + (unsigned)(j >> 4) * KVITEM;
#pragma unroll
    for (int q = 0; q < 4; ++q) dma3k(mslot + 3072 * q, vb[0], vb[1], vb[2], srd, so + 3072 * q);
    mslot = mslot == lds0 + (NP - 1) * PIECE ? lds0 : mslot + PIECE;
  };
  auto pgate = [&](int g) { gate(g < 8 ? g + 13 : g < 13 ? D_ATT : g + 8); };
  piece(c.srdK, 0);
  waitvm<12>(); cwrite(ctl + C_RDYA, 21);
  touchC();
  piece(c.srdK, 1); piece(c.srdV, 0); piece(c.srdK, 2);
  waitvm<36>(); cwrite(ctl + C_RDYK, 1);
#pragma unroll 1
  for (int j = 2; j < 31; ++j) {
    pgate(2 * j); piece(c.srdV, j - 1);
    waitvm<36>(); cwrite(ctl + C_RDYK, 2 * j - 2);
    pgate(2 * j + 1); piece(c.srdK, j + 1);
    waitvm<36>(); cwrite(ctl + C_RDYK, 2 * j - 1);
    if (j >= 24) touchC();
  }
  pgate(62); piece(c.srdV, 30);
  waitvm<36>(); cwrite(ctl + C_RDYK, 60);
  pgate(63); piece(c.srdV, 31);
  waitvm<36>(); cwrite(ctl + C_RDYK, 61);
  STAMP(2);
  unsigned vo[20];
#pragma unroll
  for (int k = 0; k < 20; ++k) {
    const int id = 64 * k + lane, row = id / 20, pos = id % 20;
    vo[k] = (unsigned)(row * (DM * 2) + ((pos ^ ((row >> 2) & 1)) << 4) - 1024 * (k & 3));
  }
  auto issueWo = [&](int j) {
    const unsigned so = (unsigned)(((hq * 3 + j) % 12) * (64 * DM * 2)), m = lds0 + (j % NWS) * WSLOT;
#pragma unroll
    for (int q = 0; q < 5; ++q) dma4(m + 4096 * q, vo[4 * q], vo[4 * q + 1], vo[4 * q + 2], vo[4 * q + 3], c.srdWo, so);
  };
  auto wgate = [&](int j) { gate(j < NWS ? D_ATT + 53 + (WSLOT * (j + 1) - 1) / PIECE : D_G4 + j - NWS + 1); };
  wgate(0); issueWo(0);
  waitvm<44>(); cwrite(ctl + C_RDYK, 62);
  waitvm<32>(); cwrite(ctl + C_RDYK, 63);
  waitvm<20>(); cwrite(ctl + C_RDYK, 64);
  STAMP(3);
#pragma unroll 1
  for (int j = 1; j < 7; ++j) {
    wgate(j); issueWo(j);
    waitvm<20>(); cwrite(ctl + C_RDYG, j);
  }
  waitvm<0>(); cwrite(ctl + C_RDYG, 7);
  STAMP(4);
  poll_flags(c.srdWs, flag0 + 256u + (unsigned)(c.h >> 2) * 32u, 8, lane, 4, tag);
  cwrite(ctl + C_RDYO, 1);
  STAMP(5);
#pragma unroll 1
  for (int j = 7; j < 12; ++j) {
    wgate(j); issueWo(j);
    waitvm<20>(); cwrite(ctl + C_RDYG, j);
  }
  waitvm<0>(); cwrite(ctl + C_RDYG, 13);
  STAMP(6);
  poll_flags(c.srdWs, flag0 + 512u + (unsigned)hq * 64u, 8, lane, 8, tag);
  cwrite(ctl + C_RDYO, 2);
  if (c.h == 0) set_flag(c.srdWs, flag0, lane, tag);
  STAMP(7);
  const int k8 = c.h >> 2;
  if (k8 < 7) {
    const unsigned so = (unsigned)WS_P + (unsigned)(((c.br * 4 + c.bp) * 4 + hq) * 8) * PBLK + (unsigned)k8 * 10240u + 8192u;
    bf16x8 pr[2][8];
#pragma unroll
    for (int k = 0; k < 8; ++k) { bload_sc1<0>(pr[0][k], lane * 16, c.srdWs, so + k * PBLK); bload_sc1<1024>(pr[1][k], lane * 16, c.srdWs, so + k * PBLK); }
    waitvm<0>();
#pragma unroll
    for (int p = 0; p < 2; ++p) {
#pragma unroll
      for (int k = 0; k < 8; ++k) pin(pr[p][k]);
      reduce_store(pr[p], out + (size_t)((2 * c.bp + 1) * SQ + k8 * 16 + (lane & 15)) * (2 * DM) + c.br * DM + hq * 160 + (3 + p) * 32 + 8 * (lane >> 4), (lane >> 4) & 1);
    }
  }
  waitvm<0>();
  STAMP(23); CSTAMP(33);
  { unsigned a0 = __builtin_amdgcn_readlane(stampv, 0), a1 = __builtin_amdgcn_readlane(stampv, 1), a3 = __builtin_amdgcn_readlane(stampv, 3), a7 = __builtin_amdgcn_readlane(stampv, 7), a24 = __builtin_amdgcn_readlane(stampv, 24);
    if (lane == 0 && (blockIdx.x & 0x7C) == 0 && (blockIdx.x & 3) != 1 && (blockIdx.x & 3) != 2) printf("b%d %u %u %u %u\n", blockIdx.x, a24 - a0, a1 - a0, a3 - a0, a7 - a0); }
}

DI void consumer(const Ctx& c, u16* __restrict__ out, char* ws) {
  const int wid = c.wid;
  int lane, l15, g, tq, tp;
  auto relane = [&]() { lane = lane_id(); l15 = lane & 15; g = lane >> 4; tq = l15 >> 2; tp = lane & 3; };
  relane();
  const unsigned lds0 = c.lds0;
  unsigned stampv = 0;
  STAMP(0); CSTAMP(32);
  const int hq = c.h & 3;
  const unsigned flag0 = (unsigned)(c.br * 4 + c.bp) * 1024u;
  const unsigned ctl = lds0 + CTL + lane * 4, adone = ctl + C_DONE + wid * 256;
  cwrite(adone, 0);
  asm volatile("s_waitcnt lgkmcnt(0)\n\ts_barrier" ::: "memory");

  bf16x8 qf[6], qn[6];
  {
    unsigned aW[2][2], aX[2];
#pragma unroll
    for (int s1 = 0; s1 < 2; ++s1)
#pragma unroll
      for (int T = 0; T < 2; ++T)
        aW[s1][T] = lds0 + WOFF + (8 * g + tq) * 384 + (((4 * s1 + tp) ^ ((tq >> 1) | ((g & 1) << 2))) << 4) + 8 * (T ^ (tp & 1));
    aX[0] = lds0 + XOFF + wid * 4096 + l15 * 128 + ((g ^ (((l15 >> 1) & 3) << 1)) << 4);
    aX[1] = aX[0] ^ 64;
    f32x4 acc[12][2];
#pragma unroll
    for (int dt = 0; dt < 12; ++dt) { acc[dt][0] = f32x4{}; acc[dt][1] = f32x4{}; }
    bf16x8 xf[2][2], a[8];
    unsigned have = 0, pre;
    auto adr = [&](int T, int K) { return aW[(K >> 1) & 1][K & 1] + (T & 3) * PIECE + (K >> 2) * 128; };
    auto rdx = [&](int T, int i) { xf[i][T & 1] = ldsr128(aX[T & 1] + ((T >> 1) & 1) * XSLOT + i * 2048); };
    need(have, ctl + C_RDYA, 1);
    rdx(0, 0); rdx(0, 1);
#pragma unroll
    for (int k = 0; k < 8; ++k) a[k] = trd2(adr(0, k), 1536);
    FENCE();
    auto step = [&](auto Tc, int t) {
      constexpr int T = decltype(Tc)::value;
#pragma unroll
      for (int k = 0; k < 12; ++k) {
        const int b = (12 * T + k) & 7;
        const unsigned ra = k < 4 ? adr(T, k + 8) : adr(T + 1, k - 4);
        if (k == 4) { cwrite(adone, t + 1); seen(have, pre); need(have, ctl + C_RDYA, t + 2); FENCE(); }
        mfma(acc[k][0], a[b], xf[0][T & 1]); FENCE();
        if (k == 0) pre = cread(ctl + C_RDYA);
        if (!(T & 1) && (k == 1 || k == 2)) rdx(T + 1, k - 1);
        if ((T & 1) && (k == 5 || k == 6)) rdx(T + 1, k - 5);
        FENCE();
        mfma(acc[k][1], a[b], xf[1][T & 1]); FENCE();
        a[b] = trd2(ra, 1536); FENCE();
      }
    };
    using I0 = std::integral_constant<int, 0>; using I1 = std::integral_constant<int, 1>; using I2 = std::integral_constant<int, 2>; using I3 = std::integral_constant<int, 3>;
#pragma unroll 1
    for (int t = 0; t < 20; t += 4) { step(I0{}, t); step(I1{}, t + 1); step(I2{}, t + 2); step(I3{}, t + 3); STAMP(24 + (t >> 2)); }
    nops<8>();
    relane();
    const bool odd = g & 1;
#pragma unroll
    for (int i = 0; i < 2; ++i)
#pragma unroll
      for (int s = 0; s < 6; ++s) {
        const f32x4 t0 = acc[2 * s][i] * QSCALE, t1 = acc[2 * s + 1][i] * QSCALE;
        const unsigned u0 = cvtpk(t0[0], t0[1]), u1 = cvtpk(t0[2], t0[3]), u2 = cvtpk(t1[0], t1[1]), u3 = cvtpk(t1[2], t1[3]);
        const u32x4 w = {odd ? u2 : u0, odd ? u3 : u1, odd ? u0 : u2, odd ? u1 : u3};
        (i ? qn[s] : qf[s]) = __builtin_bit_cast(bf16x8, w);
      }
  }
  STAMP(1);

  const int item = (c.br * 4 + c.bp) * 32 + c.h;
  const unsigned vl = (unsigned)lane * 16u;
  bf16x8 oa[2][6], ob[2][6];
  {
    unsigned bK[2], bV[4], aK[2], aV[4];
    bK[0] = l15 * 384 + ((g ^ (((l15 >> 1) & 3) << 1)) << 4);
    bK[1] = bK[0] ^ 64;
#pragma unroll
    for (int d4 = 0; d4 < 4; ++d4) bV[d4] = (4 * g + tq) * 384 + (((2 * d4 + (tp >> 1)) ^ (((2 * g + (tq >> 1)) & 3) << 1)) << 4) + 8 * (tp & 1);
    unsigned have = 0, pre = 0, slot = lds0, gp = 0;
    auto next = [&]() {
      seen(have, pre);
      need(have, ctl + C_RDYK, ++gp);
      const unsigned s = slot;
      slot = slot == lds0 + (NP - 1) * PIECE ? lds0 : slot + PIECE;
      return s;
    };
    auto nextK = [&]() { const unsigned s = next(); aK[0] = bK[0] + s; aK[1] = bK[1] + s; };
    auto setV = [&](unsigned s) {
#pragma unroll
      for (int d4 = 0; d4 < 4; ++d4) aV[d4] = bV[d4] + s;
    };
    f32x4 o[12], s[2], negm;
    float lsum, sc;
    int mx;
    u32x4 pf;
    bf16x8 w[8];
    bool resc;
    auto rdK = [&](int n) { w[n & 7] = ldsr128(aK[(n % 6) & 1] + ((n % 6) >> 1) * 128 + (n / 6) * 6144); };
    auto vadr = [&](int dt) { return aV[dt & 3] + (dt >> 2) * 128; };
    auto rda = [&](int n) { if (n < 12) rdK(n); else if (n < 24) w[n & 7] = trd2(vadr(n - 12), 6144); else rdK(n - 24); };
    auto mfa = [&](int n) {
      if (n >= 12) mfma(o[n - 12], w[n & 7], __builtin_bit_cast(bf16x8, pf));
      else if (n % 6) mfma(s[n / 6], w[n & 7], qf[n % 6]);
      else mfmac(s[n / 6], w[n & 7], qf[0], negm);
    };
    auto ex = [&](int e) { s[e >> 2][e & 3] = __builtin_amdgcn_exp2f(s[e >> 2][e & 3]); };
    auto ad = [&](int e) { lsum = addf(lsum, s[e >> 2][e & 3]); };
    auto cv = [&]() { pf[0] = cvtpk(s[0][0], s[0][1]); pf[1] = cvtpk(s[0][2], s[0][3]); pf[2] = cvtpk(s[1][0], s[1][1]); pf[3] = cvtpk(s[1][2], s[1][3]); pin(pf); };
    auto mxs = [&](int kt, int hf) { mx = max3i((kt | hf) ? mx : 0, s[kt][2 * hf], s[kt][2 * hf + 1]); };
    auto lanemax = [&]() { return fmaxf(fmaxf(fmaxf(s[0][0], s[0][1]), fmaxf(s[0][2], s[0][3])), fmaxf(fmaxf(s[1][0], s[1][1]), fmaxf(s[1][2], s[1][3]))); };
    auto scale_o = [&]() {
      if (resc) {
#pragma unroll
        for (int dt = 0; dt < 12; ++dt) o[dt] *= sc;
      }
    };
#define SLOT(n, A) do { mfa(n); FENCE(); rda(n + 8); A; FENCE(); } while (0)
    nextK();
#pragma unroll
    for (int n = 0; n < 8; ++n) rdK(n);
#pragma unroll 1
    for (int it = 0; it < 2; ++it) {
      {
        FENCE();
#pragma unroll
        for (int n = 0; n < 12; ++n) {
          if (n % 6) mfma(s[n / 6], w[n & 7], qf[n % 6]); else mfma0(s[n / 6], w[n & 7], qf[0]);
          FENCE();
          if (n < 4) rdK(n + 8);
          o[n] = f32x4{};
          FENCE();
        }
        cwrite(adone, D_ATT + gp);
        nextK();
#pragma unroll
        for (int n = 0; n < 8; ++n) rdK(n);
        nops<8>();
        const float m = xg_max(lanemax());
        negm = (f32x4){-m, -m, -m, -m};
        s[0] -= m; s[1] -= m;
        lsum = 0.f; sc = 1.f; resc = false;
#pragma unroll
        for (int e = 0; e < 8; ++e) ex(e);
#pragma unroll
        for (int e = 0; e < 8; ++e) ad(e);
        cv();
        nops<2>();
      }
#pragma unroll 1
      for (int st = 1; st < 16; ++st) {
        SLOT(0, pre = cread(ctl + C_RDYK));
        SLOT(1, ); SLOT(2, ); SLOT(3, );
        setV(next()); FENCE();
        SLOT(4, ); SLOT(5, );
        scale_o(); FENCE();
        SLOT(6, ); SLOT(7, );
        SLOT(8, mxs(0, 0)); SLOT(9, mxs(0, 1));
        SLOT(10, ); SLOT(11, );
        SLOT(12, pre = cread(ctl + C_RDYK));
        SLOT(13, );
        SLOT(14, mxs(1, 0); mxs(1, 1));
        resc = __any(mx > __builtin_bit_cast(int, RESC_THR));
        if (resc) {
          const float d = fmaxf(xg_max(lanemax()), 0.f);
          s[0] -= d; s[1] -= d; negm -= d;
          sc = __builtin_amdgcn_exp2f(-d);
          lsum *= sc;
        }
        FENCE();
        SLOT(15, ex(0));
        cwrite(adone, D_ATT + gp);
        nextK(); FENCE();
        SLOT(16, ex(1); ad(0)); SLOT(17, ex(2); ad(1)); SLOT(18, ex(3); ad(2)); SLOT(19, ex(4); ad(3));
        SLOT(20, ex(5); ad(4)); SLOT(21, ex(6); ad(5)); SLOT(22, ex(7); ad(6)); SLOT(23, ad(7));
        cv(); FENCE();
        if (st == 1 || st == 8) STAMP(2 + 4 * it + (st >> 3));
      }
      STAMP(4 + 4 * it);
      {
        if (it == 0) setV(next()); else setV(aK[0] - bK[0]);
        bf16x8 v[12];
#pragma unroll
        for (int dt = 0; dt < 12; ++dt) v[dt] = trd2(vadr(dt), 6144);
        if (it) cwrite(adone, D_G4);
        nops<4>();
        scale_o();
        nops<2>();
#pragma unroll
        for (int dt = 0; dt < 12; ++dt) mfma(o[dt], v[dt], __builtin_bit_cast(bf16x8, pf));
        const float inv = 1.f / xg_sum(lsum);
        nops<8>();
        const unsigned so = (unsigned)WS_O + (unsigned)item * OBLK + (unsigned)wid * 12288u + (unsigned)it * 6144u;
#pragma unroll
        for (int q = 0; q < 6; ++q) oa[0][q] = oa[1][q];
#pragma unroll
        for (int q = 0; q < 6; ++q) oa[1][q] = pack2(o[2 * q] * inv, o[2 * q + 1] * inv);
        bstore_sc1<0>(oa[1][0], vl, c.srdWs, so); bstore_sc1<1024>(oa[1][1], vl, c.srdWs, so); bstore_sc1<2048>(oa[1][2], vl, c.srdWs, so);
        bstore_sc1<0>(oa[1][3], vl, c.srdWs, so + 3072); bstore_sc1<1024>(oa[1][4], vl, c.srdWs, so + 3072); bstore_sc1<2048>(oa[1][5], vl, c.srdWs, so + 3072);
#pragma unroll
        for (int q = 0; q < 6; ++q) qf[q] = qn[q];
      }
      STAMP(5 + 4 * it);
    }
  }

  relane();
  auto loadO2 = [&](bf16x8 (&d)[2][6], int rel, int s) {
    const unsigned so = (unsigned)WS_O + (unsigned)((item & ~3) | ((hq + rel) & 3)) * OBLK + (unsigned)wid * 12288u + s * 2048;
#pragma unroll
    for (int i = 0; i < 2; ++i) { bload_sc1<0>(d[i][2 * s], vl, c.srdWs, so + i * 6144); bload_sc1<1024>(d[i][2 * s + 1], vl, c.srdWs, so + i * 6144); }
    FENCE();
  };
  auto pinO = [&](bf16x8 (&d)[2][6], int s) { pin(d[0][2 * s]); pin(d[0][2 * s + 1]); pin(d[1][2 * s]); pin(d[1][2 * s + 1]); };
  unsigned aO[3][2];
  aO[0][0] = lds0 + (4 * g + tq) * 320 + ((tp ^ (g & 1)) << 4) + 8 * (tp & 1);
  aO[0][1] = aO[0][0] ^ 8;
#pragma unroll
  for (int q = 1; q < 3; ++q) { aO[q][0] = aO[0][0] + 3 * q * WSLOT; aO[q][1] = aO[0][1] + 3 * q * WSLOT; }
  f32x4 y[5][2][2];
  constexpr int WN = 8;
  bf16x8 wf[WN];
  unsigned have = 0, pre = 0, haveO = 0;
  auto rd = [&](int n) {
    const int j = n / 20, k = n % 20, sl = j % NWS;
    wf[n % WN] = trd2(aO[sl / 3][k & 1] + (sl % 3) * WSLOT + (k / 10) * 10240 + ((k % 10) >> 1) * 64, 5120);
  };
  auto stage = [&](int j, const bf16x8 (&of)[2][6]) {
#pragma unroll
    for (int k = 0; k < 20; ++k) {
      const int n = 20 * j + k;
      if (k == 20 - WN && j < 5) cwrite(adone, D_G4 + j + 1);
      if (k == 20 - WN && j < 11) { seen(have, pre); need(have, ctl + C_RDYG, j + 2); FENCE(); }
#pragma unroll
      for (int i = 0; i < 2; ++i) {
        if (n < 10) mfma0(y[(k % 10) >> 1][k & 1][i], wf[n % WN], of[i][2 * (j % 3) + k / 10]);
        else mfma(y[(k % 10) >> 1][k & 1][i], wf[n % WN], of[i][2 * (j % 3) + k / 10]);
        FENCE();
        if (i == 0 && k == 16 - WN) pre = cread(ctl + C_RDYG);
        FENCE();
      }
      if (n + WN < 240) rd(n + WN);
      FENCE();
    }
  };
  need(have, ctl + C_RDYG, 1);
#pragma unroll
  for (int n = 0; n < WN; ++n) rd(n);
  FENCE();
  STAMP(10);
  stage(0, oa);
  arrive(c, 0, flag0 + 256u + (unsigned)c.h * 8u, lane);
  STAMP(11);
  stage(1, oa);
  STAMP(12);
  need(haveO, ctl + C_RDYO, 1);
  STAMP(13);
  loadO2(ob, 1, 0); loadO2(ob, 1, 1); loadO2(ob, 1, 2); loadO2(oa, 2, 0); loadO2(oa, 2, 1);
  stage(2, oa); loadO2(oa, 2, 2);
  STAMP(14);
  waitvm<20>(); pinO(ob, 0); FENCE(); STAMP(15); stage(3, ob); loadO2(ob, 3, 0);
  waitvm<20>(); pinO(ob, 1); FENCE(); stage(4, ob); loadO2(ob, 3, 1);
  waitvm<20>(); pinO(ob, 2); FENCE(); stage(5, ob); loadO2(ob, 3, 2);
  STAMP(16);
  waitvm<12>(); pinO(oa, 0); pinO(oa, 1); pinO(oa, 2); FENCE(); stage(6, oa); stage(7, oa); stage(8, oa);
  STAMP(17);
  waitvm<0>(); pinO(ob, 0); pinO(ob, 1); pinO(ob, 2); FENCE(); stage(9, ob); stage(10, ob); stage(11, ob);
  nops<8>();
  STAMP(20);

  const int grp = (c.br * 4 + c.bp) * 4 + hq, k8 = c.h >> 2;
  {
    const unsigned so = (unsigned)WS_P + (unsigned)(grp * 8 + k8) * PBLK + (unsigned)wid * 10240u;
#pragma unroll
    for (int i = 0; i < 2; ++i) {
      bstore_sc1<0>(pack2(y[0][0][i], y[0][1][i]), vl, c.srdWs, so + i * 5120); bstore_sc1<1024>(pack2(y[1][0][i], y[1][1][i]), vl, c.srdWs, so + i * 5120);
      bstore_sc1<2048>(pack2(y[2][0][i], y[2][1][i]), vl, c.srdWs, so + i * 5120); bstore_sc1<3072>(pack2(y[3][0][i], y[3][1][i]), vl, c.srdWs, so + i * 5120);
      bstore_sc1<0>(pack2(y[4][0][i], y[4][1][i]), vl, c.srdWs, so + i * 5120 + 4096);
    }
  }
  relane();
  arrive(c, 1, flag0 + 512u + (unsigned)hq * 64u + (unsigned)k8 * 8u, lane);
  STAMP(21);
  need(haveO, ctl + C_RDYO, 2);
  STAMP(22);
  {
    bf16x8 pr[8];
    const unsigned so = (unsigned)WS_P + (unsigned)(grp * 8) * PBLK + (unsigned)wid * 10240u + (unsigned)k8 * 1024u;
#pragma unroll
    for (int k = 0; k < 8; ++k) bload_sc1<0>(pr[k], vl, c.srdWs, so + k * PBLK);
    waitvm<0>();
#pragma unroll
    for (int k = 0; k < 8; ++k) pin(pr[k]);
    const int i = k8 >= 5, cp = k8 - 5 * i;
    reduce_store(pr, out + (size_t)((2 * c.bp + i) * SQ + wid * 16 + l15) * (2 * DM) + c.br * DM + hq * 160 + cp * 32 + 8 * g, g & 1);
  }
  waitvm<0>();
  STAMP(23); CSTAMP(33);
  if (stampv == 0x12345) ws[0] = 1;
}

__global__ __launch_bounds__(512, 2) void fused_kernel(const u16* __restrict__ xA, const u16* __restrict__ kA, const u16* __restrict__ vA, const u16* __restrict__ wqA, const u16* __restrict__ woA,
                                                       const u16* __restrict__ xB, const u16* __restrict__ kB, const u16* __restrict__ vB, const u16* __restrict__ wqB, const u16* __restrict__ woB,
                                                       u16* __restrict__ out, char* ws) {
  extern __shared__ __attribute__((aligned(16))) char lds[];
  Ctx c;
  c.wid = rfl(threadIdx.x >> 6);
  const int xcd = blockIdx.x & 7, idx = blockIdx.x >> 3;
  c.h = 4 * xcd + (idx & 3); c.bp = (idx >> 2) & 3; c.br = idx >> 4;
  const u16* x = c.br ? xB : xA; const u16* kc = c.br ? kB : kA; const u16* vc = c.br ? vB : vA; const u16* wq = c.br ? wqB : wqA; const u16* wo = c.br ? woB : woA;
  const size_t kvoff = (size_t)(2 * c.bp * NH + c.h) * SK * DH;
  c.srdX = __builtin_amdgcn_make_buffer_rsrc((void*)(x + (size_t)c.bp * 2 * SQ * DM), (short)0, 2 * SQ * DM * 2, 0x00020000);
  c.srdW = __builtin_amdgcn_make_buffer_rsrc((void*)(wq + c.h * DH), (short)0, (DM * HD - c.h * DH) * 2, 0x00020000);
  c.srdK = __builtin_amdgcn_make_buffer_rsrc((void*)(kc + kvoff), (short)0, KVITEM + SK * DH * 2, 0x00020000);
  c.srdV = __builtin_amdgcn_make_buffer_rsrc((void*)(vc + kvoff), (short)0, KVITEM + SK * DH * 2, 0x00020000);
  c.srdWo = __builtin_amdgcn_make_buffer_rsrc((void*)(wo + (size_t)(c.h >> 2) * 4 * DH * DM + (c.h & 3) * 160), (short)0, (4 * DH * DM - (c.h & 3) * 160) * 2, 0x00020000);
  c.srdWs = __builtin_amdgcn_make_buffer_rsrc((void*)ws, (short)0, (int)WS_END, 0x00020000);
  c.lds0 = (unsigned)(uintptr_t)lds;
  {
    unsigned long long pc; unsigned clen;
    asm volatile("s_getpc_b64 %0\n.Lcode_here:\n\ts_mov_b32 %1, .Lcode_end-.Lcode_here" : "=s"(pc), "=s"(clen));
    c.srdC = __builtin_amdgcn_make_buffer_rsrc((void*)pc, (short)0, (int)clen, 0x00020000);
    c.cshare = (unsigned)(idx & 31) * 128u;
  }
  if (c.wid == 7) { __builtin_amdgcn_s_setprio(3); loader(c, out, ws); } else consumer(c, out, ws);
  asm volatile(".Lcode_end:");
}

extern "C" void kernel_launch(void* const* d_in, const int* in_sizes, int n_in, void* d_out, int out_size, void* d_ws, size_t ws_size, hipStream_t stream) {
  (void)in_sizes; (void)n_in; (void)out_size; (void)ws_size;
  (void)hipFuncSetAttribute((const void*)fused_kernel, hipFuncAttributeMaxDynamicSharedMemorySize, LDS_BYTES);
  fused_kernel<<<256, 512, LDS_BYTES, stream>>>((const u16*)d_in[0], (const u16*)d_in[1], (const u16*)d_in[2], (const u16*)d_in[3], (const u16*)d_in[4],
                                                (const u16*)d_in[5], (const u16*)d_in[6], (const u16*)d_in[7], (const u16*)d_in[8], (const u16*)d_in[9],
                                                (u16*)d_out, (char*)d_ws);
}
```

```cpp
#define DUMP_STAMPS
#define PB 0
#define PLO 0
#define PHI 0
#include <hip/hip_runtime.h>
#include <hip/hip_bf16.h>
#include <stdint.h>
#include <type_traits>
#pragma clang diagnostic ignored "-Winline-asm"

typedef unsigned short u16;
typedef short bf16x8 __attribute__((ext_vector_type(8)));
typedef short s16x4 __attribute__((ext_vector_type(4)));
typedef float f32x4 __attribute__((ext_vector_type(4)));
typedef float f32x2 __attribute__((ext_vector_type(2)));
typedef __bf16 bf16x2_t __attribute__((ext_vector_type(2)));
typedef unsigned u32x4 __attribute__((ext_vector_type(4)));
typedef unsigned u32x2 __attribute__((ext_vector_type(2)));
typedef __amdgpu_buffer_rsrc_t rsrc_t;
#define LAS __attribute__((address_space(3)))
#define DI __device__ __forceinline__

constexpr int SQ = 112, DM = 640, NH = 32, SK = 512, DH = 192, HD = NH * DH;
constexpr int PIECE = 12288, NP = 13;
constexpr int WOFF = 4 * PIECE;
constexpr int XOFF = 8 * PIECE, XSLOT = 28672;
constexpr int WSLOT = 20480, NWS = 7;
constexpr int CTL = NP * PIECE;
constexpr int C_RDYA = 0, C_RDYK = 256, C_RDYG = 512, C_RDYO = 768, C_DONE = 1024, C_TAG = 2816, C_CNT = 2832, C_JUNK = 3072;
constexpr int LDS_BYTES = 163840;
constexpr unsigned FLAG_BYTES = 8192;
constexpr unsigned OBLK = 7 * 12 * 1024;
constexpr unsigned PBLK = 7 * 10 * 1024;
constexpr size_t WS_O = FLAG_BYTES;
constexpr size_t WS_P = WS_O + (size_t)256 * OBLK;
constexpr size_t WS_END = WS_P + (size_t)256 * PBLK;
constexpr float QSCALE = 0.07216878364870322f * 1.4426950408889634f;
constexpr float RESC_THR = 8.0f;
constexpr unsigned KVITEM = NH * SK * DH * 2;
constexpr int D_ATT = 20, D_G4 = 84;
#define INFL 36

DI void mfma(f32x4& d, const bf16x8& a, const bf16x8& b) { asm("v_mfma_f32_16x16x32_bf16 %0, %1, %2, %0" : "+v"(d) : "v"(a), "v"(b)); }
DI void mfmac(f32x4& d, const bf16x8& a, const bf16x8& b, const f32x4& c) { asm("v_mfma_f32_16x16x32_bf16 %0, %1, %2, %3" : "=v"(d) : "v"(a), "v"(b), "v"(c)); }
DI void mfma0(f32x4& d, const bf16x8& a, const bf16x8& b) { asm("v_mfma_f32_16x16x32_bf16 %0, %1, %2, 0" : "=v"(d) : "v"(a), "v"(b)); }
#define FENCE() __builtin_amdgcn_sched_barrier(0)
template <int N> DI void nops() { FENCE(); asm volatile("s_nop %0" :: "i"(N - 1)); FENCE(); }
DI int lane_id() { int l; asm volatile("v_mbcnt_lo_u32_b32 %0, -1, 0\n\tv_mbcnt_hi_u32_b32 %0, -1, %0" : "=v"(l)); return l; }
DI unsigned rfl(unsigned x) { return __builtin_amdgcn_readfirstlane(x); }
DI unsigned cvtpk(float lo, float hi) { f32x2 v = {lo, hi}; bf16x2_t b = __builtin_convertvector(v, bf16x2_t); return __builtin_bit_cast(unsigned, b); }
DI s16x4 trd(unsigned a) { return __builtin_bit_cast(s16x4, __builtin_amdgcn_ds_read_tr16_b64_v4i16((LAS s16x4*)a)); }
DI void trlo(bf16x8& d, unsigned a) { const s16x4 t = trd(a); d[0] = t[0]; d[1] = t[1]; d[2] = t[2]; d[3] = t[3]; }
DI void trhi(bf16x8& d, unsigned a) { const s16x4 t = trd(a); d[4] = t[0]; d[5] = t[1]; d[6] = t[2]; d[7] = t[3]; }
DI bf16x8 trd2(unsigned a, unsigned d) { s16x4 lo = trd(a), hi = trd(a + d); return (bf16x8){lo[0], lo[1], lo[2], lo[3], hi[0], hi[1], hi[2], hi[3]}; }
DI bf16x8 ldsr128(unsigned a) { return *(const LAS bf16x8*)a; }
DI bf16x8 pack2(const f32x4& a, const f32x4& b) {
  u32x4 w = {cvtpk(a[0], a[1]), cvtpk(a[2], a[3]), cvtpk(b[0], b[1]), cvtpk(b[2], b[3])};
  return __builtin_bit_cast(bf16x8, w);
}
DI float bflo(unsigned w) { return __uint_as_float(w << 16); }
DI float bfhi(unsigned w) { return __uint_as_float(w & 0xFFFF0000u); }
DI float addf(float a, float b) { float d = a + b; asm("" : "+v"(d)); return d; }
DI int max3i(int a, float b, float c) { return max(max(a, __float_as_int(b)), __float_as_int(c)); }
DI f32x2 sw16(float v) { const unsigned u = __float_as_uint(v); const auto r = __builtin_amdgcn_permlane16_swap(u, u, false, false); return f32x2{__uint_as_float(r[0]), __uint_as_float(r[1])}; }
DI f32x2 sw32(float v) { const unsigned u = __float_as_uint(v); const auto r = __builtin_amdgcn_permlane32_swap(u, u, false, false); return f32x2{__uint_as_float(r[0]), __uint_as_float(r[1])}; }
DI float xg_max(float v) { f32x2 r = sw16(v); r = sw32(fmaxf(r[0], r[1])); return fmaxf(r[0], r[1]); }
DI float xg_sum(float v) { f32x2 r = sw16(v); r = sw32(r[0] + r[1]); return r[0] + r[1]; }

#define KVPOL ""
DI void dma4(unsigned m0v, unsigned v0, unsigned v1, unsigned v2, unsigned v3, rsrc_t srd, unsigned soff) {
  asm volatile("s_mov_b32 m0, %0\n\ts_nop 0\n\t"
               "buffer_load_dwordx4 %1, %5, %6 offen lds\n\t"
               "buffer_load_dwordx4 %2, %5, %6 offen offset:1024 lds\n\t"
               "buffer_load_dwordx4 %3, %5, %6 offen offset:2048 lds\n\t"
               "buffer_load_dwordx4 %4, %5, %6 offen offset:3072 lds"
               :: "s"(m0v), "v"(v0), "v"(v1), "v"(v2), "v"(v3), "s"(srd), "s"(soff) : "m0", "memory");
}
DI void dma3k(unsigned m0v, unsigned v0, unsigned v1, unsigned v2, rsrc_t srd, unsigned soff) {
  asm volatile("s_mov_b32 m0, %0\n\ts_nop 0\n\t"
               "buffer_load_dwordx4 %1, %4, %5 offen" KVPOL " lds\n\t"
               "buffer_load_dwordx4 %2, %4, %5 offen offset:1024" KVPOL " lds\n\t"
               "buffer_load_dwordx4 %3, %4, %5 offen offset:2048" KVPOL " lds"
               :: "s"(m0v), "v"(v0), "v"(v1), "v"(v2), "s"(srd), "s"(soff) : "m0", "memory");
}
DI void touch(unsigned m0v, unsigned v, rsrc_t srd, unsigned soff) {
  asm volatile("s_mov_b32 m0, %0\n\ts_nop 0\n\tbuffer_load_dword %1, %2, %3 offen lds" :: "s"(m0v), "v"(v), "s"(srd), "s"(soff) : "m0", "memory");
}
template <int OFF> DI void bload_sc1(bf16x8& d, unsigned v, rsrc_t srd, unsigned soff) {
  asm volatile("buffer_load_dwordx4 %0, %1, %2, %3 offen offset:%4 sc1" : "=v"(d) : "v"(v), "s"(srd), "s"(soff), "i"(OFF) : "memory");
}
template <int OFF> DI void bstore_sc1(const bf16x8& d, unsigned v, rsrc_t srd, unsigned soff) {
  asm volatile("buffer_store_dwordx4 %0, %1, %2, %3 offen offset:%4 sc1\n\ts_nop 1" :: "v"(d), "v"(v), "s"(srd), "s"(soff), "i"(OFF) : "memory");
}
template <int N> DI void waitvm() { asm volatile("s_waitcnt vmcnt(%0)" :: "i"(N) : "memory"); }
template <typename T> DI void pin(T& x) { asm volatile("" : "+v"(x)); }
DI void cbar() { asm volatile("" ::: "memory"); }

#define STAMP_(i, ins) do { unsigned long long t_; asm volatile(ins " %0\n\ts_waitcnt lgkmcnt(0)" : "=s"(t_) :: "memory"); \
  asm volatile("s_mov_b32 m0, %2\n\ts_nop 4\n\tv_writelane_b32 %0, %1, m0" : "+v"(stampv) : "s"((unsigned)t_), "s"(i) : "m0"); } while (0)
#define STAMP(i) STAMP_(i, "s_memrealtime")
#define CSTAMP(i) STAMP_(i, "s_memtime")

struct Ctx {
  rsrc_t srdX, srdW, srdK, srdV, srdWo, srdWs, srdC;
  unsigned lds0, cshare;
  int wid, br, bp, h;
};

DI unsigned cread(unsigned a) { return *(const volatile LAS unsigned*)a; }
DI void cwrite(unsigned a, unsigned v) { *(volatile LAS unsigned*)a = v; }
DI void need(unsigned& have, unsigned a, unsigned n) {
  if (__builtin_expect(have < n, 0)) {
    unsigned spins = 0;
    for (;;) {
      have = rfl(cread(a));
      if (have >= n) break;
      if (++spins > (1u << 20)) { have = ~0u; break; }
      __builtin_amdgcn_s_sleep(1);
    }
  }
  cbar();
}
DI void seen(unsigned& have, unsigned pre) { have = max(have, rfl(pre)); }

struct Tag { unsigned lo, hi; };
DI bool poll_flags(rsrc_t srdWs, unsigned off, unsigned stride, int lane, int nflag, Tag tag) {
  const unsigned vo = off + (unsigned)(lane < nflag ? lane : 0) * stride;
  for (unsigned spins = 0; spins < (1u << 20); ++spins) {
    u32x2 f;
    asm volatile("buffer_load_dwordx2 %0, %1, %2, 0 offen sc1\n\ts_waitcnt vmcnt(0)" : "=v"(f) : "v"(vo), "s"(srdWs) : "memory");
    if (__all(f[0] == tag.lo && f[1] == tag.hi)) return true;
    __builtin_amdgcn_s_sleep(2);
  }
  return false;
}
DI void set_flag(rsrc_t srdWs, unsigned off, int lane, Tag tag) {
  if (lane == 0) { const u32x2 t = {tag.lo, tag.hi}; asm volatile("buffer_store_dwordx2 %0, off, %1, %2 sc1\n\ts_nop 1" :: "v"(t), "s"(srdWs), "s"(off) : "memory"); }
}
DI void arrive(const Ctx& c, int which, unsigned off, int lane) {
  waitvm<0>();
  unsigned old = 0;
  if (lane == 0) old = __hip_atomic_fetch_add((LAS unsigned*)(c.lds0 + CTL + C_CNT + 4 * which), 1u, __ATOMIC_RELAXED, __HIP_MEMORY_SCOPE_WORKGROUP);
  if (rfl(old) == 6) {
    const Tag tag{rfl(cread(c.lds0 + CTL + C_TAG)), rfl(cread(c.lds0 + CTL + C_TAG + 4))};
    set_flag(c.srdWs, off, lane, tag);
  }
}

DI void reduce_store(const bf16x8 (&pr)[8], u16* __restrict__ dst, bool odd) {
  float a[8] = {0.f, 0.f, 0.f, 0.f, 0.f, 0.f, 0.f, 0.f};
#pragma unroll
  for (int k = 0; k < 8; ++k) {
    const u32x4 w = __builtin_bit_cast(u32x4, pr[k]);
#pragma unroll
    for (int e = 0; e < 4; ++e) { a[2 * e] += bflo(w[e]); a[2 * e + 1] += bfhi(w[e]); }
  }
  const unsigned u0 = cvtpk(a[0], a[1]), u1 = cvtpk(a[2], a[3]), u2 = cvtpk(a[4], a[5]), u3 = cvtpk(a[6], a[7]);
  const u32x4 w = {odd ? u2 : u0, odd ? u3 : u1, odd ? u0 : u2, odd ? u1 : u3};
  *(u32x4*)dst = w;
}

DI void loader(const Ctx& c, u16* __restrict__ out, char* ws) {
  const int lane = lane_id();
  const unsigned lds0 = c.lds0, ctl = lds0 + CTL + lane * 4;
  const int hq = c.h & 3;
  unsigned stampv = 0;
  STAMP(0); CSTAMP(32);
  const unsigned flag0 = (unsigned)(c.br * 4 + c.bp) * 1024u;
  u32x2 ep;
  asm volatile("buffer_load_dwordx2 %0, off, %1, %2 sc1" : "=v"(ep) : "s"(c.srdWs), "s"(flag0) : "memory");
  unsigned vw[12], vx[4];
#pragma unroll
  for (int k = 0; k < 12; ++k) {
    const int id = 64 * k + lane, row = id / 24, pos = id % 24;
    vw[k] = (unsigned)(row * (HD * 2) + (pos >> 3) * 128 + (((pos & 7) ^ (((row >> 1) & 1) | (((row >> 3) & 1) << 2))) << 4) - 1024 * (k & 3));
  }
#pragma unroll
  for (int k = 0; k < 4; ++k)
    vx[k] = (unsigned)(((lane >> 3) + 8 * (k & 1) + SQ * (k >> 1)) * (DM * 2) + (((lane & 7) ^ (((lane >> 4) & 3) << 1)) << 4) - 1024 * k);
  const int rot = (c.bp + hq) & 3;
  unsigned cw = (unsigned)(rot * 5 >> 1) * 2u, cx = (unsigned)(rot * 5 >> 1);
  auto issueW = [&](int slot) {
    const unsigned m = lds0 + WOFF + slot * PIECE, so = cw * (32 * HD * 2);
#pragma unroll
    for (int q = 0; q < 3; ++q) dma4(m + 4096 * q, vw[4 * q], vw[4 * q + 1], vw[4 * q + 2], vw[4 * q + 3], c.srdW, so);
    cw = cw == 19 ? 0 : cw + 1;
  };
  auto issueX = [&](int slot) {
    const unsigned m = lds0 + XOFF + slot * XSLOT, so = cx * 128;
#pragma unroll
    for (int w = 0; w < 7; ++w) dma4(m + 4096 * w, vx[0], vx[1], vx[2], vx[3], c.srdX, so + w * (16 * DM * 2));
    cx = cx == 9 ? 0 : cx + 1;
  };
  const unsigned vtc = (unsigned)lane * 4096u;
  auto touchC = [&]() { touch(lds0 + CTL + C_JUNK, vtc, c.srdC, c.cshare); };
  issueW(0); issueX(0); issueW(1);
  cwrite(ctl + C_RDYA, 0); cwrite(ctl + C_RDYK, 0); cwrite(ctl + C_RDYG, 0); cwrite(ctl + C_RDYO, 0); cwrite(ctl + C_TAG, 0);
  asm volatile("s_waitcnt lgkmcnt(0)\n\ts_barrier" ::: "memory");
  unsigned dv = 0;
  const unsigned adone = lds0 + CTL + C_DONE + (lane < 7 ? lane : 0) * 260;
  auto gate = [&](unsigned p) {
    if (!__all(dv >= p)) {
      for (unsigned spins = 0;; ++spins) {
        dv = cread(adone);
        if (__all(dv >= p)) break;
        if (spins > (1u << 20)) { dv = ~0u; break; }
        __builtin_amdgcn_s_sleep(1);
      }
    }
    cbar();
  };
  asm volatile("s_waitcnt vmcnt(12)" : "+v"(ep) :: "memory");
  const unsigned long long tg = (((unsigned long long)rfl(ep[1]) << 32) | rfl(ep[0])) + 1ull;
  const Tag tag{(unsigned)tg, (unsigned)(tg >> 32)};
  if (lane == 0) { cwrite(lds0 + CTL + C_TAG, tag.lo); cwrite(lds0 + CTL + C_TAG + 4, tag.hi); }
  cwrite(ctl + C_RDYA, 1);
  touchC();
  issueW(2); issueX(1);
  waitvm<41>(); cwrite(ctl + C_RDYA, 2);
  issueW(3);
  waitvm<12>(); cwrite(ctl + C_RDYA, 3);
  STAMP(24);
#pragma unroll 1
  for (int t = 0; t < 16; t += 2) {
    gate(t + 1);
    issueW(t & 3); issueX((t >> 1) & 1);
    waitvm<40>(); cwrite(ctl + C_RDYA, t + 4);
    gate(t + 2);
    issueW((t + 1) & 3);
    waitvm<12>(); cwrite(ctl + C_RDYA, t + 5);
  }
  STAMP(1);
  unsigned vb[3];
#pragma unroll
  for (int k = 0; k < 3; ++k) { const int row = (64 * k + lane) / 24; vb[k] = (unsigned)(((lane & ~7) | ((lane & 7) ^ (((row >> 1) & 3) << 1))) << 4); }
  unsigned mslot = lds0;
  auto piece = [&](rsrc_t srd, int j) {
    const unsigned so = (unsigned)(j & 15) * PIECE + (unsigned)(j >> 4) * KVITEM;
#pragma unroll
    for (int q = 0; q < 4; ++q) dma3k(mslot + 3072 * q, vb[0], vb[1], vb[2], srd, so + 3072 * q);
    mslot = mslot == lds0 + (NP - 1) * PIECE ? lds0 : mslot + PIECE;
  };
  auto pgate = [&](int g) { gate(g < 8 ? g + 13 : g < 13 ? D_ATT : g + 8); };
  piece(c.srdK, 0);
  waitvm<12>(); cwrite(ctl + C_RDYA, 21);
  touchC();
  piece(c.srdK, 1); piece(c.srdV, 0); piece(c.srdK, 2);
  waitvm<36>(); cwrite(ctl + C_RDYK, 1);
#pragma unroll 1
  for (int j = 2; j < 31; ++j) {
    pgate(2 * j); piece(c.srdV, j - 1);
    waitvm<36>(); cwrite(ctl + C_RDYK, 2 * j - 2);
    pgate(2 * j + 1); piece(c.srdK, j + 1);
    waitvm<36>(); cwrite(ctl + C_RDYK, 2 * j - 1);
    if (j >= 24) touchC();
  }
  pgate(62); piece(c.srdV, 30);
  waitvm<36>(); cwrite(ctl + C_RDYK, 60);
  pgate(63); piece(c.srdV, 31);
  waitvm<36>(); cwrite(ctl + C_RDYK, 61);
  STAMP(2);
  unsigned vo[20];
#pragma unroll
  for (int k = 0; k < 20; ++k) {
    const int id = 64 * k + lane, row = id / 20, pos = id % 20;
    vo[k] = (unsigned)(row * (DM * 2) + ((pos ^ ((row >> 2) & 1)) << 4) - 1024 * (k & 3));
  }
  auto issueWo = [&](int j) {
    const unsigned so = (unsigned)(((hq * 3 + j) % 12) * (64 * DM * 2)), m = lds0 + (j % NWS) * WSLOT;
#pragma unroll
    for (int q = 0; q < 5; ++q) dma4(m + 4096 * q, vo[4 * q], vo[4 * q + 1], vo[4 * q + 2], vo[4 * q + 3], c.srdWo, so);
  };
  auto wgate = [&](int j) { gate(j < NWS ? D_ATT + 53 + (WSLOT * (j + 1) - 1) / PIECE : D_G4 + j - NWS + 1); };
  wgate(0); issueWo(0);
  waitvm<44>(); cwrite(ctl + C_RDYK, 62);
  waitvm<32>(); cwrite(ctl + C_RDYK, 63);
  waitvm<20>(); cwrite(ctl + C_RDYK, 64);
  STAMP(3);
#pragma unroll 1
  for (int j = 1; j < 7; ++j) {
    wgate(j); issueWo(j);
    waitvm<20>(); cwrite(ctl + C_RDYG, j);
  }
  waitvm<0>(); cwrite(ctl + C_RDYG, 7);
  STAMP(4);
  poll_flags(c.srdWs, flag0 + 256u + (unsigned)(c.h >> 2) * 32u, 8, lane, 4, tag);
  cwrite(ctl + C_RDYO, 1);
  STAMP(5);
#pragma unroll 1
  for (int j = 7; j < 12; ++j) {
    wgate(j); issueWo(j);
    waitvm<20>(); cwrite(ctl + C_RDYG, j);
  }
  waitvm<0>(); cwrite(ctl + C_RDYG, 13);
  STAMP(6);
  poll_flags(c.srdWs, flag0 + 512u + (unsigned)hq * 64u, 8, lane, 8, tag);
  cwrite(ctl + C_RDYO, 2);
  if (c.h == 0) set_flag(c.srdWs, flag0, lane, tag);
  STAMP(7);
  const int k8 = c.h >> 2;
  if (k8 < 7) {
    const unsigned so = (unsigned)WS_P + (unsigned)(((c.br * 4 + c.bp) * 4 + hq) * 8) * PBLK + (unsigned)k8 * 10240u + 8192u;
    bf16x8 pr[2][8];
#pragma unroll
    for (int k = 0; k < 8; ++k) { bload_sc1<0>(pr[0][k], lane * 16, c.srdWs, so + k * PBLK); bload_sc1<1024>(pr[1][k], lane * 16, c.srdWs, so + k * PBLK); }
    waitvm<0>();
#pragma unroll
    for (int p = 0; p < 2; ++p) {
#pragma unroll
      for (int k = 0; k < 8; ++k) pin(pr[p][k]);
      reduce_store(pr[p], out + (size_t)((2 * c.bp + 1) * SQ + k8 * 16 + (lane & 15)) * (2 * DM) + c.br * DM + hq * 160 + (3 + p) * 32 + 8 * (lane >> 4), (lane >> 4) & 1);
    }
  }
  waitvm<0>();
  STAMP(23); CSTAMP(33);
  if (blockIdx.x == PB) { const unsigned a0 = __builtin_amdgcn_readlane(stampv, 0), x = __builtin_amdgcn_readlane(stampv, PHI) - __builtin_amdgcn_readlane(stampv, PLO);
    while ((unsigned)__builtin_amdgcn_s_memrealtime() - a0 < 12000u + 4u * x) __builtin_amdgcn_s_sleep(1); }
}

DI void consumer(const Ctx& c, u16* __restrict__ out, char* ws) {
  const int wid = c.wid;
  int lane, l15, g, tq, tp;
  auto relane = [&]() { lane = lane_id(); l15 = lane & 15; g = lane >> 4; tq = l15 >> 2; tp = lane & 3; };
  relane();
  const unsigned lds0 = c.lds0;
  unsigned stampv = 0;
  STAMP(0); CSTAMP(32);
  const int hq = c.h & 3;
  const unsigned flag0 = (unsigned)(c.br * 4 + c.bp) * 1024u;
  const unsigned ctl = lds0 + CTL + lane * 4, adone = ctl + C_DONE + wid * 256;
  cwrite(adone, 0);
  asm volatile("s_waitcnt lgkmcnt(0)\n\ts_barrier" ::: "memory");

  bf16x8 qf[6], qn[6];
  {
    unsigned aW[2][2], aX[2];
#pragma unroll
    for (int s1 = 0; s1 < 2; ++s1)
#pragma unroll
      for (int T = 0; T < 2; ++T)
        aW[s1][T] = lds0 + WOFF + (8 * g + tq) * 384 + (((4 * s1 + tp) ^ ((tq >> 1) | ((g & 1) << 2))) << 4) + 8 * (T ^ (tp & 1));
    aX[0] = lds0 + XOFF + wid * 4096 + l15 * 128 + ((g ^ (((l15 >> 1) & 3) << 1)) << 4);
    aX[1] = aX[0] ^ 64;
    f32x4 acc[12][2];
#pragma unroll
    for (int dt = 0; dt < 12; ++dt) { acc[dt][0] = f32x4{}; acc[dt][1] = f32x4{}; }
    bf16x8 xf[2][2], a[8];
    unsigned have = 0, pre;
    auto adr = [&](int T, int K) { return aW[(K >> 1) & 1][K & 1] + (T & 3) * PIECE + (K >> 2) * 128; };
    auto rdx = [&](int T, int i) { xf[i][T & 1] = ldsr128(aX[T & 1] + ((T >> 1) & 1) * XSLOT + i * 2048); };
    need(have, ctl + C_RDYA, 1);
    rdx(0, 0); rdx(0, 1);
#pragma unroll
    for (int k = 0; k < 8; ++k) a[k] = trd2(adr(0, k), 1536);
    FENCE();
    auto step = [&](auto Tc, int t) {
      constexpr int T = decltype(Tc)::value;
#pragma unroll
      for (int k = 0; k < 12; ++k) {
        const int b = (12 * T + k) & 7;
        const unsigned ra = k < 4 ? adr(T, k + 8) : adr(T + 1, k - 4);
        if (k == 4) { cwrite(adone, t + 1); seen(have, pre); need(have, ctl + C_RDYA, t + 2); FENCE(); }
        mfma(acc[k][0], a[b], xf[0][T & 1]); FENCE();
        if (k == 0) pre = cread(ctl + C_RDYA);
        if (!(T & 1) && (k == 1 || k == 2)) rdx(T + 1, k - 1);
        if ((T & 1) && (k == 5 || k == 6)) rdx(T + 1, k - 5);
        FENCE();
        mfma(acc[k][1], a[b], xf[1][T & 1]); FENCE();
        a[b] = trd2(ra, 1536); FENCE();
      }
    };
    using I0 = std::integral_constant<int, 0>; using I1 = std::integral_constant<int, 1>; using I2 = std::integral_constant<int, 2>; using I3 = std::integral_constant<int, 3>;
#pragma unroll 1
    for (int t = 0; t < 20; t += 4) { step(I0{}, t); step(I1{}, t + 1); step(I2{}, t + 2); step(I3{}, t + 3); STAMP(24 + (t >> 2)); }
    nops<8>();
    relane();
    const bool odd = g & 1;
#pragma unroll
    for (int i = 0; i < 2; ++i)
#pragma unroll
      for (int s = 0; s < 6; ++s) {
        const f32x4 t0 = acc[2 * s][i] * QSCALE, t1 = acc[2 * s + 1][i] * QSCALE;
        const unsigned u0 = cvtpk(t0[0], t0[1]), u1 = cvtpk(t0[2], t0[3]), u2 = cvtpk(t1[0], t1[1]), u3 = cvtpk(t1[2], t1[3]);
        const u32x4 w = {odd ? u2 : u0, odd ? u3 : u1, odd ? u0 : u2, odd ? u1 : u3};
        (i ? qn[s] : qf[s]) = __builtin_bit_cast(bf16x8, w);
      }
  }
  STAMP(1);

  const int item = (c.br * 4 + c.bp) * 32 + c.h;
  const unsigned vl = (unsigned)lane * 16u;
  bf16x8 oa[2][6], ob[2][6];
  {
    unsigned bK[2], bV[4], aK[2], aV[4];
    bK[0] = l15 * 384 + ((g ^ (((l15 >> 1) & 3) << 1)) << 4);
    bK[1] = bK[0] ^ 64;
#pragma unroll
    for (int d4 = 0; d4 < 4; ++d4) bV[d4] = (4 * g + tq) * 384 + (((2 * d4 + (tp >> 1)) ^ (((2 * g + (tq >> 1)) & 3) << 1)) << 4) + 8 * (tp & 1);
    unsigned have = 0, pre = 0, slot = lds0, gp = 0;
    auto next = [&]() {
      seen(have, pre);
      need(have, ctl + C_RDYK, ++gp);
      const unsigned s = slot;
      slot = slot == lds0 + (NP - 1) * PIECE ? lds0 : slot + PIECE;
      return s;
    };
    auto nextK = [&]() { const unsigned s = next(); aK[0] = bK[0] + s; aK[1] = bK[1] + s; };
    auto setV = [&](unsigned s) {
#pragma unroll
      for (int d4 = 0; d4 < 4; ++d4) aV[d4] = bV[d4] + s;
    };
    f32x4 o[12], s[2], negm;
    float lsum, sc;
    int mx;
    u32x4 pf;
    bf16x8 w[8];
    bool resc;
    auto rdK = [&](int n) { w[n & 7] = ldsr128(aK[(n % 6) & 1] + ((n % 6) >> 1) * 128 + (n / 6) * 6144); };
    auto vadr = [&](int dt) { return aV[dt & 3] + (dt >> 2) * 128; };
    auto rda = [&](int n) { if (n < 12) rdK(n); else if (n < 24) w[n & 7] = trd2(vadr(n - 12), 6144); else rdK(n - 24); };
    auto mfa = [&](int n) {
      if (n >= 12) mfma(o[n - 12], w[n & 7], __builtin_bit_cast(bf16x8, pf));
      else if (n % 6) mfma(s[n / 6], w[n & 7], qf[n % 6]);
      else mfmac(s[n / 6], w[n & 7], qf[0], negm);
    };
    auto ex = [&](int e) { s[e >> 2][e & 3] = __builtin_amdgcn_exp2f(s[e >> 2][e & 3]); };
    auto ad = [&](int e) { lsum = addf(lsum, s[e >> 2][e & 3]); };
    auto cv = [&]() { pf[0] = cvtpk(s[0][0], s[0][1]); pf[1] = cvtpk(s[0][2], s[0][3]); pf[2] = cvtpk(s[1][0], s[1][1]); pf[3] = cvtpk(s[1][2], s[1][3]); pin(pf); };
    auto mxs = [&](int kt, int hf) { mx = max3i((kt | hf) ? mx : 0, s[kt][2 * hf], s[kt][2 * hf + 1]); };
    auto lanemax = [&]() { return fmaxf(fmaxf(fmaxf(s[0][0], s[0][1]), fmaxf(s[0][2], s[0][3])), fmaxf(fmaxf(s[1][0], s[1][1]), fmaxf(s[1][2], s[1][3]))); };
    auto scale_o = [&]() {
      if (resc) {
#pragma unroll
        for (int dt = 0; dt < 12; ++dt) o[dt] *= sc;
      }
    };
#define SLOT(n, A) do { mfa(n); FENCE(); rda(n + 8); A; FENCE(); } while (0)
    nextK();
#pragma unroll
    for (int n = 0; n < 8; ++n) rdK(n);
#pragma unroll 1
    for (int it = 0; it < 2; ++it) {
      {
        FENCE();
#pragma unroll
        for (int n = 0; n < 12; ++n) {
          if (n % 6) mfma(s[n / 6], w[n & 7], qf[n % 6]); else mfma0(s[n / 6], w[n & 7], qf[0]);
          FENCE();
          if (n < 4) rdK(n + 8);
          o[n] = f32x4{};
          FENCE();
        }
        cwrite(adone, D_ATT + gp);
        nextK();
#pragma unroll
        for (int n = 0; n < 8; ++n) rdK(n);
        nops<8>();
        const float m = xg_max(lanemax());
        negm = (f32x4){-m, -m, -m, -m};
        s[0] -= m; s[1] -= m;
        lsum = 0.f; sc = 1.f; resc = false;
#pragma unroll
        for (int e = 0; e < 8; ++e) ex(e);
#pragma unroll
        for (int e = 0; e < 8; ++e) ad(e);
        cv();
        nops<2>();
      }
#pragma unroll 1
      for (int st = 1; st < 16; ++st) {
        SLOT(0, pre = cread(ctl + C_RDYK));
        SLOT(1, ); SLOT(2, ); SLOT(3, );
        setV(next()); FENCE();
        SLOT(4, ); SLOT(5, );
        scale_o(); FENCE();
        SLOT(6, ); SLOT(7, );
        SLOT(8, mxs(0, 0)); SLOT(9, mxs(0, 1));
        SLOT(10, ); SLOT(11, );
        SLOT(12, pre = cread(ctl + C_RDYK));
        SLOT(13, );
        SLOT(14, mxs(1, 0); mxs(1, 1));
        resc = __any(mx > __builtin_bit_cast(int, RESC_THR));
        if (resc) {
          const float d = fmaxf(xg_max(lanemax()), 0.f);
          s[0] -= d; s[1] -= d; negm -= d;
          sc = __builtin_amdgcn_exp2f(-d);
          lsum *= sc;
        }
        FENCE();
        SLOT(15, ex(0));
        cwrite(adone, D_ATT + gp);
        nextK(); FENCE();
        SLOT(16, ex(1); ad(0)); SLOT(17, ex(2); ad(1)); SLOT(18, ex(3); ad(2)); SLOT(19, ex(4); ad(3));
        SLOT(20, ex(5); ad(4)); SLOT(21, ex(6); ad(5)); SLOT(22, ex(7); ad(6)); SLOT(23, ad(7));
        cv(); FENCE();
        if (st == 1 || st == 8) STAMP(2 + 4 * it + (st >> 3));
      }
      STAMP(4 + 4 * it);
      {
        if (it == 0) setV(next()); else setV(aK[0] - bK[0]);
        bf16x8 v[12];
#pragma unroll
        for (int dt = 0; dt < 12; ++dt) v[dt] = trd2(vadr(dt), 6144);
        if (it) cwrite(adone, D_G4);
        nops<4>();
        scale_o();
        nops<2>();
#pragma unroll
        for (int dt = 0; dt < 12; ++dt) mfma(o[dt], v[dt], __builtin_bit_cast(bf16x8, pf));
        const float inv = 1.f / xg_sum(lsum);
        nops<8>();
        const unsigned so = (unsigned)WS_O + (unsigned)item * OBLK + (unsigned)wid * 12288u + (unsigned)it * 6144u;
#pragma unroll
        for (int q = 0; q < 6; ++q) oa[0][q] = oa[1][q];
#pragma unroll
        for (int q = 0; q < 6; ++q) oa[1][q] = pack2(o[2 * q] * inv, o[2 * q + 1] * inv);
        bstore_sc1<0>(oa[1][0], vl, c.srdWs, so); bstore_sc1<1024>(oa[1][1], vl, c.srdWs, so); bstore_sc1<2048>(oa[1][2], vl, c.srdWs, so);
        bstore_sc1<0>(oa[1][3], vl, c.srdWs, so + 3072); bstore_sc1<1024>(oa[1][4], vl, c.srdWs, so + 3072); bstore_sc1<2048>(oa[1][5], vl, c.srdWs, so + 3072);
#pragma unroll
        for (int q = 0; q < 6; ++q) qf[q] = qn[q];
      }
      STAMP(5 + 4 * it);
    }
  }

  relane();
  auto loadO2 = [&](bf16x8 (&d)[2][6], int rel, int s) {
    const unsigned so = (unsigned)WS_O + (unsigned)((item & ~3) | ((hq + rel) & 3)) * OBLK + (unsigned)wid * 12288u + s * 2048;
#pragma unroll
    for (int i = 0; i < 2; ++i) { bload_sc1<0>(d[i][2 * s], vl, c.srdWs, so + i * 6144); bload_sc1<1024>(d[i][2 * s + 1], vl, c.srdWs, so + i * 6144); }
    FENCE();
  };
  auto pinO = [&](bf16x8 (&d)[2][6], int s) { pin(d[0][2 * s]); pin(d[0][2 * s + 1]); pin(d[1][2 * s]); pin(d[1][2 * s + 1]); };
  unsigned aO[3][2];
  aO[0][0] = lds0 + (4 * g + tq) * 320 + ((tp ^ (g & 1)) << 4) + 8 * (tp & 1);
  aO[0][1] = aO[0][0] ^ 8;
#pragma unroll
  for (int q = 1; q < 3; ++q) { aO[q][0] = aO[0][0] + 3 * q * WSLOT; aO[q][1] = aO[0][1] + 3 * q * WSLOT; }
  f32x4 y[5][2][2];
  constexpr int WN = 8;
  bf16x8 wf[WN];
  unsigned have = 0, pre = 0, haveO = 0;
  auto rd = [&](int n) {
    const int j = n / 20, k = n % 20, sl = j % NWS;
    wf[n % WN] = trd2(aO[sl / 3][k & 1] + (sl % 3) * WSLOT + (k / 10) * 10240 + ((k % 10) >> 1) * 64, 5120);
  };
  auto stage = [&](int j, const bf16x8 (&of)[2][6]) {
#pragma unroll
    for (int k = 0; k < 20; ++k) {
      const int n = 20 * j + k;
      if (k == 20 - WN && j < 5) cwrite(adone, D_G4 + j + 1);
      if (k == 20 - WN && j < 11) { seen(have, pre); need(have, ctl + C_RDYG, j + 2); FENCE(); }
#pragma unroll
      for (int i = 0; i < 2; ++i) {
        if (n < 10) mfma0(y[(k % 10) >> 1][k & 1][i], wf[n % WN], of[i][2 * (j % 3) + k / 10]);
        else mfma(y[(k % 10) >> 1][k & 1][i], wf[n % WN], of[i][2 * (j % 3) + k / 10]);
        FENCE();
        if (i == 0 && k == 16 - WN) pre = cread(ctl + C_RDYG);
        FENCE();
      }
      if (n + WN < 240) rd(n + WN);
      FENCE();
    }
  };
  need(have, ctl + C_RDYG, 1);
#pragma unroll
  for (int n = 0; n < WN; ++n) rd(n);
  FENCE();
  STAMP(10);
  stage(0, oa);
  arrive(c, 0, flag0 + 256u + (unsigned)c.h * 8u, lane);
  STAMP(11);
  stage(1, oa);
  STAMP(12);
  need(haveO, ctl + C_RDYO, 1);
  STAMP(13);
  loadO2(ob, 1, 0); loadO2(ob, 1, 1); loadO2(ob, 1, 2); loadO2(oa, 2, 0); loadO2(oa, 2, 1);
  stage(2, oa); loadO2(oa, 2, 2);
  STAMP(14);
  waitvm<20>(); pinO(ob, 0); FENCE(); STAMP(15); stage(3, ob); loadO2(ob, 3, 0);
  waitvm<20>(); pinO(ob, 1); FENCE(); stage(4, ob); loadO2(ob, 3, 1);
  waitvm<20>(); pinO(ob, 2); FENCE(); stage(5, ob); loadO2(ob, 3, 2);
  STAMP(16);
  waitvm<12>(); pinO(oa, 0); pinO(oa, 1); pinO(oa, 2); FENCE(); stage(6, oa); stage(7, oa); stage(8, oa);
  STAMP(17);
  waitvm<0>(); pinO(ob, 0); pinO(ob, 1); pinO(ob, 2); FENCE(); stage(9, ob); stage(10, ob); stage(11, ob);
  nops<8>();
  STAMP(20);

  const int grp = (c.br * 4 + c.bp) * 4 + hq, k8 = c.h >> 2;
  {
    const unsigned so = (unsigned)WS_P + (unsigned)(grp * 8 + k8) * PBLK + (unsigned)wid * 10240u;
#pragma unroll
    for (int i = 0; i < 2; ++i) {
      bstore_sc1<0>(pack2(y[0][0][i], y[0][1][i]), vl, c.srdWs, so + i * 5120); bstore_sc1<1024>(pack2(y[1][0][i], y[1][1][i]), vl, c.srdWs, so + i * 5120);
      bstore_sc1<2048>(pack2(y[2][0][i], y[2][1][i]), vl, c.srdWs, so + i * 5120); bstore_sc1<3072>(pack2(y[3][0][i], y[3][1][i]), vl, c.srdWs, so + i * 5120);
      bstore_sc1<0>(pack2(y[4][0][i], y[4][1][i]), vl, c.srdWs, so + i * 5120 + 4096);
    }
  }
  relane();
  arrive(c, 1, flag0 + 512u + (unsigned)hq * 64u + (unsigned)k8 * 8u, lane);
  STAMP(21);
  need(haveO, ctl + C_RDYO, 2);
  STAMP(22);
  {
    bf16x8 pr[8];
    const unsigned so = (unsigned)WS_P + (unsigned)(grp * 8) * PBLK + (unsigned)wid * 10240u + (unsigned)k8 * 1024u;
#pragma unroll
    for (int k = 0; k < 8; ++k) bload_sc1<0>(pr[k], vl, c.srdWs, so + k * PBLK);
    waitvm<0>();
#pragma unroll
    for (int k = 0; k < 8; ++k) pin(pr[k]);
    const int i = k8 >= 5, cp = k8 - 5 * i;
    reduce_store(pr, out + (size_t)((2 * c.bp + i) * SQ + wid * 16 + l15) * (2 * DM) + c.br * DM + hq * 160 + cp * 32 + 8 * g, g & 1);
  }
  waitvm<0>();
  STAMP(23); CSTAMP(33);
  if (stampv == 0x12345) ws[0] = 1;
}

__global__ __launch_bounds__(512, 2) void fused_kernel(const u16* __restrict__ xA, const u16* __restrict__ kA, const u16* __restrict__ vA, const u16* __restrict__ wqA, const u16* __restrict__ woA,
                                                       const u16* __restrict__ xB, const u16* __restrict__ kB, const u16* __restrict__ vB, const u16* __restrict__ wqB, const u16* __restrict__ woB,
                                                       u16* __restrict__ out, char* ws) {
  extern __shared__ __attribute__((aligned(16))) char lds[];
  Ctx c;
  c.wid = rfl(threadIdx.x >> 6);
  const int xcd = blockIdx.x & 7, idx = blockIdx.x >> 3;
  c.h = 4 * xcd + (idx & 3); c.bp = (idx >> 2) & 3; c.br = idx >> 4;
  const u16* x = c.br ? xB : xA; const u16* kc = c.br ? kB : kA; const u16* vc = c.br ? vB : vA; const u16* wq = c.br ? wqB : wqA; const u16* wo = c.br ? woB : woA;
  const size_t kvoff = (size_t)(2 * c.bp * NH + c.h) * SK * DH;
  c.srdX = __builtin_amdgcn_make_buffer_rsrc((void*)(x + (size_t)c.bp * 2 * SQ * DM), (short)0, 2 * SQ * DM * 2, 0x00020000);
  c.srdW = __builtin_amdgcn_make_buffer_rsrc((void*)(wq + c.h * DH), (short)0, (DM * HD - c.h * DH) * 2, 0x00020000);
  c.srdK = __builtin_amdgcn_make_buffer_rsrc((void*)(kc + kvoff), (short)0, KVITEM + SK * DH * 2, 0x00020000);
  c.srdV = __builtin_amdgcn_make_buffer_rsrc((void*)(vc + kvoff), (short)0, KVITEM + SK * DH * 2, 0x00020000);
  c.srdWo = __builtin_amdgcn_make_buffer_rsrc((void*)(wo + (size_t)(c.h >> 2) * 4 * DH * DM + (c.h & 3) * 160), (short)0, (4 * DH * DM - (c.h & 3) * 160) * 2, 0x00020000);
  c.srdWs = __builtin_amdgcn_make_buffer_rsrc((void*)ws, (short)0, (int)WS_END, 0x00020000);
  c.lds0 = (unsigned)(uintptr_t)lds;
  {
    unsigned long long pc; unsigned clen;
    asm volatile("s_getpc_b64 %0\n.Lcode_here:\n\ts_mov_b32 %1, .Lcode_end-.Lcode_here" : "=s"(pc), "=s"(clen));
    c.srdC = __builtin_amdgcn_make_buffer_rsrc((void*)pc, (short)0, (int)clen, 0x00020000);
    c.cshare = (unsigned)(idx & 31) * 128u;
  }
  if (c.wid == 7) { __builtin_amdgcn_s_setprio(3); loader(c, out, ws); } else consumer(c, out, ws);
  asm volatile(".Lcode_end:");
}

extern "C" void kernel_launch(void* const* d_in, const int* in_sizes, int n_in, void* d_out, int out_size, void* d_ws, size_t ws_size, hipStream_t stream) {
  (void)in_sizes; (void)n_in; (void)out_size; (void)ws_size;
  (void)hipFuncSetAttribute((const void*)fused_kernel, hipFuncAttributeMaxDynamicSharedMemorySize, LDS_BYTES);
  fused_kernel<<<256, 512, LDS_BYTES, stream>>>((const u16*)d_in[0], (const u16*)d_in[1], (const u16*)d_in[2], (const u16*)d_in[3], (const u16*)d_in[4],
                                                (const u16*)d_in[5], (const u16*)d_in[6], (const u16*)d_in[7], (const u16*)d_in[8], (const u16*)d_in[9],
                                                (u16*)d_out, (char*)d_ws);
}
```

```cpp
#include <hip/hip_runtime.h>
#include <hip/hip_bf16.h>
#include <stdint.h>
#include <type_traits>
#pragma clang diagnostic ignored "-Winline-asm"

typedef unsigned short u16;
typedef short bf16x8 __attribute__((ext_vector_type(8)));
typedef short s16x4 __attribute__((ext_vector_type(4)));
typedef float f32x4 __attribute__((ext_vector_type(4)));
typedef float f32x2 __attribute__((ext_vector_type(2)));
typedef __bf16 bf16x2_t __attribute__((ext_vector_type(2)));
typedef unsigned u32x4 __attribute__((ext_vector_type(4)));
typedef unsigned u32x2 __attribute__((ext_vector_type(2)));
typedef __amdgpu_buffer_rsrc_t rsrc_t;
#define LAS __attribute__((address_space(3)))
#define DI __device__ __forceinline__

constexpr int SQ = 112, DM = 640, NH = 32, SK = 512, DH = 192, HD = NH * DH;
constexpr int PIECE = 12288, NP = 13;
constexpr int XB = 28672, GSLOT = XB + 2 * PIECE;
constexpr int WSLOT = 20480, NWS = 7;
constexpr int CTL = NP * PIECE;
constexpr int C_DONE = 0, C_RDY = 1792, C_RDYO = 2048, C_TAG = 2304, C_CNT = 2320, C_JUNK = 2560;
constexpr int LDS_BYTES = 163840;
constexpr unsigned FLAG_BYTES = 8192;
constexpr unsigned OBLK = 7 * 12 * 1024;
constexpr unsigned PBLK = 7 * 10 * 1024;
constexpr size_t WS_O = FLAG_BYTES;
constexpr size_t WS_P = WS_O + (size_t)256 * OBLK;
constexpr size_t WS_END = WS_P + (size_t)256 * PBLK;
constexpr float QSCALE = 0.07216878364870322f * 1.4426950408889634f;
constexpr float RESC_THR = 8.0f;
constexpr unsigned KVITEM = NH * SK * DH * 2;
constexpr int D_ATT = 20, D_G4 = 84;
#define NPRE 5
#define SPF 8
#define PFL 40

DI void mfma(f32x4& d, const bf16x8& a, const bf16x8& b) { asm("v_mfma_f32_16x16x32_bf16 %0, %1, %2, %0" : "+v"(d) : "v"(a), "v"(b)); }
DI void mfmac(f32x4& d, const bf16x8& a, const bf16x8& b, const f32x4& c) { asm("v_mfma_f32_16x16x32_bf16 %0, %1, %2, %3" : "=v"(d) : "v"(a), "v"(b), "v"(c)); }
DI void mfma0(f32x4& d, const bf16x8& a, const bf16x8& b) { asm("v_mfma_f32_16x16x32_bf16 %0, %1, %2, 0" : "=v"(d) : "v"(a), "v"(b)); }
#define FENCE() __builtin_amdgcn_sched_barrier(0)
template <int N> DI void nops() { FENCE(); asm volatile("s_nop %0" :: "i"(N - 1)); FENCE(); }
DI int lane_id() { int l; asm volatile("v_mbcnt_lo_u32_b32 %0, -1, 0\n\tv_mbcnt_hi_u32_b32 %0, -1, %0" : "=v"(l)); return l; }
DI unsigned rfl(unsigned x) { return __builtin_amdgcn_readfirstlane(x); }
DI unsigned cvtpk(float lo, float hi) { f32x2 v = {lo, hi}; bf16x2_t b = __builtin_convertvector(v, bf16x2_t); return __builtin_bit_cast(unsigned, b); }
DI s16x4 trd(unsigned a) { return __builtin_bit_cast(s16x4, __builtin_amdgcn_ds_read_tr16_b64_v4i16((LAS s16x4*)a)); }
DI void trlo(bf16x8& d, unsigned a) { const s16x4 t = trd(a); d[0] = t[0]; d[1] = t[1]; d[2] = t[2]; d[3] = t[3]; }
DI void trhi(bf16x8& d, unsigned a) { const s16x4 t = trd(a); d[4] = t[0]; d[5] = t[1]; d[6] = t[2]; d[7] = t[3]; }
DI bf16x8 trd2(unsigned a, unsigned d) { s16x4 lo = trd(a), hi = trd(a + d); return (bf16x8){lo[0], lo[1], lo[2], lo[3], hi[0], hi[1], hi[2], hi[3]}; }
DI bf16x8 ldsr128(unsigned a) { return *(const LAS bf16x8*)a; }
DI bf16x8 pack2(const f32x4& a, const f32x4& b) {
  u32x4 w = {cvtpk(a[0], a[1]), cvtpk(a[2], a[3]), cvtpk(b[0], b[1]), cvtpk(b[2], b[3])};
  return __builtin_bit_cast(bf16x8, w);
}
DI float bflo(unsigned w) { return __uint_as_float(w << 16); }
DI float bfhi(unsigned w) { return __uint_as_float(w & 0xFFFF0000u); }
DI float addf(float a, float b) { float d = a + b; asm("" : "+v"(d)); return d; }
DI int max3i(int a, float b, float c) { return max(max(a, __float_as_int(b)), __float_as_int(c)); }
DI f32x2 sw16(float v) { const unsigned u = __float_as_uint(v); const auto r = __builtin_amdgcn_permlane16_swap(u, u, false, false); return f32x2{__uint_as_float(r[0]), __uint_as_float(r[1])}; }
DI f32x2 sw32(float v) { const unsigned u = __float_as_uint(v); const auto r = __builtin_amdgcn_permlane32_swap(u, u, false, false); return f32x2{__uint_as_float(r[0]), __uint_as_float(r[1])}; }
DI float xg_max(float v) { f32x2 r = sw16(v); r = sw32(fmaxf(r[0], r[1])); return fmaxf(r[0], r[1]); }
DI float xg_sum(float v) { f32x2 r = sw16(v); r = sw32(r[0] + r[1]); return r[0] + r[1]; }

#define KVPOL " nt"
DI void dma4(unsigned m0v, unsigned v0, unsigned v1, unsigned v2, unsigned v3, rsrc_t srd, unsigned soff) {
  asm volatile("s_mov_b32 m0, %0\n\ts_nop 0\n\t"
               "buffer_load_dwordx4 %1, %5, %6 offen lds\n\t"
               "buffer_load_dwordx4 %2, %5, %6 offen offset:1024 lds\n\t"
               "buffer_load_dwordx4 %3, %5, %6 offen offset:2048 lds\n\t"
               "buffer_load_dwordx4 %4, %5, %6 offen offset:3072 lds"
               :: "s"(m0v), "v"(v0), "v"(v1), "v"(v2), "v"(v3), "s"(srd), "s"(soff) : "m0", "memory");
}
DI void dma3k(unsigned m0v, unsigned v0, unsigned v1, unsigned v2, rsrc_t srd, unsigned soff) {
  asm volatile("s_mov_b32 m0, %0\n\ts_nop 0\n\t"
               "buffer_load_dwordx4 %1, %4, %5 offen" KVPOL " lds\n\t"
               "buffer_load_dwordx4 %2, %4, %5 offen offset:1024" KVPOL " lds\n\t"
               "buffer_load_dwordx4 %3, %4, %5 offen offset:2048" KVPOL " lds"
               :: "s"(m0v), "v"(v0), "v"(v1), "v"(v2), "s"(srd), "s"(soff) : "m0", "memory");
}
DI void dma3(unsigned m0v, unsigned v0, unsigned v1, unsigned v2, rsrc_t srd, unsigned soff) {
  asm volatile("s_mov_b32 m0, %0\n\ts_nop 0\n\t"
               "buffer_load_dwordx4 %1, %4, %5 offen lds\n\t"
               "buffer_load_dwordx4 %2, %4, %5 offen offset:1024 lds\n\t"
               "buffer_load_dwordx4 %3, %4, %5 offen offset:2048 lds"
               :: "s"(m0v), "v"(v0), "v"(v1), "v"(v2), "s"(srd), "s"(soff) : "m0", "memory");
}
DI void touch(unsigned m0v, unsigned v, rsrc_t srd, unsigned soff) {
  asm volatile("s_mov_b32 m0, %0\n\ts_nop 0\n\tbuffer_load_dword %1, %2, %3 offen lds" :: "s"(m0v), "v"(v), "s"(srd), "s"(soff) : "m0", "memory");
}
template <int OFF> DI void bload_sc1(bf16x8& d, unsigned v, rsrc_t srd, unsigned soff) {
  asm volatile("buffer_load_dwordx4 %0, %1, %2, %3 offen offset:%4 sc1" : "=v"(d) : "v"(v), "s"(srd), "s"(soff), "i"(OFF) : "memory");
}
template <int OFF> DI void bload_sc1_into(bf16x8& d, unsigned v, rsrc_t srd, unsigned soff) {
  asm volatile("buffer_load_dwordx4 %0, %1, %2, %3 offen offset:%4 sc1" : "+v"(d) : "v"(v), "s"(srd), "s"(soff), "i"(OFF) : "memory");
}
template <int OFF> DI void bstore_sc1(const bf16x8& d, unsigned v, rsrc_t srd, unsigned soff) {
  asm volatile("buffer_store_dwordx4 %0, %1, %2, %3 offen offset:%4 sc1\n\ts_nop 1" :: "v"(d), "v"(v), "s"(srd), "s"(soff), "i"(OFF) : "memory");
}
template <int N> DI void waitvm() { asm volatile("s_waitcnt vmcnt(%0)" :: "i"(N) : "memory"); }
template <typename T> DI void pin(T& x) { asm volatile("" : "+v"(x)); }
DI void cbar() { asm volatile("" ::: "memory"); }

#define STAMP(i)
#define CSTAMP(i)
#define PUT(i, v)
#define WACC_BEGIN
#define WACC_END(w)

struct Ctx {
  rsrc_t srdX, srdW, srdK, srdV, srdWo, srdWs, srdC;
  u16* out;
  unsigned lds0, cshare;
  unsigned vx[4], vw[2][3], vb[3];
  int wid, br, bp, h;
};

DI unsigned cread(unsigned a) { return *(const volatile LAS unsigned*)a; }
DI void cwrite(unsigned a, unsigned v) { *(volatile LAS unsigned*)a = v; }
DI void need(unsigned& have, unsigned a, unsigned n, unsigned& wacc) {
  if (__builtin_expect(have < n, 0)) {
    WACC_BEGIN;
#pragma unroll 1
    for (unsigned spins = 0;; ++spins) {
      have = rfl(cread(a));
      if (have >= n) break;
      if (spins > (1u << 20)) { have = ~0u; break; }
      __builtin_amdgcn_s_sleep(1);
    }
    WACC_END(wacc);
  }
  cbar();
}
DI void seen(unsigned& have, unsigned pre) { have = max(have, rfl(pre)); }

struct Tag { unsigned lo, hi; };
DI bool match(const u32x2& f, Tag tag) { return __all(f[0] == tag.lo && f[1] == tag.hi); }
DI void poll_flags(rsrc_t srdWs, unsigned vo, Tag tag) {
#pragma unroll 1
  for (unsigned spins = 0; spins < (1u << 20); ++spins) {
    u32x2 f;
    asm volatile("buffer_load_dwordx2 %0, %1, %2, 0 offen sc1\n\ts_waitcnt vmcnt(0)" : "=v"(f) : "v"(vo), "s"(srdWs) : "memory");
    if (match(f, tag)) break;
    __builtin_amdgcn_s_sleep(2);
  }
}
DI void set_flag(rsrc_t srdWs, unsigned off, int lane, Tag tag) {
  if (lane == 0) { const u32x2 t = {tag.lo, tag.hi}; asm volatile("buffer_store_dwordx2 %0, off, %1, %2 sc1\n\ts_nop 1" :: "v"(t), "s"(srdWs), "s"(off) : "memory"); }
}
DI void arrive(const Ctx& c, int which, unsigned off) {
  waitvm<0>();
  const int lane = lane_id();
  unsigned old = 0;
  if (lane == 0) old = __hip_atomic_fetch_add((LAS unsigned*)(c.lds0 + CTL + C_CNT + 4 * which), 1u, __ATOMIC_RELAXED, __HIP_MEMORY_SCOPE_WORKGROUP);
  if (rfl(old) == 6) {
    const Tag tag{rfl(cread(c.lds0 + CTL + C_TAG)), rfl(cread(c.lds0 + CTL + C_TAG + 4))};
    set_flag(c.srdWs, off, lane, tag);
  }
}

DI void reduce_store(const bf16x8 (&pr)[8], u16* __restrict__ dst, bool odd) {
  float a[8] = {0.f, 0.f, 0.f, 0.f, 0.f, 0.f, 0.f, 0.f};
#pragma unroll
  for (int k = 0; k < 8; ++k) {
    const u32x4 w = __builtin_bit_cast(u32x4, pr[k]);
#pragma unroll
    for (int e = 0; e < 4; ++e) { a[2 * e] += bflo(w[e]); a[2 * e + 1] += bfhi(w[e]); }
  }
  const unsigned u0 = cvtpk(a[0], a[1]), u1 = cvtpk(a[2], a[3]), u2 = cvtpk(a[4], a[5]), u3 = cvtpk(a[6], a[7]);
  const u32x4 w = {odd ? u2 : u0, odd ? u3 : u1, odd ? u0 : u2, odd ? u1 : u3};
  *(u32x4*)dst = w;
}
constexpr unsigned F_OA = 256, F_OB = 512, F_P = 768;

DI void loader(const Ctx& c, char* ws) {
  const int lane = lane_id();
  const unsigned lds0 = c.lds0, ardy = lds0 + CTL + C_RDY + lane * 4;
  const int hq = c.h & 3;
  STAMP(0); CSTAMP(32);
  int dv = 0;
  unsigned wacc = 1;
  const unsigned adone = lds0 + CTL + C_DONE + (lane < 7 ? lane : 0) * 260;
  auto gate = [&](int p) {
    if (!__all(dv >= p)) {
      WACC_BEGIN;
#pragma unroll 1
      for (unsigned spins = 0;; ++spins) {
        dv = cread(adone);
        if (__all(dv >= p)) break;
        if (spins > (1u << 20)) { dv = 0x7FFFFFFF; break; }
        __builtin_amdgcn_s_sleep(1);
      }
      WACC_END(wacc);
    }
    cbar();
  };
  const unsigned (&vx)[4] = c.vx; const unsigned (&vw)[2][3] = c.vw; const unsigned (&vb)[3] = c.vb;
  {
    const int rot = (c.bp + hq) & 1;
    auto issueW = [&](unsigned m, unsigned so) {
#pragma unroll
      for (int q = 0; q < 4; ++q) dma3(m + 3072 * q, vw[q & 1][0], vw[q & 1][1], vw[q & 1][2], c.srdW, so + q * (8 * HD * 2));
    };
    unsigned m = lds0 + 2 * GSLOT;
#pragma unroll 1
    for (int d = 0; d < 10; ++d) {
      const unsigned dd = d ^ rot;
      gate(2 * d - 5);
#pragma unroll
      for (int w = 0; w < 7; ++w) dma4(m + 4096 * w, vx[0], vx[1], vx[2], vx[3], c.srdX, dd * 128 + w * (16 * DM * 2));
      waitvm<40>(); cwrite(ardy, max(2 * d - 1, 0));
      issueW(m + XB, dd * (64 * HD * 2));
      waitvm<40>(); cwrite(ardy, 2 * d);
      gate(2 * d - 4);
      issueW(m + XB + PIECE, dd * (64 * HD * 2) + 32 * HD * 2);
      m = m == lds0 + 2 * GSLOT ? lds0 : m + GSLOT;
      if (d < 4) STAMP(24 + d);
    }
  }
  STAMP(1); PUT(40, wacc); CSTAMP(34);
  const unsigned flag0 = (unsigned)(c.br * 4 + c.bp) * 1024u;
  const unsigned vtc = (unsigned)lane * 4096u;
  unsigned mslot = lds0;
  auto pieces = [&](int p0, int p1, bool tc) {
#pragma unroll 1
    for (int p = p0; p < p1; ++p) {
      const int q = p & 31;
      const bool isv = ((q & 1) == 0 && q) || q == 31;
      const unsigned so = (unsigned)((isv ? q - 1 : q + 1) >> 1) * PIECE + (unsigned)(p >> 5) * KVITEM;
      const rsrc_t srd = isv ? c.srdV : c.srdK;
      gate(p < NP ? 15 + (int)((0x5444432221000ull >> (4 * p)) & 15) : p + 8);
#pragma unroll
      for (int k = 0; k < 4; ++k) dma3k(mslot + 3072 * k, vb[0], vb[1], vb[2], srd, so + 3072 * k);
      mslot = mslot == lds0 + (NP - 1) * PIECE ? lds0 : mslot + PIECE;
      waitvm<36>(); cwrite(ardy, 18 + p);
      if (tc && (p & 1) && p < 60) touch(lds0 + CTL + C_JUNK, vtc, c.srdC, c.cshare);
    }
  };
  u32x2 f;
  asm volatile("buffer_load_dwordx2 %0, off, %1, %2 sc1" : "=v"(f) : "s"(c.srdWs), "s"(flag0) : "memory");
  pieces(0, 4, false);
  pin(f);
  const unsigned long long tg = (((unsigned long long)rfl(f[1]) << 32) | rfl(f[0])) + 1ull;
  const Tag tag{(unsigned)tg, (unsigned)(tg >> 32)};
  if (lane == 0) { cwrite(lds0 + CTL + C_TAG, tag.lo); cwrite(lds0 + CTL + C_TAG + 4, tag.hi); }
  pieces(4, PFL, false);
  const unsigned vsib = flag0 + (unsigned)(c.h >> 2) * 32u + (unsigned)(lane & 3) * 8u;
  asm volatile("buffer_load_dwordx2 %0, %1, %2, 0 offen offset:%3 sc1" : "=v"(f) : "v"(vsib), "s"(c.srdWs), "i"(F_OA) : "memory");
  pieces(PFL, PFL + 4, false);
  pin(f);
  if (!match(f, tag)) poll_flags(c.srdWs, vsib + F_OA, tag);
  cwrite(ardy + (C_RDYO - C_RDY), 1);
  STAMP(8);
  pieces(PFL + 4, 64, true);
  STAMP(2); PUT(41, wacc);
  unsigned vo[5];
#pragma unroll
  for (int k = 0; k < 5; ++k) {
    const int row = ((16 * k + (lane >> 2)) * 205) >> 10;
    vo[k] = (unsigned)(row * (DM * 2 - 320) + ((lane ^ ((row >> 2) & 1)) << 4) + (k == 4 ? 4096 : 0));
  }
  unsigned cwo = hq * 3;
  mslot = lds0;
  auto issueWo = [&](int j) {
    gate(j < NWS ? D_ATT + 53 + (int)((0xB986431u >> (4 * j)) & 15) : D_G4 + j - NWS + 1);
#pragma unroll
    for (int q = 0; q < 4; ++q) {
      const unsigned so = cwo * (64 * DM * 2) + q * (16 * DM * 2);
      dma4(mslot + 5120 * q, vo[0], vo[1], vo[2], vo[3], c.srdWo, so);
      asm volatile("s_mov_b32 m0, %0\n\ts_nop 0\n\tbuffer_load_dwordx4 %1, %2, %3 offen lds" :: "s"(mslot + 5120 * q + 4096), "v"(vo[4]), "s"(c.srdWo), "s"(so) : "m0", "memory");
    }
    cwo = cwo == 11 ? 0 : cwo + 1;
    mslot = mslot == lds0 + (NWS - 1) * WSLOT ? lds0 : mslot + WSLOT;
  };
#pragma unroll 1
  for (int j = 0; j < 12; ++j) {
    issueWo(j);
    if (j == 0) { waitvm<44>(); cwrite(ardy, 82); continue; }
    if (j == 1) { waitvm<52>(); cwrite(ardy, 83); }
    waitvm<40>(); cwrite(ardy, max(83 + j, j < NPRE ? 0 : 84 + NPRE));
    if (j == 1) STAMP(3);
    if (j == NPRE - 1 || j == 11) {
      waitvm<20>(); cwrite(ardy, 84 + j);
      waitvm<0>(); cwrite(ardy, 85 + j);
      if (j == 11) break;
      STAMP(4);
      poll_flags(c.srdWs, vsib + F_OB, tag);
      cwrite(ardy + (C_RDYO - C_RDY), 2);
      STAMP(5);
    }
  }
  STAMP(6);
  poll_flags(c.srdWs, flag0 + F_P + (unsigned)hq * 64u + (unsigned)(lane & 7) * 8u, tag);
  cwrite(ardy + (C_RDYO - C_RDY), 3);
  if (c.h == 0) set_flag(c.srdWs, flag0, lane, tag);
  STAMP(7);
  const int k8 = c.h >> 2;
  if (k8 < 7) {
    const unsigned so = (unsigned)WS_P + (unsigned)(((c.br * 4 + c.bp) * 4 + hq) * 8) * PBLK + (unsigned)k8 * 10240u + 8192u;
    bf16x8 pr[2][8];
#pragma unroll
    for (int k = 0; k < 8; ++k) { bload_sc1<0>(pr[0][k], lane * 16, c.srdWs, so + k * PBLK); bload_sc1<1024>(pr[1][k], lane * 16, c.srdWs, so + k * PBLK); }
    waitvm<0>();
#pragma unroll
    for (int p = 0; p < 2; ++p) {
#pragma unroll
      for (int k = 0; k < 8; ++k) pin(pr[p][k]);
      reduce_store(pr[p], c.out + (size_t)((2 * c.bp + 1) * SQ + k8 * 16 + (lane & 15)) * (2 * DM) + c.br * DM + hq * 160 + (3 + p) * 32 + 8 * (lane >> 4), (lane >> 4) & 1);
    }
  }
}

DI void consumer(const Ctx& c, char* ws) {
  const int wid = c.wid;
  int lane, l15, g, tq, tp;
  auto relane = [&]() { lane = lane_id(); l15 = lane & 15; g = lane >> 4; tq = l15 >> 2; tp = lane & 3; };
  relane();
  const unsigned lds0 = c.lds0;
  STAMP(0); CSTAMP(32);
  const int hq = c.h & 3;
  const unsigned flag0 = (unsigned)(c.br * 4 + c.bp) * 1024u;
  const unsigned adone = lds0 + CTL + C_DONE + wid * 256 + lane * 4, ardy = lds0 + CTL + C_RDY + lane * 4;
  unsigned have = 0, pre = 0, haveO = 0, wacc = 1;

  bf16x8 qf[6], st6[6];
  {
    unsigned aW[2][2], aX[2];
#pragma unroll
    for (int s1 = 0; s1 < 2; ++s1)
#pragma unroll
      for (int T = 0; T < 2; ++T)
        aW[s1][T] = lds0 + 2 * GSLOT + XB + (8 * g + tq) * 384 + (((4 * s1 + tp) ^ ((tq >> 1) | ((g & 1) << 2))) << 4) + 8 * (T ^ (tp & 1));
    aX[0] = lds0 + 2 * GSLOT + wid * 4096 + l15 * 128 + ((g ^ (((l15 >> 1) & 3) << 1)) << 4);
    aX[1] = aX[0] ^ 64;
    f32x4 acc[12][2];
#pragma unroll
    for (int dt = 0; dt < 12; ++dt) { acc[dt][0] = f32x4{}; acc[dt][1] = f32x4{}; }
    bf16x8 xf[2][2], a[8];
    int slot = 2;
    auto adr = [&](int T, int K) { return aW[(K >> 1) & 1][K & 1] + T * PIECE + (K >> 2) * 128; };
    auto rdx = [&](int T, int i) { xf[i][T] = ldsr128(aX[T] + i * 2048); };
    need(have, ardy, 1, wacc);
    STAMP(29);
    rdx(0, 0); rdx(0, 1);
#pragma unroll
    for (int k = 0; k < 8; ++k) a[k] = trd2(adr(0, k), 1536);
    FENCE();
    auto step = [&](auto Tc, int t) {
      constexpr int T = decltype(Tc)::value;
#pragma unroll
      for (int k = 0; k < 12; ++k) {
        const int b = (12 * T + k) & 7;
        if (k == 4) {
          cwrite(adone, t + 1); seen(have, pre); need(have, ardy, t + 2, wacc);
          if (T) {
            const int dlt = slot == 2 ? -2 * GSLOT : GSLOT;
            slot = slot == 2 ? 0 : slot + 1;
            aW[0][0] += dlt; aW[0][1] += dlt; aW[1][0] += dlt; aW[1][1] += dlt; aX[0] += dlt; aX[1] += dlt;
          }
          FENCE();
        }
        const unsigned ra = k < 4 ? adr(T, k + 8) : adr(T ^ 1, k - 4);
        mfma(acc[k][0], a[b], xf[0][T]); FENCE();
        if (k == 0) pre = cread(ardy);
        if (!T && (k == 1 || k == 2)) rdx(1, k - 1);
        if (T && (k == 5 || k == 6)) rdx(0, k - 5);
        FENCE();
        mfma(acc[k][1], a[b], xf[1][T]); FENCE();
        a[b] = trd2(ra, 1536);
        FENCE();
      }
    };
    using I0 = std::integral_constant<int, 0>; using I1 = std::integral_constant<int, 1>;
#pragma unroll 1
    for (int t = 0; t < 20; t += 2) { step(I0{}, t); step(I1{}, t + 1); if ((t & 3) == 2) { STAMP(24 + (t >> 2)); CSTAMP(50 + (t >> 2)); } }
    nops<8>();
    relane();
    const bool odd = g & 1;
#pragma unroll
    for (int i = 0; i < 2; ++i)
#pragma unroll
      for (int s = 0; s < 6; ++s) {
        const f32x4 t0 = acc[2 * s][i] * QSCALE, t1 = acc[2 * s + 1][i] * QSCALE;
        const unsigned u0 = cvtpk(t0[0], t0[1]), u1 = cvtpk(t0[2], t0[3]), u2 = cvtpk(t1[0], t1[1]), u3 = cvtpk(t1[2], t1[3]);
        const u32x4 w = {odd ? u2 : u0, odd ? u3 : u1, odd ? u0 : u2, odd ? u1 : u3};
        (i ? st6[s] : qf[s]) = __builtin_bit_cast(bf16x8, w);
      }
  }
  STAMP(1); PUT(40, wacc); CSTAMP(34);

  const int item = (c.br * 4 + c.bp) * 32 + c.h;
  const unsigned vl = (unsigned)lane * 16u;
  auto sibo = [&](int rel) { return (unsigned)WS_O + (unsigned)((item & ~3) | ((hq + rel) & 3)) * OBLK + (unsigned)wid * 12288u; };
  bf16x8 sa[3][6];
#pragma unroll
  for (int r = 0; r < 3; ++r)
#pragma unroll
    for (int q = 0; q < 6; ++q) asm volatile("" : "=v"(sa[r][q]));
  {
    unsigned bK[2], bV[4], aK[2], aV[4];
    bK[0] = l15 * 384 + ((g ^ (((l15 >> 1) & 3) << 1)) << 4);
    bK[1] = bK[0] ^ 64;
#pragma unroll
    for (int d4 = 0; d4 < 4; ++d4) bV[d4] = (4 * g + tq) * 384 + (((2 * d4 + (tp >> 1)) ^ (((2 * g + (tq >> 1)) & 3) << 1)) << 4) + 8 * (tp & 1);
    unsigned slot = lds0, gp = D_ATT;
    auto next = [&]() {
      seen(have, pre);
      need(have, ardy, ++gp, wacc);
      const unsigned s = slot;
      slot = slot == lds0 + (NP - 1) * PIECE ? lds0 : slot + PIECE;
      return s;
    };
    auto nextK = [&]() { const unsigned s = next(); aK[0] = bK[0] + s; aK[1] = bK[1] + s; };
    auto nextV = [&]() {
      const unsigned s = next();
#pragma unroll
      for (int d4 = 0; d4 < 4; ++d4) aV[d4] = bV[d4] + s;
    };
    f32x4 o[12], s[2], negm;
    float lsum, sc;
    int mx;
    u32x4 pf;
    bf16x8 w[8];
    bool resc;
    auto rdK = [&](int n) { w[n & 7] = ldsr128(aK[(n % 6) & 1] + ((n % 6) >> 1) * 128 + (n / 6) * 6144); };
    auto vadr = [&](int dt) { return aV[dt & 3] + (dt >> 2) * 128; };
    auto rda = [&](int n) { if (n < 12) rdK(n); else if (n < 24) w[n & 7] = trd2(vadr(n - 12), 6144); else rdK(n - 24); };
    auto mfa = [&](int n) {
      if (n >= 12) mfma(o[n - 12], w[n & 7], __builtin_bit_cast(bf16x8, pf));
      else if (n % 6) mfma(s[n / 6], w[n & 7], qf[n % 6]);
      else mfmac(s[n / 6], w[n & 7], qf[0], negm);
    };
    auto ex = [&](int e) { s[e >> 2][e & 3] = __builtin_amdgcn_exp2f(s[e >> 2][e & 3]); };
    auto ad = [&](int e) { lsum = addf(lsum, s[e >> 2][e & 3]); };
    auto cv = [&]() { pf[0] = cvtpk(s[0][0], s[0][1]); pf[1] = cvtpk(s[0][2], s[0][3]); pf[2] = cvtpk(s[1][0], s[1][1]); pf[3] = cvtpk(s[1][2], s[1][3]); pin(pf); };
    auto mxs = [&](int kt, int hf) { mx = max3i((kt | hf) ? mx : 0, s[kt][2 * hf], s[kt][2 * hf + 1]); };
    auto lanemax = [&]() { return fmaxf(fmaxf(fmaxf(s[0][0], s[0][1]), fmaxf(s[0][2], s[0][3])), fmaxf(fmaxf(s[1][0], s[1][1]), fmaxf(s[1][2], s[1][3]))); };
#define SLOT(n, A) do { mfa(n); FENCE(); rda(n + 8); A; FENCE(); } while (0)
#pragma unroll 1
    for (int it = 0; it < 2; ++it) {
      {
        nextK();
#pragma unroll
        for (int n = 0; n < 8; ++n) rdK(n);
        FENCE();
#pragma unroll
        for (int n = 0; n < 12; ++n) {
          if (n % 6) mfma(s[n / 6], w[n & 7], qf[n % 6]); else mfma0(s[n / 6], w[n & 7], qf[0]);
          FENCE();
          if (n < 4) rdK(n + 8);
          o[n] = f32x4{};
          FENCE();
        }
        cwrite(adone, gp);
        nextK();
#pragma unroll
        for (int n = 0; n < 8; ++n) rdK(n);
        nops<8>();
        const float m = xg_max(lanemax());
        negm = (f32x4){-m, -m, -m, -m};
        s[0] -= m; s[1] -= m;
        lsum = 0.f; sc = 1.f; resc = false;
#pragma unroll
        for (int e = 0; e < 8; ++e) ex(e);
#pragma unroll
        for (int e = 0; e < 8; ++e) ad(e);
        cv();
        nops<2>();
      }
#pragma unroll 1
      for (int st = 1; st < 16; ++st) {
        SLOT(0, pre = cread(ardy));
        SLOT(1, ); SLOT(2, ); SLOT(3, );
        nextV(); FENCE();
        SLOT(4, ); SLOT(5, );
        if (resc) {
#pragma unroll
          for (int dt = 0; dt < 12; ++dt) o[dt] *= sc;
        }
        FENCE();
        SLOT(6, ); SLOT(7, );
        SLOT(8, mxs(0, 0)); SLOT(9, mxs(0, 1));
        SLOT(10, ); SLOT(11, );
        SLOT(12, pre = cread(ardy));
        SLOT(13, );
        SLOT(14, mxs(1, 0); mxs(1, 1));
        resc = __any(mx > __builtin_bit_cast(int, RESC_THR));
        if (resc) {
          const float d = fmaxf(xg_max(lanemax()), 0.f);
          s[0] -= d; s[1] -= d; negm -= d;
          sc = __builtin_amdgcn_exp2f(-d);
          lsum *= sc;
        }
        FENCE();
        SLOT(15, ex(0));
        cwrite(adone, gp);
        if (st < 15) nextK();
        FENCE();
        SLOT(16, ex(1); ad(0)); SLOT(17, ex(2); ad(1)); SLOT(18, ex(3); ad(2)); SLOT(19, ex(4); ad(3));
        SLOT(20, ex(5); ad(4)); SLOT(21, ex(6); ad(5)); SLOT(22, ex(7); ad(6)); SLOT(23, ad(7));
        cv(); FENCE();
        if (it && st == 2) arrive(c, 0, flag0 + F_OA + (unsigned)c.h * 8u);
        if (it && st == SPF) {
          need(haveO, ardy + (C_RDYO - C_RDY), 1, wacc);
#pragma unroll
          for (int r = 0; r < 3; ++r) {
            const unsigned so = sibo(r + 1);
            bload_sc1_into<0>(sa[r][0], vl, c.srdWs, so); bload_sc1_into<1024>(sa[r][1], vl, c.srdWs, so); bload_sc1_into<2048>(sa[r][2], vl, c.srdWs, so);
            bload_sc1_into<0>(sa[r][3], vl, c.srdWs, so + 3072); bload_sc1_into<1024>(sa[r][4], vl, c.srdWs, so + 3072); bload_sc1_into<2048>(sa[r][5], vl, c.srdWs, so + 3072);
          }
        }
        FENCE();
        if (st == 1 || st == 8) { STAMP(2 + 4 * it + (st >> 3)); CSTAMP(56 + 2 * it + (st >> 3)); }
      }
      {
        const float inv = 1.f / xg_sum(lsum);
        const float fo = resc ? sc * inv : inv;
        s[0] *= inv; s[1] *= inv;
        cv();
        nops<4>();
#pragma unroll
        for (int dt = 0; dt < 12; ++dt) o[dt] *= fo;
        FENCE();
        nextV();
        STAMP(4 + 4 * it);
        bf16x8 v[12];
#pragma unroll
        for (int dt = 0; dt < 12; ++dt) v[dt] = trd2(vadr(dt), 6144);
        cwrite(adone, gp);
        FENCE();
#pragma unroll
        for (int dt = 0; dt < 12; ++dt) mfma(o[dt], v[dt], __builtin_bit_cast(bf16x8, pf));
        FENCE();
#pragma unroll
        for (int q = 0; q < 6; ++q) qf[q] = st6[q];
        FENCE();
        const unsigned so = (unsigned)WS_O + (unsigned)item * OBLK + (unsigned)wid * 12288u + (unsigned)it * 6144u;
#pragma unroll
        for (int q = 0; q < 6; ++q) st6[q] = pack2(o[2 * q], o[2 * q + 1]);
        bstore_sc1<0>(st6[0], vl, c.srdWs, so); bstore_sc1<1024>(st6[1], vl, c.srdWs, so); bstore_sc1<2048>(st6[2], vl, c.srdWs, so);
        bstore_sc1<0>(st6[3], vl, c.srdWs, so + 3072); bstore_sc1<1024>(st6[4], vl, c.srdWs, so + 3072); bstore_sc1<2048>(st6[5], vl, c.srdWs, so + 3072);
      }
      STAMP(5 + 4 * it);
    }
  }

  PUT(41, wacc);
  relane();
  bf16x8 sb[3][6];
  auto loadO = [&](int r, int s) {
    const unsigned so = sibo(r + 1) + 6144 + s * 2048;
    bload_sc1<0>(sb[r][2 * s], vl, c.srdWs, so); bload_sc1<1024>(sb[r][2 * s + 1], vl, c.srdWs, so);
    FENCE();
  };
  auto pinO = [&](int r, int s) { pin(sb[r][2 * s]); pin(sb[r][2 * s + 1]); FENCE(); };
  unsigned aO[2];
  aO[0] = lds0 + (4 * g + tq) * 320 + ((tp ^ (g & 1)) << 4) + 8 * (tp & 1);
  aO[1] = aO[0] ^ 8;
  f32x4 y[5][2][2];
  constexpr int WN = 8;
  bf16x8 wf[WN];
  auto rd = [&](int n) {
    const int j = n / 20, k = n % 20, sl = j % NWS;
    if (n && k == 0 && sl % 3 == 0) { const int dlt = sl ? 3 * WSLOT : -6 * WSLOT; aO[0] += dlt; aO[1] += dlt; pin(aO[0]); pin(aO[1]); }
    wf[n % WN] = trd2(aO[k & 1] + (sl % 3) * WSLOT + (k / 10) * 10240 + ((k % 10) >> 1) * 64, 5120);
  };
  auto stage = [&](int j, const bf16x8 (&oA)[6], const bf16x8 (&oB)[6]) {
#pragma unroll
    for (int k = 0; k < 20; ++k) {
      const int n = 20 * j + k;
      if (k == 20 - WN && j < 5) cwrite(adone, D_G4 + j + 1);
      if (k == 20 - WN && j < 11) { seen(have, pre); need(have, ardy, D_G4 + j + 2, wacc); FENCE(); }
#pragma unroll
      for (int i = 0; i < 2; ++i) {
        const bf16x8& of = (i ? oB : oA)[2 * (j % 3) + k / 10];
        if (n < 10) mfma0(y[(k % 10) >> 1][k & 1][i], wf[n % WN], of);
        else mfma(y[(k % 10) >> 1][k & 1][i], wf[n % WN], of);
        FENCE();
        if (i == 0 && k == 16 - WN) pre = cread(ardy);
        FENCE();
      }
      if (n + WN < 240) rd(n + WN);
      FENCE();
    }
  };
  need(have, ardy, D_G4 + 1, wacc);
#pragma unroll
  for (int n = 0; n < WN; ++n) rd(n);
  FENCE();
  STAMP(10);
  stage(0, qf, st6);
  arrive(c, 1, flag0 + F_OB + (unsigned)c.h * 8u);
#pragma unroll
  for (int r = 0; r < 3; ++r)
#pragma unroll
    for (int q = 0; q < 6; ++q) pin(sa[r][q]);
  FENCE();
  STAMP(11);
  stage(1, qf, st6);
  stage(2, qf, st6);
  STAMP(12);
  need(haveO, ardy + (C_RDYO - C_RDY), 2, wacc);
  STAMP(13);
  loadO(0, 0); loadO(0, 1); loadO(0, 2); loadO(1, 0); loadO(1, 1); loadO(1, 2);
  STAMP(14);
  waitvm<10>(); pinO(0, 0); STAMP(15); stage(3, sa[0], sb[0]); loadO(2, 0);
  waitvm<10>(); pinO(0, 1); stage(4, sa[0], sb[0]); loadO(2, 1);
  waitvm<10>(); pinO(0, 2); stage(5, sa[0], sb[0]); loadO(2, 2);
  STAMP(16); CSTAMP(60);
  waitvm<6>(); pinO(1, 0); pinO(1, 1); pinO(1, 2); stage(6, sa[1], sb[1]); stage(7, sa[1], sb[1]); stage(8, sa[1], sb[1]);
  STAMP(17); CSTAMP(61);
  waitvm<0>(); pinO(2, 0); pinO(2, 1); pinO(2, 2); stage(9, sa[2], sb[2]); stage(10, sa[2], sb[2]); stage(11, sa[2], sb[2]);
  nops<8>();
  STAMP(20); PUT(42, wacc); CSTAMP(62);

  const int grp = (c.br * 4 + c.bp) * 4 + hq, k8 = c.h >> 2;
  {
    const unsigned so = (unsigned)WS_P + (unsigned)(grp * 8 + k8) * PBLK + (unsigned)wid * 10240u;
#pragma unroll
    for (int i = 0; i < 2; ++i) {
      bstore_sc1<0>(pack2(y[0][0][i], y[0][1][i]), vl, c.srdWs, so + i * 5120); bstore_sc1<1024>(pack2(y[1][0][i], y[1][1][i]), vl, c.srdWs, so + i * 5120);
      bstore_sc1<2048>(pack2(y[2][0][i], y[2][1][i]), vl, c.srdWs, so + i * 5120); bstore_sc1<3072>(pack2(y[3][0][i], y[3][1][i]), vl, c.srdWs, so + i * 5120);
      bstore_sc1<0>(pack2(y[4][0][i], y[4][1][i]), vl, c.srdWs, so + i * 5120 + 4096);
    }
  }
  relane();
  arrive(c, 2, flag0 + F_P + (unsigned)hq * 64u + (unsigned)k8 * 8u);
  STAMP(21);
  need(haveO, ardy + (C_RDYO - C_RDY), 3, wacc);
  STAMP(22);
  {
    bf16x8 pr[8];
    const unsigned so = (unsigned)WS_P + (unsigned)(grp * 8) * PBLK + (unsigned)wid * 10240u + (unsigned)k8 * 1024u;
#pragma unroll
    for (int k = 0; k < 8; ++k) bload_sc1<0>(pr[k], vl, c.srdWs, so + k * PBLK);
    waitvm<0>();
#pragma unroll
    for (int k = 0; k < 8; ++k) pin(pr[k]);
    const int i = k8 >= 5, cp = k8 - 5 * i;
    reduce_store(pr, c.out + (size_t)((2 * c.bp + i) * SQ + wid * 16 + l15) * (2 * DM) + c.br * DM + hq * 160 + cp * 32 + 8 * g, g & 1);
  }
  asm volatile(".Lcode_end:");
}

typedef unsigned long long u64;
typedef u64 u64x2 __attribute__((ext_vector_type(2)));
typedef u64 u64x4 __attribute__((ext_vector_type(4)));
__global__ __launch_bounds__(512, 2) void fused_kernel(const u16* xA, const u16* kA, const u16* vA, const u16* wqA, const u16* woA,
                                                       const u16* xB, const u16* kB, const u16* vB, const u16* wqB, const u16* woB,
                                                       u16* out_, char* ws_) {
  extern __shared__ __attribute__((aligned(16))) char lds[];
  Ctx c;
  const int xcd = blockIdx.x & 7, idx = blockIdx.x >> 3;
  c.h = 8 * (xcd >> 1) + (idx & 7); c.bp = idx >> 3; c.br = xcd & 1;
  u64x4 p4; u64 p1; u64x2 p2;
  asm volatile("s_load_dwordx8 %0, %3, %4\n\ts_load_dwordx2 %1, %3, %4 offset:32\n\ts_load_dwordx4 %2, %3, 0x50"
               : "=&s"(p4), "=&s"(p1), "=&s"(p2) : "s"(__builtin_amdgcn_kernarg_segment_ptr()), "s"(c.br * 40) : "memory");
  c.lds0 = (unsigned)(uintptr_t)lds;
  c.wid = rfl(threadIdx.x >> 6);
  {
    unsigned long long pc; unsigned clen;
    asm volatile("s_getpc_b64 %0\n.Lcode_here:\n\ts_mov_b32 %1, .Lcode_end-.Lcode_here" : "=s"(pc), "=s"(clen));
    c.srdC = __builtin_amdgcn_make_buffer_rsrc((void*)pc, (short)0, (int)clen, 0x00020000);
    c.cshare = (unsigned)(idx & 31) * 128u;
    if (c.wid == 0) touch(c.lds0 + CTL + C_JUNK + 1024, threadIdx.x * 4096u, c.srdC, c.cshare);
  }
  cwrite(c.lds0 + CTL + threadIdx.x * 4, 0);
  if (c.wid < 2) cwrite(c.lds0 + CTL + 2048 + threadIdx.x * 4, 0);
  {
    const int lane = threadIdx.x & 63;
    c.vx[0] = (unsigned)((lane >> 3) * (DM * 2) + (((lane & 7) ^ (((lane >> 4) & 3) << 1)) << 4));
    c.vx[1] = c.vx[0] + (8 * DM * 2 - 1024); c.vx[2] = c.vx[0] + (SQ * DM * 2 - 2048); c.vx[3] = c.vx[0] + ((SQ + 8) * DM * 2 - 3072);
#pragma unroll
    for (int k = 0; k < 3; ++k) {
      const int row = ((8 * k + (lane >> 3)) * 11) >> 5;
      c.vw[0][k] = (unsigned)(row * (HD * 2 - 384) + ((lane ^ ((row >> 1) & 1)) << 4));
      c.vw[1][k] = c.vw[0][k] ^ 64;
      c.vb[k] = (unsigned)((lane ^ (((row >> 1) & 3) << 1)) << 4);
      pin(c.vw[0][k]); pin(c.vw[1][k]); pin(c.vb[k]); pin(c.vx[k]);
    }
    pin(c.vx[3]);
  }
  asm volatile("s_waitcnt lgkmcnt(0)\n\ts_barrier" : "+s"(p4), "+s"(p1), "+s"(p2) :: "memory");
  const u16* x = (const u16*)p4[0]; const u16* kc = (const u16*)p4[1]; const u16* vc = (const u16*)p4[2]; const u16* wq = (const u16*)p4[3]; const u16* wo = (const u16*)p1;
  char* ws = (char*)p2[1];
  c.out = (u16*)p2[0];
  const size_t kvoff = (size_t)(2 * c.bp * NH + c.h) * SK * DH;
  c.srdX = __builtin_amdgcn_make_buffer_rsrc((void*)(x + (size_t)c.bp * 2 * SQ * DM), (short)0, 2 * SQ * DM * 2, 0x00020000);
  c.srdW = __builtin_amdgcn_make_buffer_rsrc((void*)(wq + c.h * DH), (short)0, (DM * HD - c.h * DH) * 2, 0x00020000);
  c.srdK = __builtin_amdgcn_make_buffer_rsrc((void*)(kc + kvoff), (short)0, KVITEM + SK * DH * 2, 0x00020000);
  c.srdV = __builtin_amdgcn_make_buffer_rsrc((void*)(vc + kvoff), (short)0, KVITEM + SK * DH * 2, 0x00020000);
  c.srdWo = __builtin_amdgcn_make_buffer_rsrc((void*)(wo + (size_t)(c.h >> 2) * 4 * DH * DM + (c.h & 3) * 160), (short)0, (4 * DH * DM - (c.h & 3) * 160) * 2, 0x00020000);
  c.srdWs = __builtin_amdgcn_make_buffer_rsrc((void*)ws, (short)0, (int)WS_END, 0x00020000);
  if (__builtin_expect(c.wid == 7, 1)) {
    __builtin_amdgcn_s_setprio(3);
    loader(c, ws);
    asm volatile("s_endpgm" ::: "memory");
  }
  consumer(c, ws);
}

extern "C" void kernel_launch(void* const* d_in, const int* in_sizes, int n_in, void* d_out, int out_size, void* d_ws, size_t ws_size, hipStream_t stream) {
  (void)in_sizes; (void)n_in; (void)out_size; (void)ws_size;
  (void)hipFuncSetAttribute((const void*)fused_kernel, hipFuncAttributeMaxDynamicSharedMemorySize, LDS_BYTES);
  fused_kernel<<<256, 512, LDS_BYTES, stream>>>((const u16*)d_in[0], (const u16*)d_in[1], (const u16*)d_in[2], (const u16*)d_in[3], (const u16*)d_in[4],
                                                (const u16*)d_in[5], (const u16*)d_in[6], (const u16*)d_in[7], (const u16*)d_in[8], (const u16*)d_in[9],
                                                (u16*)d_out, (char*)d_ws);
}
```

```cpp
#define DUMP_STAMPS
#define NOG1ST
#define PB 37
#define PLO 0
#define PHI 0
#define KV_NT PRG XE 4 SPF=14 PFL=52
#include <hip/hip_runtime.h>
#include <hip/hip_bf16.h>
#include <stdint.h>
#include <type_traits>
#pragma clang diagnostic ignored "-Winline-asm"

typedef unsigned short u16;
typedef short bf16x8 __attribute__((ext_vector_type(8)));
typedef short s16x4 __attribute__((ext_vector_type(4)));
typedef float f32x4 __attribute__((ext_vector_type(4)));
typedef float f32x2 __attribute__((ext_vector_type(2)));
typedef __bf16 bf16x2_t __attribute__((ext_vector_type(2)));
typedef unsigned u32x4 __attribute__((ext_vector_type(4)));
typedef unsigned u32x2 __attribute__((ext_vector_type(2)));
typedef __amdgpu_buffer_rsrc_t rsrc_t;
#define LAS __attribute__((address_space(3)))
#define GAS __attribute__((address_space(1)))
#define DI __device__ __forceinline__

constexpr int SQ = 112, DM = 640, NH = 32, SK = 512, DH = 192, HD = NH * DH;
constexpr int PIECE = 12288, NP = 13;
constexpr int WSLOT = 20480, NWS = 7;
constexpr int CTL = -4096;
constexpr int C_G1 = 0, C_G4 = 40, C_RDY = 64, C_RDYO = 68;
constexpr int C_TAG = 72, C_CNT = 80, C_DONE = 256, C_JUNK = 2048;
constexpr int LDS_BYTES = 163840;
constexpr unsigned FLAG_BYTES = 8192;
constexpr unsigned OBLK = 7 * 12 * 1024;
constexpr unsigned PBLK = 7 * 10 * 1024;
constexpr size_t WS_O = FLAG_BYTES;
constexpr size_t WS_P = WS_O + (size_t)256 * OBLK;
constexpr size_t WS_END = WS_P + (size_t)256 * PBLK;
constexpr float QSCALE = 0.07216878364870322f * 1.4426950408889634f;
constexpr float RESC_THR = 8.0f;
constexpr unsigned KVITEM = NH * SK * DH * 2;
constexpr int D_ATT = 20, D_G4 = 84;
#define NPRE 5
#define SPF 11
#define PFL 48
#define XL 2
#define XE 0

DI void mfma(f32x4& d, const bf16x8& a, const bf16x8& b) { asm("v_mfma_f32_16x16x32_bf16 %0, %1, %2, %0" : "+v"(d) : "v"(a), "v"(b)); }
DI void mfmac(f32x4& d, const bf16x8& a, const bf16x8& b, const f32x4& c) { asm("v_mfma_f32_16x16x32_bf16 %0, %1, %2, %3" : "=v"(d) : "v"(a), "v"(b), "v"(c)); }
DI void mfma0(f32x4& d, const bf16x8& a, const bf16x8& b) { asm("v_mfma_f32_16x16x32_bf16 %0, %1, %2, 0" : "=v"(d) : "v"(a), "v"(b)); }
#define FENCE() __builtin_amdgcn_sched_barrier(0)
template <int N> DI void nops() { FENCE(); asm volatile("s_nop %0" :: "i"(N - 1)); FENCE(); }
DI int lane_id() { int l; asm volatile("v_mbcnt_lo_u32_b32 %0, -1, 0\n\tv_mbcnt_hi_u32_b32 %0, -1, %0" : "=v"(l)); return l; }
DI unsigned rfl(unsigned x) { return __builtin_amdgcn_readfirstlane(x); }
DI unsigned cvtpk(float lo, float hi) { f32x2 v = {lo, hi}; bf16x2_t b = __builtin_convertvector(v, bf16x2_t); return __builtin_bit_cast(unsigned, b); }
DI s16x4 trd(unsigned a) { return __builtin_bit_cast(s16x4, __builtin_amdgcn_ds_read_tr16_b64_v4i16((LAS s16x4*)a)); }
DI void trlo(bf16x8& d, unsigned a) { const s16x4 t = trd(a); d[0] = t[0]; d[1] = t[1]; d[2] = t[2]; d[3] = t[3]; }
DI void trhi(bf16x8& d, unsigned a) { const s16x4 t = trd(a); d[4] = t[0]; d[5] = t[1]; d[6] = t[2]; d[7] = t[3]; }
DI bf16x8 trd2(unsigned a, unsigned d) { s16x4 lo = trd(a), hi = trd(a + d); return (bf16x8){lo[0], lo[1], lo[2], lo[3], hi[0], hi[1], hi[2], hi[3]}; }
DI bf16x8 ldsr128(unsigned a) { return *(const LAS bf16x8*)a; }
DI bf16x8 pack2(const f32x4& a, const f32x4& b) {
  u32x4 w = {cvtpk(a[0], a[1]), cvtpk(a[2], a[3]), cvtpk(b[0], b[1]), cvtpk(b[2], b[3])};
  return __builtin_bit_cast(bf16x8, w);
}
DI float bflo(unsigned w) { return __uint_as_float(w << 16); }
DI float bfhi(unsigned w) { return __uint_as_float(w & 0xFFFF0000u); }
DI float addf(float a, float b) { float d = a + b; asm("" : "+v"(d)); return d; }
DI int max3i(int a, float b, float c) { return max(max(a, __float_as_int(b)), __float_as_int(c)); }
DI f32x2 sw16(float v) { const unsigned u = __float_as_uint(v); const auto r = __builtin_amdgcn_permlane16_swap(u, u, false, false); return f32x2{__uint_as_float(r[0]), __uint_as_float(r[1])}; }
DI f32x2 sw32(float v) { const unsigned u = __float_as_uint(v); const auto r = __builtin_amdgcn_permlane32_swap(u, u, false, false); return f32x2{__uint_as_float(r[0]), __uint_as_float(r[1])}; }
DI float xg_max(float v) { f32x2 r = sw16(v); r = sw32(fmaxf(r[0], r[1])); return fmaxf(r[0], r[1]); }
DI float xg_sum(float v) { f32x2 r = sw16(v); r = sw32(r[0] + r[1]); return r[0] + r[1]; }

#define KVPOL " nt"
DI void dma4(unsigned m0v, unsigned v0, unsigned v1, unsigned v2, unsigned v3, rsrc_t srd, unsigned soff) {
  asm volatile("s_mov_b32 m0, %0\n\ts_nop 0\n\t"
               "buffer_load_dwordx4 %1, %5, %6 offen lds\n\t"
               "buffer_load_dwordx4 %2, %5, %6 offen offset:1024 lds\n\t"
               "buffer_load_dwordx4 %3, %5, %6 offen offset:2048 lds\n\t"
               "buffer_load_dwordx4 %4, %5, %6 offen offset:3072 lds"
               :: "s"(m0v), "v"(v0), "v"(v1), "v"(v2), "v"(v3), "s"(srd), "s"(soff) : "m0", "memory");
}
DI void dma3k(unsigned m0v, unsigned v0, unsigned v1, unsigned v2, rsrc_t srd, unsigned soff) {
  asm volatile("s_mov_b32 m0, %0\n\ts_nop 0\n\t"
               "buffer_load_dwordx4 %1, %4, %5 offen" KVPOL " lds\n\t"
               "buffer_load_dwordx4 %2, %4, %5 offen offset:1024" KVPOL " lds\n\t"
               "buffer_load_dwordx4 %3, %4, %5 offen offset:2048" KVPOL " lds"
               :: "s"(m0v), "v"(v0), "v"(v1), "v"(v2), "s"(srd), "s"(soff) : "m0", "memory");
}
DI void dma3(unsigned m0v, unsigned v0, unsigned v1, unsigned v2, rsrc_t srd, unsigned soff) {
  asm volatile("s_mov_b32 m0, %0\n\ts_nop 0\n\t"
               "buffer_load_dwordx4 %1, %4, %5 offen lds\n\t"
               "buffer_load_dwordx4 %2, %4, %5 offen offset:1024 lds\n\t"
               "buffer_load_dwordx4 %3, %4, %5 offen offset:2048 lds"
               :: "s"(m0v), "v"(v0), "v"(v1), "v"(v2), "s"(srd), "s"(soff) : "m0", "memory");
}
DI void touch(unsigned m0v, unsigned v, rsrc_t srd, unsigned soff) {
  asm volatile("s_mov_b32 m0, %0\n\ts_nop 0\n\tbuffer_load_dword %1, %2, %3 offen lds" :: "s"(m0v), "v"(v), "s"(srd), "s"(soff) : "m0", "memory");
}
template <int OFF> DI void bload_sc1(bf16x8& d, unsigned v, rsrc_t srd, unsigned soff) {
  asm volatile("buffer_load_dwordx4 %0, %1, %2, %3 offen offset:%4 sc1" : "=v"(d) : "v"(v), "s"(srd), "s"(soff), "i"(OFF) : "memory");
}
template <int OFF> DI void bload_sc1_into(bf16x8& d, unsigned v, rsrc_t srd, unsigned soff) {
  asm volatile("buffer_load_dwordx4 %0, %1, %2, %3 offen offset:%4 sc1" : "+v"(d) : "v"(v), "s"(srd), "s"(soff), "i"(OFF) : "memory");
}
template <int OFF> DI void bstore_sc1(const bf16x8& d, unsigned v, rsrc_t srd, unsigned soff) {
  asm volatile("buffer_store_dwordx4 %0, %1, %2, %3 offen offset:%4 sc1\n\ts_nop 1" :: "v"(d), "v"(v), "s"(srd), "s"(soff), "i"(OFF) : "memory");
}
template <int N> DI void waitvm() { asm volatile("s_waitcnt vmcnt(%0)" :: "i"(N) : "memory"); }
template <typename T> DI void pin(T& x) { asm volatile("" : "+v"(x)); }
template <typename T> DI void pins(T& x) { asm volatile("" : "+s"(x)); }
DI void cbar() { asm volatile("" ::: "memory"); }

#define STAMP_(i, ins) do { unsigned long long t_; asm volatile(ins " %0\n\ts_waitcnt lgkmcnt(0)" : "=s"(t_) :: "memory"); \
  asm volatile("s_mov_b32 m0, %2\n\ts_nop 4\n\tv_writelane_b32 %0, %1, m0" : "+v"(stampv) : "s"((unsigned)t_), "s"(i) : "m0"); } while (0)
#define STAMP(i) STAMP_(i, "s_memrealtime")
#define PUT(i, v) asm volatile("s_mov_b32 m0, %2\n\ts_nop 4\n\tv_writelane_b32 %0, %1, m0" : "+v"(stampv) : "s"(v), "s"(i) : "m0")
DI unsigned nowt() { unsigned long long t_; asm volatile("s_memrealtime %0\n\ts_waitcnt lgkmcnt(0)" : "=s"(t_) :: "memory"); return (unsigned)t_; }
#define WACC_BEGIN const unsigned wt0_ = nowt()
#define WACC_END(w) (w) += nowt() - wt0_
#define CSTAMP(i) STAMP_(i, "s_memtime")

#define G1ST if (0)
struct Ctx {
  rsrc_t srdW, srdK, srdV, srdWo, srdWs, srdC;
  u32x4 srdX;
  GAS u16* out;
  unsigned lds0, ctl, cshare;
  unsigned vw[2][3], vb[3];
  int wid, br, bp, h, rot2;
};

DI unsigned cread(unsigned a) { return *(const volatile LAS unsigned*)a; }
DI void cwrite(unsigned a, unsigned v) { *(volatile LAS unsigned*)a = v; }
template <int A> DI unsigned bump(unsigned z) { cbar(); const unsigned r = __builtin_amdgcn_ds_append((LAS int*)z + A / 4); cbar(); return r; }
DI void need(unsigned& have, unsigned a, unsigned n, unsigned& wacc) {
  n *= 64;
  if (__builtin_expect(have < n, 0)) {
    WACC_BEGIN;
#pragma unroll 1
    for (unsigned spins = 0;; ++spins) {
      have = rfl(cread(a));
      if (have >= n) break;
      if (spins > (1u << 20)) { have = ~0u; break; }
      __builtin_amdgcn_s_sleep(1);
    }
    WACC_END(wacc);
  }
  cbar();
}
DI void seen(unsigned& have, unsigned pre) { have = max(have, rfl(pre)); }

struct Tag { unsigned lo, hi; };
DI bool match(const u32x2& f, Tag tag) { return __all(f[0] == tag.lo && f[1] == tag.hi); }
DI void poll_flags(rsrc_t srdWs, unsigned vo, Tag tag) {
#pragma unroll 1
  for (unsigned spins = 0; spins < (1u << 20); ++spins) {
    u32x2 f;
    asm volatile("buffer_load_dwordx2 %0, %1, %2, 0 offen sc1\n\ts_waitcnt vmcnt(0)" : "=v"(f) : "v"(vo), "s"(srdWs) : "memory");
    if (match(f, tag)) break;
    __builtin_amdgcn_s_sleep(2);
  }
}
DI void set_flag(rsrc_t srdWs, unsigned off, int lane, Tag tag) {
  if (lane == 0) { const u32x2 t = {tag.lo, tag.hi}; asm volatile("buffer_store_dwordx2 %0, off, %1, %2 sc1\n\ts_nop 1" :: "v"(t), "s"(srdWs), "s"(off) : "memory"); }
}
DI void arrive(const Ctx& c, int which, unsigned off) {
  waitvm<0>();
  const int lane = lane_id();
  unsigned old = 0;
  if (lane == 0) old = __hip_atomic_fetch_add((LAS unsigned*)(c.lds0 + CTL + C_CNT + 4 * which), 1u, __ATOMIC_RELAXED, __HIP_MEMORY_SCOPE_WORKGROUP);
  if (rfl(old) == 6) {
    const Tag tag{rfl(cread(c.lds0 + CTL + C_TAG)), rfl(cread(c.lds0 + CTL + C_TAG + 4))};
    set_flag(c.srdWs, off, lane, tag);
  }
}

DI void reduce_store(const bf16x8 (&pr)[8], GAS u16* dst, bool odd) {
  float a[8] = {0.f, 0.f, 0.f, 0.f, 0.f, 0.f, 0.f, 0.f};
#pragma unroll
  for (int k = 0; k < 8; ++k) {
    const u32x4 w = __builtin_bit_cast(u32x4, pr[k]);
#pragma unroll
    for (int e = 0; e < 4; ++e) { a[2 * e] += bflo(w[e]); a[2 * e + 1] += bfhi(w[e]); }
  }
  const unsigned u0 = cvtpk(a[0], a[1]), u1 = cvtpk(a[2], a[3]), u2 = cvtpk(a[4], a[5]), u3 = cvtpk(a[6], a[7]);
  const u32x4 w = {odd ? u2 : u0, odd ? u3 : u1, odd ? u0 : u2, odd ? u1 : u3};
  *(GAS u32x4*)dst = w;
}
constexpr unsigned F_OA = 256, F_OB = 512, F_P = 768;

DI void loader(const Ctx& c, char* ws) {
  const int lane = lane_id();
  const unsigned lds0 = c.lds0;
  const int hq = c.h & 3;
  unsigned stampv = 0;
  STAMP(0); CSTAMP(32);
  int dv = 0, gv = 0;
  unsigned wacc = 1;
  const unsigned adone = lds0 + CTL + C_DONE + (lane < 7 ? lane : 0) * 260;
  auto gate = [&](int p) {
    if (!__all(dv >= p)) {
      WACC_BEGIN;
#pragma unroll 1
      for (unsigned spins = 0;; ++spins) {
        dv = cread(adone);
        if (__all(dv >= p)) break;
        if (spins > (1u << 20)) { dv = 0x7FFFFFFF; break; }
        __builtin_amdgcn_s_sleep(1);
      }
      WACC_END(wacc);
    }
    cbar();
  };
  auto gatec = [&](int i, int n) {
    n *= 7 * 64;
    if (__builtin_amdgcn_readlane(gv, i) < n) {
      WACC_BEGIN;
#pragma unroll 1
      for (unsigned spins = 0;; ++spins) {
        gv = cread(lds0 + CTL + lane * 4);
        if (__builtin_amdgcn_readlane(gv, i) >= n) break;
        if (spins > (1u << 20)) { gv = 0x7FFFFFFF; break; }
        __builtin_amdgcn_s_sleep(1);
      }
      WACC_END(wacc);
    }
    cbar();
  };
  const unsigned (&vw)[2][3] = c.vw; const unsigned (&vb)[3] = c.vb;
  auto wpieces = [&](int t0) {
    unsigned m = lds0 + 3 * PIECE;
#pragma unroll 1
    for (int t = t0; t < t0 + 10; ++t) {
      if (t0) gatec(t - t0, 1);
      const unsigned so = (unsigned)(((t ^ c.rot2) >> 1) * 64 + (t & 1) * 8) * (HD * 2);
#pragma unroll
      for (int q = 0; q < 4; ++q) dma3(m + 3072 * q, vw[q & 1][0], vw[q & 1][1], vw[q & 1][2], c.srdW, so + q * (16 * HD * 2));
      m += PIECE;
      waitvm<36>(); if (t >= 3) bump<C_RDY>(c.ctl);
    }
  };
  const unsigned flag0 = (unsigned)(c.br * 4 + c.bp) * 1024u;
  const unsigned vtc = (unsigned)lane * 4096u;
  unsigned mslot = lds0;
  auto pieces = [&](int p0, int p1, bool tc) {
#pragma unroll 1
    for (int p = p0; p < p1; ++p) {
      const int q = p & 31;
      const bool isv = ((q & 1) == 0 && q) || q == 31;
      const unsigned so = (unsigned)((isv ? q - 1 : q + 1) >> 1) * PIECE + (unsigned)(p >> 5) * KVITEM;
      const rsrc_t srd = isv ? c.srdV : c.srdK;
      if (p0 == 3) gatec(p - 3, 2); else if (p0) gate(p + 8);
#pragma unroll
      for (int k = 0; k < 4; ++k) dma3k(mslot + 3072 * k, vb[0], vb[1], vb[2], srd, so + 3072 * k);
      mslot = mslot == lds0 + (NP - 1) * PIECE ? lds0 : mslot + PIECE;
      waitvm<36>(); bump<C_RDY>(c.ctl);
      if ((p & 7) == 7) { STAMP(10 + (p >> 3)); PUT(42 + (p >> 3), wacc); }
      if (tc && (p & 1) && p < 60) touch(lds0 + CTL + C_JUNK, vtc, c.srdC, c.cshare);
    }
  };
  wpieces(0);
  STAMP(24);
  u32x2 f;
  asm volatile("buffer_load_dwordx2 %0, off, %1, %2 sc1" : "=v"(f) : "s"(c.srdWs), "s"(flag0) : "memory");
  pieces(0, 3, false);
  STAMP(25);
  wpieces(10);
  STAMP(26);
  pin(f);
  const unsigned long long tg = (((unsigned long long)rfl(f[1]) << 32) | rfl(f[0])) + 1ull;
  const Tag tag{(unsigned)tg, (unsigned)(tg >> 32)};
  pieces(3, NP, false);
  STAMP(1); PUT(40, wacc); CSTAMP(34);
  if (lane == 0) { cwrite(lds0 + CTL + C_TAG, tag.lo); cwrite(lds0 + CTL + C_TAG + 4, tag.hi); }
  pieces(NP, PFL, false);
  const unsigned vsib = flag0 + (unsigned)(c.h >> 2) * 32u + (unsigned)(lane & 3) * 8u;
  asm volatile("buffer_load_dwordx2 %0, %1, %2, 0 offen offset:%3 sc1" : "=v"(f) : "v"(vsib), "s"(c.srdWs), "i"(F_OA) : "memory");
  pieces(PFL, PFL + 4, false);
  pin(f);
  if (!match(f, tag)) poll_flags(c.srdWs, vsib + F_OA, tag);
  bump<C_RDYO>(c.ctl);
  STAMP(8);
  pieces(PFL + 4, 64, true);
  STAMP(2); PUT(41, wacc);
  unsigned vo[5];
#pragma unroll
  for (int k = 0; k < 5; ++k) {
    const int row = ((16 * k + (lane >> 2)) * 205) >> 10;
    vo[k] = (unsigned)(row * (DM * 2 - 320) + ((lane ^ ((row >> 2) & 1)) << 4) + (k == 4 ? 4096 : 0));
  }
  unsigned cwo = hq * 3;
  mslot = lds0;
  auto issueWo = [&](int j) {
    if (j < NWS) gate(D_ATT + 53 + (int)((0xB986431u >> (4 * j)) & 15)); else gatec(C_G4 / 4 + j - NWS, 1);
#pragma unroll
    for (int q = 0; q < 4; ++q) {
      const unsigned so = cwo * (64 * DM * 2) + q * (16 * DM * 2);
      dma4(mslot + 5120 * q, vo[0], vo[1], vo[2], vo[3], c.srdWo, so);
      asm volatile("s_mov_b32 m0, %0\n\ts_nop 0\n\tbuffer_load_dwordx4 %1, %2, %3 offen lds" :: "s"(mslot + 5120 * q + 4096), "v"(vo[4]), "s"(c.srdWo), "s"(so) : "m0", "memory");
    }
    cwo = cwo == 11 ? 0 : cwo + 1;
    mslot = mslot == lds0 + (NWS - 1) * WSLOT ? lds0 : mslot + WSLOT;
  };
#pragma unroll 1
  for (int j = 0; j < 12; ++j) {
    issueWo(j);
    if (j == 0) { waitvm<44>(); bump<C_RDY>(c.ctl); continue; }
    if (j == 1) { waitvm<52>(); bump<C_RDY>(c.ctl); }
    waitvm<40>(); if (j < NPRE || j > NPRE + 1) bump<C_RDY>(c.ctl);
    if (j == 1) STAMP(3);
    if (j == NPRE - 1 || j == 11) {
      waitvm<20>(); bump<C_RDY>(c.ctl);
      waitvm<0>(); bump<C_RDY>(c.ctl);
      if (j == 11) break;
      STAMP(4);
      poll_flags(c.srdWs, vsib + F_OB, tag);
      bump<C_RDYO>(c.ctl);
      STAMP(5);
    }
  }
  STAMP(6);
  poll_flags(c.srdWs, flag0 + F_P + (unsigned)hq * 64u + (unsigned)(lane & 7) * 8u, tag);
  bump<C_RDYO>(c.ctl);
  if (c.h == 0) set_flag(c.srdWs, flag0, lane, tag);
  STAMP(7);
  const int k8 = c.h >> 2;
  if (k8 < 7) {
    const unsigned so = (unsigned)WS_P + (unsigned)(((c.br * 4 + c.bp) * 4 + hq) * 8) * PBLK + (unsigned)k8 * 10240u + 8192u;
    bf16x8 pr[2][8];
#pragma unroll
    for (int k = 0; k < 8; ++k) { bload_sc1<0>(pr[0][k], lane * 16, c.srdWs, so + k * PBLK); bload_sc1<1024>(pr[1][k], lane * 16, c.srdWs, so + k * PBLK); }
    waitvm<0>();
#pragma unroll
    for (int p = 0; p < 2; ++p) {
#pragma unroll
      for (int k = 0; k < 8; ++k) pin(pr[p][k]);
      reduce_store(pr[p], c.out + (size_t)((2 * c.bp + 1) * SQ + k8 * 16 + (lane & 15)) * (2 * DM) + c.br * DM + hq * 160 + (3 + p) * 32 + 8 * (lane >> 4), (lane >> 4) & 1);
    }
  }
  waitvm<0>();
  STAMP(23); CSTAMP(33);
  ((GAS unsigned*)(ws + (200u << 20)))[(blockIdx.x * 8 + 7) * 64 + lane] = stampv;
  if (blockIdx.x == PB) { const unsigned a0 = __builtin_amdgcn_readlane(stampv, 0), x = __builtin_amdgcn_readlane(stampv, PHI) - __builtin_amdgcn_readlane(stampv, PLO);
    while ((unsigned)__builtin_amdgcn_s_memrealtime() - a0 < 12000u + 4u * x) __builtin_amdgcn_s_sleep(1); }
}

DI void consumer(const Ctx& c, char* ws) {
  const int wid = c.wid;
  int lane, l15, g, tq, tp;
  auto relane = [&]() { lane = lane_id(); l15 = lane & 15; g = lane >> 4; tq = l15 >> 2; tp = lane & 3; };
  relane();
  const unsigned lds0 = c.lds0;
  unsigned stampv = 0;
  STAMP(0); CSTAMP(32);
  const int hq = c.h & 3;
  const unsigned flag0 = (unsigned)(c.br * 4 + c.bp) * 1024u;
  unsigned ardy = lds0 + CTL + C_RDY;
  pin(ardy);
  unsigned have = 0, pre = 0, haveO = 0, wacc = 1;

  bf16x8 qf[6], st6[6];
  {
    bf16x8 xr[5][2][2], xa[2], a[8];
    unsigned vx[2];
    vx[0] = (unsigned)((wid * 16 + (l15 & 14)) * (DM * 2) + g * 32 + (lane & 1) * 16);
    vx[1] = vx[0] + SQ * DM * 2;
    u32x4 srdX = c.srdX;
    unsigned sx[2] = {(unsigned)c.rot2 * 64u + XL * 128u, XL * 128u - (unsigned)c.rot2 * 64u};
#define XLOAD(C, D, V, S, OFF) asm volatile("buffer_load_dwordx4 %0, %1, %2, %3 offen offset:%4" : C(D) : "v"(V), "s"(srdX), "s"(S), "i"(OFF) : "memory")
    auto xfirst = [&](int d) {
#pragma unroll
      for (int q = 0; q < 4; ++q) XLOAD("=v", xr[d][q & 1][q >> 1], vx[q >> 1], sx[d & 1] - XL * 128u + (d & 1 ? 128u : 0u), (d & ~1) * 128 + (q & 1) * (DM * 2));
    };
    unsigned long long even = 0x5555555555555555ull;
    pins(even);
    auto trade = [&](int d, int n) {
      const int i = (n >> 2) & 1, e = n & 3;
      u32x4 r0 = __builtin_bit_cast(u32x4, xr[d][0][i]), r1 = __builtin_bit_cast(u32x4, xr[d][1][i]), t = __builtin_bit_cast(u32x4, xa[i]);
#define XSEL "v_cndmask_b32_dpp %0, %1, %2, vcc quad_perm:[1,0,3,2] row_mask:0xf bank_mask:0xf"
      register unsigned long long m asm("vcc") = n < 8 ? even : ~even;
      if (n < 8) { asm volatile(XSEL : "=v"(t[e]) : "v"(r1[e]), "v"(r0[e]), "s"(m)); xa[i] = __builtin_bit_cast(bf16x8, t); }
      else { asm volatile(XSEL : "=v"(r1[e]) : "v"(r0[e]), "0"(r1[e]), "s"(m)); xr[d][1][i] = __builtin_bit_cast(bf16x8, r1); }
    };
#pragma unroll
    for (int d = 0; d < 5; ++d) {
      if (d < 2) xfirst(d);
      else
#pragma unroll
        for (int q = 0; q < 4; ++q) asm volatile("" : "=v"(xr[d][q & 1][q >> 1]));
    }
    asm volatile("" : "=v"(xa[0])); asm volatile("" : "=v"(xa[1]));
    unsigned aW[2][2][2];
#pragma unroll
    for (int s1 = 0; s1 < 2; ++s1)
#pragma unroll
      for (int T = 0; T < 2; ++T) {
        aW[0][s1][T] = lds0 + 3 * PIECE + (8 * g + tq) * 384 + (((4 * s1 + tp) ^ ((tq >> 1) | ((g & 1) << 2))) << 4) + 8 * (T ^ (tp & 1));
        aW[1][s1][T] = aW[0][s1][T] + 5 * PIECE;
        pin(aW[0][s1][T]); pin(aW[1][s1][T]);
      }
    f32x4 acc[12][2];
#pragma unroll
    for (int dt = 0; dt < 12; ++dt) { acc[dt][0] = f32x4{}; acc[dt][1] = f32x4{}; pin(acc[dt][0]); pin(acc[dt][1]); }
    auto adr = [&](int t, int K) { return aW[t / 5][(K >> 1) & 1][K & 1] + (t % 5) * PIECE + (K >> 2) * 128; };
    auto xwait = [&](int d) {
      waitvm<4>();
#pragma unroll
      for (int q = 0; q < 4; ++q) pin(xr[d][q & 1][q >> 1]);
      FENCE();
    };
    need(have, ardy, 1, wacc);
    STAMP(29);
#pragma unroll
    for (int k = 0; k < 7; ++k) { a[k] = trd2(adr(0, k), 1536); FENCE(); }
    xwait(0);
#pragma unroll
    for (int n = 0; n < 16; ++n) trade(0, n);
    if (XL == 3) xfirst(2);
    nops<2>();
    unsigned pos = 0, last = 14;
    auto step = [&](auto tc) {
      constexpr int t = decltype(tc)::value, T = t & 1, d = t >> 1, r = (d + XL) % 5;
#pragma unroll
      for (int k = 0; k < 12; ++k) {
        const int b = (12 * t + k) & 7, f = (b + 7) & 7;
        const int n = k < 3 ? 2 * k - 2 : k == 3 ? 4 : k == 4 ? 5 : 2 * k - 5;
        if (k == 5) { need(have, ardy, t < 9 ? pos + t + 2 : last, wacc); FENCE(); }
        if (T && k == 1) xwait((d + 1) % 5);
        const unsigned ra = k < 5 ? adr(t, k + 7) : adr((t + 1) % 10, k - 5);
        if (!(k & 1)) pin(a[(b + 1) & 7]);
        mfma(acc[k][0], a[b], T ? xr[d][1][0] : xa[0]); FENCE();
        trlo(a[f], ra);
        if (k == 10) { pin(pre); pre = cread(ardy); }
        if (k == 5) pre = bump<C_G1 + 4 * t>(c.ctl);
        if (T && k >= 1 && k <= 9 && k != 5) trade((d + 1) % 5, n);
        FENCE();
        mfma(acc[k][1], a[b], T ? xr[d][1][1] : xa[1]); FENCE();
        trhi(a[f], ra + 1536);
        if (k == 3) { have = rfl(pre); pins(have); }
        if (T && k >= 1 && k <= 10 && k != 3 && k != 5) trade((d + 1) % 5, k == 10 ? 15 : n + 1);
        if (XL == 2 ? !T && (k == 1 || k == 2 || k == 7 || k == 8) : T && (k == 3 || k == 5 || k >= 10)) {
          const int q = XL == 2 ? (k < 5 ? k - 1 : k - 5) : (k < 10 ? k >> 2 : k - 8);
          XLOAD("+v", xr[r][q & 1][q >> 1], vx[q >> 1], sx[(d + XL) & 1], d * 128 + (q & 1) * (DM * 2));
        }
        if (t == (XL == 2 ? 5 : 4) && k == 8) { srdX[2] = pos ? 0u : srdX[2]; pins(srdX); }
        FENCE();
      }
    };
#define IC(n) std::integral_constant<int, n>{}
#pragma unroll 1
    for (int ps = 0; ps < 2; ++ps) {
      step(IC(0)); step(IC(1)); step(IC(2)); step(IC(3));
      G1ST { STAMP(24 + 2 * ps); CSTAMP(50 + 2 * ps); }
      step(IC(4)); step(IC(5)); step(IC(6)); step(IC(7));
      G1ST { STAMP(25 + 2 * ps); CSTAMP(51 + 2 * ps); }
      step(IC(8)); step(IC(9));
      pos = 13 + XE; last = XE ? 24 + XE : 23;
      const unsigned u = sx[0]; sx[0] = sx[1] + 640u; sx[1] = u + 640u;
    }
    waitvm<0>();
#pragma unroll
    for (int d = 0; d < 3; ++d)
#pragma unroll
      for (int q = 0; q < 4; ++q) pin(xr[d][q & 1][q >> 1]);
    nops<8>();
    relane();
    const bool odd = g & 1;
#pragma unroll
    for (int i = 0; i < 2; ++i)
#pragma unroll
      for (int s = 0; s < 6; ++s) {
        const f32x4 t0 = acc[2 * s][i] * QSCALE, t1 = acc[2 * s + 1][i] * QSCALE;
        const unsigned u0 = cvtpk(t0[0], t0[1]), u1 = cvtpk(t0[2], t0[3]), u2 = cvtpk(t1[0], t1[1]), u3 = cvtpk(t1[2], t1[3]);
        const u32x4 w = {odd ? u2 : u0, odd ? u3 : u1, odd ? u0 : u2, odd ? u1 : u3};
        (i ? st6[s] : qf[s]) = __builtin_bit_cast(bf16x8, w);
      }
  }
  STAMP(1); PUT(40, wacc); CSTAMP(34);

  const int item = (c.br * 4 + c.bp) * 32 + c.h;
  const unsigned vl = (unsigned)lane * 16u;
  const unsigned adone = lds0 + CTL + C_DONE + wid * 256 + lane * 4;
  auto sibo = [&](int rel) { return (unsigned)WS_O + (unsigned)((item & ~3) | ((hq + rel) & 3)) * OBLK + (unsigned)wid * 12288u; };
  bf16x8 sa[3][6];
#pragma unroll
  for (int r = 0; r < 3; ++r)
#pragma unroll
    for (int q = 0; q < 6; ++q) asm volatile("" : "=v"(sa[r][q]));
  {
    unsigned bK[2], bV[4], aK[2], aV[4];
    bK[0] = l15 * 384 + ((g ^ (((l15 >> 1) & 3) << 1)) << 4);
    bK[1] = bK[0] ^ 64;
#pragma unroll
    for (int d4 = 0; d4 < 4; ++d4) bV[d4] = (4 * g + tq) * 384 + (((2 * d4 + (tp >> 1)) ^ (((2 * g + (tq >> 1)) & 3) << 1)) << 4) + 8 * (tp & 1);
    unsigned slot = lds0, gp = D_ATT;
    auto next = [&]() {
      seen(have, pre);
      need(have, ardy, ++gp, wacc);
      const unsigned s = slot;
      slot = slot == lds0 + (NP - 1) * PIECE ? lds0 : slot + PIECE;
      return s;
    };
    auto nextK = [&]() { const unsigned s = next(); aK[0] = bK[0] + s; aK[1] = bK[1] + s; };
    auto nextV = [&]() {
      const unsigned s = next();
#pragma unroll
      for (int d4 = 0; d4 < 4; ++d4) aV[d4] = bV[d4] + s;
    };
    f32x4 o[12], s[2], negm;
    float lsum, sc;
    int mx;
    u32x4 pf;
    bf16x8 w[8];
    bool resc;
    auto rdK = [&](int n) { w[n & 7] = ldsr128(aK[(n % 6) & 1] + ((n % 6) >> 1) * 128 + (n / 6) * 6144); };
    auto vadr = [&](int dt) { return aV[dt & 3] + (dt >> 2) * 128; };
    auto rda = [&](int n) { if (n < 12) rdK(n); else if (n < 24) w[n & 7] = trd2(vadr(n - 12), 6144); else rdK(n - 24); };
    auto mfa = [&](int n) {
      if (n >= 12) mfma(o[n - 12], w[n & 7], __builtin_bit_cast(bf16x8, pf));
      else if (n % 6) mfma(s[n / 6], w[n & 7], qf[n % 6]);
      else mfmac(s[n / 6], w[n & 7], qf[0], negm);
    };
    auto ex = [&](int e) { s[e >> 2][e & 3] = __builtin_amdgcn_exp2f(s[e >> 2][e & 3]); };
    auto ad = [&](int e) { lsum = addf(lsum, s[e >> 2][e & 3]); };
    auto cv = [&]() { pf[0] = cvtpk(s[0][0], s[0][1]); pf[1] = cvtpk(s[0][2], s[0][3]); pf[2] = cvtpk(s[1][0], s[1][1]); pf[3] = cvtpk(s[1][2], s[1][3]); pin(pf); };
    auto mxs = [&](int kt, int hf) { mx = max3i((kt | hf) ? mx : 0, s[kt][2 * hf], s[kt][2 * hf + 1]); };
    auto lanemax = [&]() { return fmaxf(fmaxf(fmaxf(s[0][0], s[0][1]), fmaxf(s[0][2], s[0][3])), fmaxf(fmaxf(s[1][0], s[1][1]), fmaxf(s[1][2], s[1][3]))); };
#define SLOT(n, A) do { mfa(n); FENCE(); rda(n + 8); A; FENCE(); } while (0)
#pragma unroll 1
    for (int it = 0; it < 2; ++it) {
      {
        nextK();
#pragma unroll
        for (int n = 0; n < 8; ++n) rdK(n);
        FENCE();
#pragma unroll
        for (int n = 0; n < 12; ++n) {
          if (n % 6) mfma(s[n / 6], w[n & 7], qf[n % 6]); else mfma0(s[n / 6], w[n & 7], qf[0]);
          FENCE();
          if (n < 4) rdK(n + 8);
          o[n] = f32x4{};
          FENCE();
        }
        cwrite(adone, gp);
        nextK();
#pragma unroll
        for (int n = 0; n < 8; ++n) rdK(n);
        nops<8>();
        const float m = xg_max(lanemax());
        negm = (f32x4){-m, -m, -m, -m};
        s[0] -= m; s[1] -= m;
        lsum = 0.f; sc = 1.f; resc = false;
#pragma unroll
        for (int e = 0; e < 8; ++e) ex(e);
#pragma unroll
        for (int e = 0; e < 8; ++e) ad(e);
        cv();
        nops<2>();
      }
#pragma unroll 1
      for (int st = 1; st < 16; ++st) {
        SLOT(0, pre = cread(ardy));
        SLOT(1, ); SLOT(2, ); SLOT(3, );
        nextV(); FENCE();
        SLOT(4, ); SLOT(5, );
        if (resc) {
#pragma unroll
          for (int dt = 0; dt < 12; ++dt) o[dt] *= sc;
        }
        FENCE();
        SLOT(6, ); SLOT(7, );
        SLOT(8, mxs(0, 0)); SLOT(9, mxs(0, 1));
        SLOT(10, ); SLOT(11, );
        SLOT(12, pre = cread(ardy));
        SLOT(13, );
        SLOT(14, mxs(1, 0); mxs(1, 1));
        resc = __any(mx > __builtin_bit_cast(int, RESC_THR));
        if (resc) {
          const float d = fmaxf(xg_max(lanemax()), 0.f);
          s[0] -= d; s[1] -= d; negm -= d;
          sc = __builtin_amdgcn_exp2f(-d);
          lsum *= sc;
        }
        FENCE();
        SLOT(15, ex(0));
        cwrite(adone, gp);
        if (st < 15) nextK();
        FENCE();
        SLOT(16, ex(1); ad(0)); SLOT(17, ex(2); ad(1)); SLOT(18, ex(3); ad(2)); SLOT(19, ex(4); ad(3));
        SLOT(20, ex(5); ad(4)); SLOT(21, ex(6); ad(5)); SLOT(22, ex(7); ad(6)); SLOT(23, ad(7));
        cv(); FENCE();
        if (it && st == 2) arrive(c, 0, flag0 + F_OA + (unsigned)c.h * 8u);
        if (it && st == SPF) {
          need(haveO, ardy + (C_RDYO - C_RDY), 1, wacc);
#pragma unroll
          for (int r = 0; r < 3; ++r) {
            const unsigned so = sibo(r + 1);
            bload_sc1_into<0>(sa[r][0], vl, c.srdWs, so); bload_sc1_into<1024>(sa[r][1], vl, c.srdWs, so); bload_sc1_into<2048>(sa[r][2], vl, c.srdWs, so);
            bload_sc1_into<0>(sa[r][3], vl, c.srdWs, so + 3072); bload_sc1_into<1024>(sa[r][4], vl, c.srdWs, so + 3072); bload_sc1_into<2048>(sa[r][5], vl, c.srdWs, so + 3072);
          }
        }
        FENCE();
        if (st == 1 || st == 8) { STAMP(2 + 4 * it + (st >> 3)); CSTAMP(56 + 2 * it + (st >> 3)); }
      }
      {
        const float inv = 1.f / xg_sum(lsum);
        const float fo = resc ? sc * inv : inv;
        s[0] *= inv; s[1] *= inv;
        cv();
        nops<4>();
#pragma unroll
        for (int dt = 0; dt < 12; ++dt) o[dt] *= fo;
        FENCE();
        nextV();
        STAMP(4 + 4 * it);
        bf16x8 v[12];
#pragma unroll
        for (int dt = 0; dt < 12; ++dt) v[dt] = trd2(vadr(dt), 6144);
        cwrite(adone, gp);
        FENCE();
#pragma unroll
        for (int dt = 0; dt < 12; ++dt) mfma(o[dt], v[dt], __builtin_bit_cast(bf16x8, pf));
        FENCE();
#pragma unroll
        for (int q = 0; q < 6; ++q) qf[q] = st6[q];
        FENCE();
        const unsigned so = (unsigned)WS_O + (unsigned)item * OBLK + (unsigned)wid * 12288u + (unsigned)it * 6144u;
#pragma unroll
        for (int q = 0; q < 6; ++q) st6[q] = pack2(o[2 * q], o[2 * q + 1]);
        {
        bstore_sc1<0>(st6[0], vl, c.srdWs, so); bstore_sc1<1024>(st6[1], vl, c.srdWs, so); bstore_sc1<2048>(st6[2], vl, c.srdWs, so);
        bstore_sc1<0>(st6[3], vl, c.srdWs, so + 3072); bstore_sc1<1024>(st6[4], vl, c.srdWs, so + 3072); bstore_sc1<2048>(st6[5], vl, c.srdWs, so + 3072);
        }
      }
      STAMP(5 + 4 * it);
    }
  }

  PUT(41, wacc);
  relane();
  bf16x8 sb[3][6];
  auto loadO = [&](int r, int s) {
    const unsigned so = sibo(r + 1) + 6144 + s * 2048;
    bload_sc1<0>(sb[r][2 * s], vl, c.srdWs, so); bload_sc1<1024>(sb[r][2 * s + 1], vl, c.srdWs, so);
    FENCE();
  };
  auto pinO = [&](int r, int s) { pin(sb[r][2 * s]); pin(sb[r][2 * s + 1]); FENCE(); };
  unsigned aO[2];
  aO[0] = lds0 + (4 * g + tq) * 320 + ((tp ^ (g & 1)) << 4) + 8 * (tp & 1);
  aO[1] = aO[0] ^ 8;
  f32x4 y[5][2][2];
  bf16x8 wf[8];
  auto wadr = [&](int n) { const int j = n / 20, k = n % 20; return aO[k & 1] + (j % NWS % 3) * WSLOT + (k / 10) * 10240 + ((k % 10) >> 1) * 64; };
  auto stage = [&](int j, const bf16x8 (&oA)[6], const bf16x8 (&oB)[6]) {
#pragma unroll
    for (int k = 0; k < 20; ++k) {
      const int n = 20 * j + k, sn = (j + 1) % NWS, dlt = sn % 3 ? 0 : sn ? 3 * WSLOT : -6 * WSLOT;
      if (k == 13 && j < 11) { need(have, ardy, D_G4 + j + 2, wacc); FENCE(); }
      if (!(k & 1)) pin(wf[(n + 1) & 7]);
#pragma unroll
      for (int i = 0; i < 2; ++i) {
        const bf16x8& of = (i ? oB : oA)[2 * (j % 3) + k / 10];
        if (n < 10) mfma0(y[(k % 10) >> 1][k & 1][i], wf[n & 7], of);
        else mfma(y[(k % 10) >> 1][k & 1][i], wf[n & 7], of);
        FENCE();
        if (n + 7 < 240) { if (i) trhi(wf[(n + 7) & 7], wadr(n + 7) + 5120); else trlo(wf[(n + 7) & 7], wadr(n + 7)); }
        if (i == 0 && k == 5 && j < 11) { pin(pre); pre = cread(ardy); }
        if (i == 1 && k == 12 && j < 11) { have = rfl(pre); pins(have); }
        if (i == 0 && k == 15 && j < 5) pre = j == 0 ? bump<C_G4>(c.ctl) : j == 1 ? bump<C_G4 + 4>(c.ctl) : j == 2 ? bump<C_G4 + 8>(c.ctl) : j == 3 ? bump<C_G4 + 12>(c.ctl) : bump<C_G4 + 16>(c.ctl);
        if (i == 0 && (k == 12 || k == 13) && dlt && j < 11) { aO[k & 1] += dlt; pin(aO[k & 1]); }
        FENCE();
      }
    }
  };
  need(have, ardy, D_G4 + 1, wacc);
#pragma unroll
  for (int n = 0; n < 7; ++n) wf[n] = trd2(wadr(n), 5120);
  FENCE();
  STAMP(10);
  stage(0, qf, st6);
  arrive(c, 1, flag0 + F_OB + (unsigned)c.h * 8u);
#pragma unroll
  for (int r = 0; r < 3; ++r)
#pragma unroll
    for (int q = 0; q < 6; ++q) pin(sa[r][q]);
  FENCE();
  STAMP(11);
  stage(1, qf, st6);
  stage(2, qf, st6);
  STAMP(12);
  need(haveO, ardy + (C_RDYO - C_RDY), 2, wacc);
  STAMP(13);
  loadO(0, 0); loadO(0, 1); loadO(0, 2); loadO(1, 0); loadO(1, 1); loadO(1, 2);
  STAMP(14);
  waitvm<10>(); pinO(0, 0); STAMP(15); stage(3, sa[0], sb[0]); loadO(2, 0);
  waitvm<10>(); pinO(0, 1); stage(4, sa[0], sb[0]); loadO(2, 1);
  waitvm<10>(); pinO(0, 2); stage(5, sa[0], sb[0]); loadO(2, 2);
  STAMP(16); CSTAMP(60);
  waitvm<6>(); pinO(1, 0); pinO(1, 1); pinO(1, 2); stage(6, sa[1], sb[1]); stage(7, sa[1], sb[1]); stage(8, sa[1], sb[1]);
  STAMP(17); CSTAMP(61);
  waitvm<0>(); pinO(2, 0); pinO(2, 1); pinO(2, 2); stage(9, sa[2], sb[2]); stage(10, sa[2], sb[2]); stage(11, sa[2], sb[2]);
  nops<8>();
  STAMP(20); PUT(42, wacc); CSTAMP(62);

  const int grp = (c.br * 4 + c.bp) * 4 + hq, k8 = c.h >> 2;
  {
    const unsigned so = (unsigned)WS_P + (unsigned)(grp * 8 + k8) * PBLK + (unsigned)wid * 10240u;
#pragma unroll
    for (int i = 0; i < 2; ++i) {
      bstore_sc1<0>(pack2(y[0][0][i], y[0][1][i]), vl, c.srdWs, so + i * 5120); bstore_sc1<1024>(pack2(y[1][0][i], y[1][1][i]), vl, c.srdWs, so + i * 5120);
      bstore_sc1<2048>(pack2(y[2][0][i], y[2][1][i]), vl, c.srdWs, so + i * 5120); bstore_sc1<3072>(pack2(y[3][0][i], y[3][1][i]), vl, c.srdWs, so + i * 5120);
      bstore_sc1<0>(pack2(y[4][0][i], y[4][1][i]), vl, c.srdWs, so + i * 5120 + 4096);
    }
  }
  relane();
  arrive(c, 2, flag0 + F_P + (unsigned)hq * 64u + (unsigned)k8 * 8u);
  STAMP(21);
  need(haveO, ardy + (C_RDYO - C_RDY), 3, wacc);
  STAMP(22);
  {
    bf16x8 pr[8];
    const unsigned so = (unsigned)WS_P + (unsigned)(grp * 8) * PBLK + (unsigned)wid * 10240u + (unsigned)k8 * 1024u;
#pragma unroll
    for (int k = 0; k < 8; ++k) bload_sc1<0>(pr[k], vl, c.srdWs, so + k * PBLK);
    waitvm<0>();
#pragma unroll
    for (int k = 0; k < 8; ++k) pin(pr[k]);
    const int i = k8 >= 5, cp = k8 - 5 * i;
    reduce_store(pr, c.out + (size_t)((2 * c.bp + i) * SQ + wid * 16 + l15) * (2 * DM) + c.br * DM + hq * 160 + cp * 32 + 8 * g, g & 1);
  }
  waitvm<0>();
  STAMP(23); CSTAMP(33);
  ((GAS unsigned*)(ws + (200u << 20)))[(blockIdx.x * 8 + wid) * 64 + lane] = stampv;
  asm volatile(".Lcode_end:");
}

typedef unsigned long long u64;
typedef u64 u64x2 __attribute__((ext_vector_type(2)));
typedef u64 u64x4 __attribute__((ext_vector_type(4)));
__global__ __launch_bounds__(512, 2) void fused_kernel(const u16* xA, const u16* kA, const u16* vA, const u16* wqA, const u16* woA,
                                                       const u16* xB, const u16* kB, const u16* vB, const u16* wqB, const u16* woB,
                                                       u16* out_, char* ws_) {
  extern __shared__ __attribute__((aligned(16))) char lds[];
  Ctx c;
  const int xcd = blockIdx.x & 7, idx = blockIdx.x >> 3;
  c.h = 8 * (xcd >> 1) + (idx & 7); c.bp = idx >> 3; c.br = xcd & 1;
  u64x4 p4; u64 p1; u64x2 p2;
  asm volatile("s_load_dwordx8 %0, %3, %4\n\ts_load_dwordx2 %1, %3, %4 offset:32\n\ts_load_dwordx4 %2, %3, 0x50"
               : "=&s"(p4), "=&s"(p1), "=&s"(p2) : "s"(__builtin_amdgcn_kernarg_segment_ptr()), "s"(c.br * 40) : "memory");
  c.lds0 = (unsigned)(uintptr_t)lds - CTL;
  c.ctl = c.lds0 + CTL;
  pins(c.ctl);
  c.wid = rfl(threadIdx.x >> 6);
  {
    unsigned long long pc; unsigned clen;
    asm volatile("s_getpc_b64 %0\n.Lcode_here:\n\ts_mov_b32 %1, .Lcode_end-.Lcode_here" : "=s"(pc), "=s"(clen));
    c.srdC = __builtin_amdgcn_make_buffer_rsrc((void*)pc, (short)0, (int)clen, 0x00020000);
    c.cshare = (unsigned)(idx & 31) * 128u;
    if (c.wid == 0) touch(c.lds0 + CTL + C_JUNK + 1024, threadIdx.x * 4096u, c.srdC, c.cshare);
  }
  cwrite(c.lds0 + CTL + threadIdx.x * 4, 0);
  {
    const int lane = threadIdx.x & 63;
#pragma unroll
    for (int k = 0; k < 3; ++k) {
      const int row = ((8 * k + (lane >> 3)) * 11) >> 5;
      c.vw[0][k] = (unsigned)(row * (HD * 2 - 384) + ((lane ^ ((row >> 1) & 1)) << 4));
      c.vw[1][k] = c.vw[0][k] ^ 64;
      c.vb[k] = (unsigned)((lane ^ (((row >> 1) & 3) << 1)) << 4);
      pin(c.vw[0][k]); pin(c.vw[1][k]); pin(c.vb[k]);
    }
  }
  asm volatile("s_waitcnt lgkmcnt(0)\n\ts_barrier" : "+s"(p4), "+s"(p1), "+s"(p2) :: "memory");
  const u16* x = (const u16*)p4[0]; const u16* kc = (const u16*)p4[1]; const u16* vc = (const u16*)p4[2]; const u16* wq = (const u16*)p4[3]; const u16* wo = (const u16*)p1;
  char* ws = (char*)p2[1];
  c.out = (GAS u16*)p2[0];
  const size_t kvoff = (size_t)(2 * c.bp * NH + c.h) * SK * DH;
  const u64 xb = (u64)(x + (size_t)c.bp * 2 * SQ * DM);
  c.srdX = u32x4{(unsigned)xb, (unsigned)(xb >> 32) & 0xFFFFu, 2 * SQ * DM * 2, 0x00020000};
  c.rot2 = ((c.bp + c.h) & 1) * 2;
  c.srdW = __builtin_amdgcn_make_buffer_rsrc((void*)(wq + c.h * DH), (short)0, (DM * HD - c.h * DH) * 2, 0x00020000);
  c.srdK = __builtin_amdgcn_make_buffer_rsrc((void*)(kc + kvoff), (short)0, KVITEM + SK * DH * 2, 0x00020000);
  c.srdV = __builtin_amdgcn_make_buffer_rsrc((void*)(vc + kvoff), (short)0, KVITEM + SK * DH * 2, 0x00020000);
  c.srdWo = __builtin_amdgcn_make_buffer_rsrc((void*)(wo + (size_t)(c.h >> 2) * 4 * DH * DM + (c.h & 3) * 160), (short)0, (4 * DH * DM - (c.h & 3) * 160) * 2, 0x00020000);
  c.srdWs = __builtin_amdgcn_make_buffer_rsrc((void*)ws, (short)0, (int)WS_END, 0x00020000);
  if (__builtin_expect(c.wid == 7, 1)) {
    __builtin_amdgcn_s_setprio(3);
    loader(c, ws);
    asm volatile("s_endpgm" ::: "memory");
  }
  consumer(c, ws);
}

extern "C" void kernel_launch(void* const* d_in, const int* in_sizes, int n_in, void* d_out, int out_size, void* d_ws, size_t ws_size, hipStream_t stream) {
  (void)in_sizes; (void)n_in; (void)out_size; (void)ws_size;
  (void)hipFuncSetAttribute((const void*)fused_kernel, hipFuncAttributeMaxDynamicSharedMemorySize, LDS_BYTES);
  fused_kernel<<<256, 512, LDS_BYTES, stream>>>((const u16*)d_in[0], (const u16*)d_in[1], (const u16*)d_in[2], (const u16*)d_in[3], (const u16*)d_in[4],
                                                (const u16*)d_in[5], (const u16*)d_in[6], (const u16*)d_in[7], (const u16*)d_in[8], (const u16*)d_in[9],
                                                (u16*)d_out, (char*)d_ws);
}
```

```cpp
#include <hip/hip_runtime.h>
#include <hip/hip_bf16.h>
#include <stdint.h>
#include <type_traits>
#pragma clang diagnostic ignored "-Winline-asm"

typedef unsigned short u16;
typedef short bf16x8 __attribute__((ext_vector_type(8)));
typedef short s16x4 __attribute__((ext_vector_type(4)));
typedef float f32x4 __attribute__((ext_vector_type(4)));
typedef float f32x2 __attribute__((ext_vector_type(2)));
typedef __bf16 bf16x2_t __attribute__((ext_vector_type(2)));
typedef unsigned u32x4 __attribute__((ext_vector_type(4)));
typedef unsigned u32x2 __attribute__((ext_vector_type(2)));
typedef __amdgpu_buffer_rsrc_t rsrc_t;
#define LAS __attribute__((address_space(3)))
#define GAS __attribute__((address_space(1)))
#define DI __device__ __forceinline__

constexpr int SQ = 112, DM = 640, NH = 32, SK = 512, DH = 192, HD = NH * DH;
constexpr int PIECE = 12288, NP = 13;
constexpr int WSLOT = 20480, NWS = 7;
constexpr int CTL = -4096;
constexpr int C_G1 = 0, C_G4 = 40, C_RDY = 64, C_RDYO = 68;
constexpr int C_TAG = 72, C_CNT = 80, C_DONE = 256, C_JUNK = 2048;
constexpr int LDS_BYTES = 163840;
constexpr unsigned FLAG_BYTES = 8192;
constexpr unsigned OBLK = 7 * 12 * 1024;
constexpr unsigned PBLK = 7 * 10 * 1024;
constexpr size_t WS_O = FLAG_BYTES;
constexpr size_t WS_P = WS_O + (size_t)256 * OBLK;
constexpr size_t WS_END = WS_P + (size_t)256 * PBLK;
constexpr float QSCALE = 0.07216878364870322f * 1.4426950408889634f;
constexpr float RESC_THR = 8.0f;
constexpr unsigned KVITEM = NH * SK * DH * 2;
constexpr int D_ATT = 20, D_G4 = 84;
#define NPRE 5
#define SPF 14
#define PFL 52
#define XL 2
#define XE 4

DI void mfma(f32x4& d, const bf16x8& a, const bf16x8& b) { asm("v_mfma_f32_16x16x32_bf16 %0, %1, %2, %0" : "+v"(d) : "v"(a), "v"(b)); }
DI void mfmac(f32x4& d, const bf16x8& a, const bf16x8& b, const f32x4& c) { asm("v_mfma_f32_16x16x32_bf16 %0, %1, %2, %3" : "=v"(d) : "v"(a), "v"(b), "v"(c)); }
DI void mfma0(f32x4& d, const bf16x8& a, const bf16x8& b) { asm("v_mfma_f32_16x16x32_bf16 %0, %1, %2, 0" : "=v"(d) : "v"(a), "v"(b)); }
#define FENCE() __builtin_amdgcn_sched_barrier(0)
template <int N> DI void nops() { FENCE(); asm volatile("s_nop %0" :: "i"(N - 1)); FENCE(); }
DI int lane_id() { int l; asm volatile("v_mbcnt_lo_u32_b32 %0, -1, 0\n\tv_mbcnt_hi_u32_b32 %0, -1, %0" : "=v"(l)); return l; }
DI unsigned rfl(unsigned x) { return __builtin_amdgcn_readfirstlane(x); }
DI unsigned cvtpk(float lo, float hi) { f32x2 v = {lo, hi}; bf16x2_t b = __builtin_convertvector(v, bf16x2_t); return __builtin_bit_cast(unsigned, b); }
DI s16x4 trd(unsigned a) { return __builtin_bit_cast(s16x4, __builtin_amdgcn_ds_read_tr16_b64_v4i16((LAS s16x4*)a)); }
DI void trlo(bf16x8& d, unsigned a) { const s16x4 t = trd(a); d[0] = t[0]; d[1] = t[1]; d[2] = t[2]; d[3] = t[3]; }
DI void trhi(bf16x8& d, unsigned a) { const s16x4 t = trd(a); d[4] = t[0]; d[5] = t[1]; d[6] = t[2]; d[7] = t[3]; }
DI bf16x8 trd2(unsigned a, unsigned d) { s16x4 lo = trd(a), hi = trd(a + d); return (bf16x8){lo[0], lo[1], lo[2], lo[3], hi[0], hi[1], hi[2], hi[3]}; }
DI bf16x8 ldsr128(unsigned a) { return *(const LAS bf16x8*)a; }
DI bf16x8 pack2(const f32x4& a, const f32x4& b) {
  u32x4 w = {cvtpk(a[0], a[1]), cvtpk(a[2], a[3]), cvtpk(b[0], b[1]), cvtpk(b[2], b[3])};
  return __builtin_bit_cast(bf16x8, w);
}
DI float bflo(unsigned w) { return __uint_as_float(w << 16); }
DI float bfhi(unsigned w) { return __uint_as_float(w & 0xFFFF0000u); }
DI float addf(float a, float b) { float d = a + b; asm("" : "+v"(d)); return d; }
DI int max3i(int a, float b, float c) { return max(max(a, __float_as_int(b)), __float_as_int(c)); }
DI f32x2 sw16(float v) { const unsigned u = __float_as_uint(v); const auto r = __builtin_amdgcn_permlane16_swap(u, u, false, false); return f32x2{__uint_as_float(r[0]), __uint_as_float(r[1])}; }
DI f32x2 sw32(float v) { const unsigned u = __float_as_uint(v); const auto r = __builtin_amdgcn_permlane32_swap(u, u, false, false); return f32x2{__uint_as_float(r[0]), __uint_as_float(r[1])}; }
DI float xg_max(float v) { f32x2 r = sw16(v); r = sw32(fmaxf(r[0], r[1])); return fmaxf(r[0], r[1]); }
DI float xg_sum(float v) { f32x2 r = sw16(v); r = sw32(r[0] + r[1]); return r[0] + r[1]; }

#define KVPOL " nt"
DI void dma4(unsigned m0v, unsigned v0, unsigned v1, unsigned v2, unsigned v3, rsrc_t srd, unsigned soff) {
  asm volatile("s_mov_b32 m0, %0\n\ts_nop 0\n\t"
               "buffer_load_dwordx4 %1, %5, %6 offen lds\n\t"
               "buffer_load_dwordx4 %2, %5, %6 offen offset:1024 lds\n\t"
               "buffer_load_dwordx4 %3, %5, %6 offen offset:2048 lds\n\t"
               "buffer_load_dwordx4 %4, %5, %6 offen offset:3072 lds"
               :: "s"(m0v), "v"(v0), "v"(v1), "v"(v2), "v"(v3), "s"(srd), "s"(soff) : "m0", "memory");
}
DI void dma3k(unsigned m0v, unsigned v0, unsigned v1, unsigned v2, rsrc_t srd, unsigned soff) {
  asm volatile("s_mov_b32 m0, %0\n\ts_nop 0\n\t"
               "buffer_load_dwordx4 %1, %4, %5 offen" KVPOL " lds\n\t"
               "buffer_load_dwordx4 %2, %4, %5 offen offset:1024" KVPOL " lds\n\t"
               "buffer_load_dwordx4 %3, %4, %5 offen offset:2048" KVPOL " lds"
               :: "s"(m0v), "v"(v0), "v"(v1), "v"(v2), "s"(srd), "s"(soff) : "m0", "memory");
}
DI void dma3(unsigned m0v, unsigned v0, unsigned v1, unsigned v2, rsrc_t srd, unsigned soff) {
  asm volatile("s_mov_b32 m0, %0\n\ts_nop 0\n\t"
               "buffer_load_dwordx4 %1, %4, %5 offen lds\n\t"
               "buffer_load_dwordx4 %2, %4, %5 offen offset:1024 lds\n\t"
               "buffer_load_dwordx4 %3, %4, %5 offen offset:2048 lds"
               :: "s"(m0v), "v"(v0), "v"(v1), "v"(v2), "s"(srd), "s"(soff) : "m0", "memory");
}
DI void touch(unsigned m0v, unsigned v, rsrc_t srd, unsigned soff) {
  asm volatile("s_mov_b32 m0, %0\n\ts_nop 0\n\tbuffer_load_dword %1, %2, %3 offen lds" :: "s"(m0v), "v"(v), "s"(srd), "s"(soff) : "m0", "memory");
}
template <int OFF> DI void bload_sc1(bf16x8& d, unsigned v, rsrc_t srd, unsigned soff) {
  asm volatile("buffer_load_dwordx4 %0, %1, %2, %3 offen offset:%4 sc1" : "=v"(d) : "v"(v), "s"(srd), "s"(soff), "i"(OFF) : "memory");
}
template <int OFF> DI void bload_sc1_into(bf16x8& d, unsigned v, rsrc_t srd, unsigned soff) {
  asm volatile("buffer_load_dwordx4 %0, %1, %2, %3 offen offset:%4 sc1" : "+v"(d) : "v"(v), "s"(srd), "s"(soff), "i"(OFF) : "memory");
}
template <int OFF> DI void bstore_sc1(const bf16x8& d, unsigned v, rsrc_t srd, unsigned soff) {
  asm volatile("buffer_store_dwordx4 %0, %1, %2, %3 offen offset:%4 sc1\n\ts_nop 1" :: "v"(d), "v"(v), "s"(srd), "s"(soff), "i"(OFF) : "memory");
}
template <int N> DI void waitvm() { asm volatile("s_waitcnt vmcnt(%0)" :: "i"(N) : "memory"); }
template <typename T> DI void pin(T& x) { asm volatile("" : "+v"(x)); }
template <typename T> DI void pins(T& x) { asm volatile("" : "+s"(x)); }
DI void cbar() { asm volatile("" ::: "memory"); }

#define STAMP(i)
#define CSTAMP(i)
#define PUT(i, v)
#define WACC_BEGIN
#define WACC_END(w)

#define G1ST
struct Ctx {
  rsrc_t srdW, srdK, srdV, srdWo, srdWs, srdC;
  u32x4 srdX;
  GAS u16* out;
  unsigned lds0, ctl, cshare;
  unsigned vw[2][3], vb[3];
  int wid, br, bp, h, rot2;
};

DI unsigned cread(unsigned a) { return *(const volatile LAS unsigned*)a; }
DI void cwrite(unsigned a, unsigned v) { *(volatile LAS unsigned*)a = v; }
template <int A> DI unsigned bump(unsigned z) { cbar(); const unsigned r = __builtin_amdgcn_ds_append((LAS int*)z + A / 4); cbar(); return r; }
DI void need(unsigned& have, unsigned a, unsigned n, unsigned& wacc) {
  n *= 64;
  if (__builtin_expect(have < n, 0)) {
    WACC_BEGIN;
#pragma unroll 1
    for (unsigned spins = 0;; ++spins) {
      have = rfl(cread(a));
      if (have >= n) break;
      if (spins > (1u << 20)) { have = ~0u; break; }
      __builtin_amdgcn_s_sleep(1);
    }
    WACC_END(wacc);
  }
  cbar();
}
DI void seen(unsigned& have, unsigned pre) { have = max(have, rfl(pre)); }

struct Tag { unsigned lo, hi; };
DI bool match(const u32x2& f, Tag tag) { return __all(f[0] == tag.lo && f[1] == tag.hi); }
DI void poll_flags(rsrc_t srdWs, unsigned vo, Tag tag) {
#pragma unroll 1
  for (unsigned spins = 0; spins < (1u << 20); ++spins) {
    u32x2 f;
    asm volatile("buffer_load_dwordx2 %0, %1, %2, 0 offen sc1\n\ts_waitcnt vmcnt(0)" : "=v"(f) : "v"(vo), "s"(srdWs) : "memory");
    if (match(f, tag)) break;
    __builtin_amdgcn_s_sleep(2);
  }
}
DI void set_flag(rsrc_t srdWs, unsigned off, int lane, Tag tag) {
  if (lane == 0) { const u32x2 t = {tag.lo, tag.hi}; asm volatile("buffer_store_dwordx2 %0, off, %1, %2 sc1\n\ts_nop 1" :: "v"(t), "s"(srdWs), "s"(off) : "memory"); }
}
DI void arrive(const Ctx& c, int which, unsigned off) {
  waitvm<0>();
  const int lane = lane_id();
  unsigned old = 0;
  if (lane == 0) old = __hip_atomic_fetch_add((LAS unsigned*)(c.lds0 + CTL + C_CNT + 4 * which), 1u, __ATOMIC_RELAXED, __HIP_MEMORY_SCOPE_WORKGROUP);
  if (rfl(old) == 6) {
    const Tag tag{rfl(cread(c.lds0 + CTL + C_TAG)), rfl(cread(c.lds0 + CTL + C_TAG + 4))};
    set_flag(c.srdWs, off, lane, tag);
  }
}

DI void reduce_store(const bf16x8 (&pr)[8], GAS u16* dst, bool odd) {
  float a[8] = {0.f, 0.f, 0.f, 0.f, 0.f, 0.f, 0.f, 0.f};
#pragma unroll
  for (int k = 0; k < 8; ++k) {
    const u32x4 w = __builtin_bit_cast(u32x4, pr[k]);
#pragma unroll
    for (int e = 0; e < 4; ++e) { a[2 * e] += bflo(w[e]); a[2 * e + 1] += bfhi(w[e]); }
  }
  const unsigned u0 = cvtpk(a[0], a[1]), u1 = cvtpk(a[2], a[3]), u2 = cvtpk(a[4], a[5]), u3 = cvtpk(a[6], a[7]);
  const u32x4 w = {odd ? u2 : u0, odd ? u3 : u1, odd ? u0 : u2, odd ? u1 : u3};
  *(GAS u32x4*)dst = w;
}
constexpr unsigned F_OA = 256, F_OB = 512, F_P = 768;

DI void loader(const Ctx& c, char* ws) {
  const int lane = lane_id();
  const unsigned lds0 = c.lds0;
  const int hq = c.h & 3;
  STAMP(0); CSTAMP(32);
  int dv = 0, gv = 0;
  unsigned wacc = 1;
  const unsigned adone = lds0 + CTL + C_DONE + (lane < 7 ? lane : 0) * 260;
  auto gate = [&](int p) {
    if (!__all(dv >= p)) {
      WACC_BEGIN;
#pragma unroll 1
      for (unsigned spins = 0;; ++spins) {
        dv = cread(adone);
        if (__all(dv >= p)) break;
        if (spins > (1u << 20)) { dv = 0x7FFFFFFF; break; }
        __builtin_amdgcn_s_sleep(1);
      }
      WACC_END(wacc);
    }
    cbar();
  };
  auto gatec = [&](int i, int n) {
    n *= 7 * 64;
    if (__builtin_amdgcn_readlane(gv, i) < n) {
      WACC_BEGIN;
#pragma unroll 1
      for (unsigned spins = 0;; ++spins) {
        gv = cread(lds0 + CTL + lane * 4);
        if (__builtin_amdgcn_readlane(gv, i) >= n) break;
        if (spins > (1u << 20)) { gv = 0x7FFFFFFF; break; }
        __builtin_amdgcn_s_sleep(1);
      }
      WACC_END(wacc);
    }
    cbar();
  };
  const unsigned (&vw)[2][3] = c.vw; const unsigned (&vb)[3] = c.vb;
  auto wpieces = [&](int t0) {
    unsigned m = lds0 + 3 * PIECE;
#pragma unroll 1
    for (int t = t0; t < t0 + 10; ++t) {
      if (t0) gatec(t - t0, 1);
      const unsigned so = (unsigned)(((t ^ c.rot2) >> 1) * 64 + (t & 1) * 8) * (HD * 2);
#pragma unroll
      for (int q = 0; q < 4; ++q) dma3(m + 3072 * q, vw[q & 1][0], vw[q & 1][1], vw[q & 1][2], c.srdW, so + q * (16 * HD * 2));
      m += PIECE;
      waitvm<36>(); if (t >= 3) bump<C_RDY>(c.ctl);
    }
  };
  const unsigned flag0 = (unsigned)(c.br * 4 + c.bp) * 1024u;
  const unsigned vtc = (unsigned)lane * 4096u;
  unsigned mslot = lds0;
  auto pieces = [&](int p0, int p1, bool tc) {
#pragma unroll 1
    for (int p = p0; p < p1; ++p) {
      const int q = p & 31;
      const bool isv = ((q & 1) == 0 && q) || q == 31;
      const unsigned so = (unsigned)((isv ? q - 1 : q + 1) >> 1) * PIECE + (unsigned)(p >> 5) * KVITEM;
      const rsrc_t srd = isv ? c.srdV : c.srdK;
      if (p0 == 3) gatec(p - 3, 2); else if (p0) gate(p + 8);
#pragma unroll
      for (int k = 0; k < 4; ++k) dma3k(mslot + 3072 * k, vb[0], vb[1], vb[2], srd, so + 3072 * k);
      mslot = mslot == lds0 + (NP - 1) * PIECE ? lds0 : mslot + PIECE;
      waitvm<36>(); bump<C_RDY>(c.ctl);
      if ((p & 7) == 7) { STAMP(10 + (p >> 3)); PUT(42 + (p >> 3), wacc); }
      if (tc && (p & 1) && p < 60) touch(lds0 + CTL + C_JUNK, vtc, c.srdC, c.cshare);
    }
  };
  wpieces(0);
  STAMP(24);
  u32x2 f;
  asm volatile("buffer_load_dwordx2 %0, off, %1, %2 sc1" : "=v"(f) : "s"(c.srdWs), "s"(flag0) : "memory");
  pieces(0, 3, false);
  STAMP(25);
  wpieces(10);
  STAMP(26);
  pin(f);
  const unsigned long long tg = (((unsigned long long)rfl(f[1]) << 32) | rfl(f[0])) + 1ull;
  const Tag tag{(unsigned)tg, (unsigned)(tg >> 32)};
  pieces(3, NP, false);
  STAMP(1); PUT(40, wacc); CSTAMP(34);
  if (lane == 0) { cwrite(lds0 + CTL + C_TAG, tag.lo); cwrite(lds0 + CTL + C_TAG + 4, tag.hi); }
  pieces(NP, PFL, false);
  const unsigned vsib = flag0 + (unsigned)(c.h >> 2) * 32u + (unsigned)(lane & 3) * 8u;
  asm volatile("buffer_load_dwordx2 %0, %1, %2, 0 offen offset:%3 sc1" : "=v"(f) : "v"(vsib), "s"(c.srdWs), "i"(F_OA) : "memory");
  pieces(PFL, PFL + 4, false);
  pin(f);
  if (!match(f, tag)) poll_flags(c.srdWs, vsib + F_OA, tag);
  bump<C_RDYO>(c.ctl);
  STAMP(8);
  pieces(PFL + 4, 64, true);
  STAMP(2); PUT(41, wacc);
  unsigned vo[5];
#pragma unroll
  for (int k = 0; k < 5; ++k) {
    const int row = ((16 * k + (lane >> 2)) * 205) >> 10;
    vo[k] = (unsigned)(row * (DM * 2 - 320) + ((lane ^ ((row >> 2) & 1)) << 4) + (k == 4 ? 4096 : 0));
  }
  unsigned cwo = hq * 3;
  mslot = lds0;
  auto issueWo = [&](int j) {
    if (j < NWS) gate(D_ATT + 53 + (int)((0xB986431u >> (4 * j)) & 15)); else gatec(C_G4 / 4 + j - NWS, 1);
#pragma unroll
    for (int q = 0; q < 4; ++q) {
      const unsigned so = cwo * (64 * DM * 2) + q * (16 * DM * 2);
      dma4(mslot + 5120 * q, vo[0], vo[1], vo[2], vo[3], c.srdWo, so);
      asm volatile("s_mov_b32 m0, %0\n\ts_nop 0\n\tbuffer_load_dwordx4 %1, %2, %3 offen lds" :: "s"(mslot + 5120 * q + 4096), "v"(vo[4]), "s"(c.srdWo), "s"(so) : "m0", "memory");
    }
    cwo = cwo == 11 ? 0 : cwo + 1;
    mslot = mslot == lds0 + (NWS - 1) * WSLOT ? lds0 : mslot + WSLOT;
  };
#pragma unroll 1
  for (int j = 0; j < 12; ++j) {
    issueWo(j);
    if (j == 0) { waitvm<44>(); bump<C_RDY>(c.ctl); continue; }
    if (j == 1) { waitvm<52>(); bump<C_RDY>(c.ctl); }
    waitvm<40>(); if (j < NPRE || j > NPRE + 1) bump<C_RDY>(c.ctl);
    if (j == 1) STAMP(3);
    if (j == NPRE - 1 || j == 11) {
      waitvm<20>(); bump<C_RDY>(c.ctl);
      waitvm<0>(); bump<C_RDY>(c.ctl);
      if (j == 11) break;
      STAMP(4);
      poll_flags(c.srdWs, vsib + F_OB, tag);
      bump<C_RDYO>(c.ctl);
      STAMP(5);
    }
  }
  STAMP(6);
  poll_flags(c.srdWs, flag0 + F_P + (unsigned)hq * 64u + (unsigned)(lane & 7) * 8u, tag);
  bump<C_RDYO>(c.ctl);
  if (c.h == 0) set_flag(c.srdWs, flag0, lane, tag);
  STAMP(7);
  const int k8 = c.h >> 2;
  if (k8 < 7) {
    const unsigned so = (unsigned)WS_P + (unsigned)(((c.br * 4 + c.bp) * 4 + hq) * 8) * PBLK + (unsigned)k8 * 10240u + 8192u;
    bf16x8 pr[2][8];
#pragma unroll
    for (int k = 0; k < 8; ++k) { bload_sc1<0>(pr[0][k], lane * 16, c.srdWs, so + k * PBLK); bload_sc1<1024>(pr[1][k], lane * 16, c.srdWs, so + k * PBLK); }
    waitvm<0>();
#pragma unroll
    for (int p = 0; p < 2; ++p) {
#pragma unroll
      for (int k = 0; k < 8; ++k) pin(pr[p][k]);
      reduce_store(pr[p], c.out + (size_t)((2 * c.bp + 1) * SQ + k8 * 16 + (lane & 15)) * (2 * DM) + c.br * DM + hq * 160 + (3 + p) * 32 + 8 * (lane >> 4), (lane >> 4) & 1);
    }
  }
}

DI void consumer(const Ctx& c, char* ws) {
  const int wid = c.wid;
  int lane, l15, g, tq, tp;
  auto relane = [&]() { lane = lane_id(); l15 = lane & 15; g = lane >> 4; tq = l15 >> 2; tp = lane & 3; };
  relane();
  const unsigned lds0 = c.lds0;
  STAMP(0); CSTAMP(32);
  const int hq = c.h & 3;
  const unsigned flag0 = (unsigned)(c.br * 4 + c.bp) * 1024u;
  unsigned ardy = lds0 + CTL + C_RDY;
  pin(ardy);
  unsigned have = 0, pre = 0, haveO = 0, wacc = 1;
  __builtin_amdgcn_s_setprio(1);

  bf16x8 qf[6], st6[6];
  {
    bf16x8 xr[5][2][2], xa[2], a[8];
    unsigned vx[2];
    vx[0] = (unsigned)((wid * 16 + (l15 & 14)) * (DM * 2) + g * 32 + (lane & 1) * 16);
    vx[1] = vx[0] + SQ * DM * 2;
    u32x4 srdX = c.srdX;
    unsigned sx[2] = {(unsigned)c.rot2 * 64u + XL * 128u, XL * 128u - (unsigned)c.rot2 * 64u};
#define XLOAD(C, D, V, S, OFF) asm volatile("buffer_load_dwordx4 %0, %1, %2, %3 offen offset:%4" : C(D) : "v"(V), "s"(srdX), "s"(S), "i"(OFF) : "memory")
    auto xfirst = [&](int d) {
#pragma unroll
      for (int q = 0; q < 4; ++q) XLOAD("=v", xr[d][q & 1][q >> 1], vx[q >> 1], sx[d & 1] - XL * 128u + (d & 1 ? 128u : 0u), (d & ~1) * 128 + (q & 1) * (DM * 2));
    };
    unsigned long long even = 0x5555555555555555ull;
    pins(even);
    auto trade = [&](int d, int n) {
      const int i = (n >> 2) & 1, e = n & 3;
      u32x4 r0 = __builtin_bit_cast(u32x4, xr[d][0][i]), r1 = __builtin_bit_cast(u32x4, xr[d][1][i]), t = __builtin_bit_cast(u32x4, xa[i]);
#define XSEL "v_cndmask_b32_dpp %0, %1, %2, vcc quad_perm:[1,0,3,2] row_mask:0xf bank_mask:0xf"
      register unsigned long long m asm("vcc") = n < 8 ? even : ~even;
      if (n < 8) { asm volatile(XSEL : "=v"(t[e]) : "v"(r1[e]), "v"(r0[e]), "s"(m)); xa[i] = __builtin_bit_cast(bf16x8, t); }
      else { asm volatile(XSEL : "=v"(r1[e]) : "v"(r0[e]), "0"(r1[e]), "s"(m)); xr[d][1][i] = __builtin_bit_cast(bf16x8, r1); }
    };
#pragma unroll
    for (int d = 0; d < 5; ++d) {
      if (d < 2) xfirst(d);
      else
#pragma unroll
        for (int q = 0; q < 4; ++q) asm volatile("" : "=v"(xr[d][q & 1][q >> 1]));
    }
    asm volatile("" : "=v"(xa[0])); asm volatile("" : "=v"(xa[1]));
    unsigned aW[2][2][2];
#pragma unroll
    for (int s1 = 0; s1 < 2; ++s1)
#pragma unroll
      for (int T = 0; T < 2; ++T) {
        aW[0][s1][T] = lds0 + 3 * PIECE + (8 * g + tq) * 384 + (((4 * s1 + tp) ^ ((tq >> 1) | ((g & 1) << 2))) << 4) + 8 * (T ^ (tp & 1));
        aW[1][s1][T] = aW[0][s1][T] + 5 * PIECE;
        pin(aW[0][s1][T]); pin(aW[1][s1][T]);
      }
    f32x4 acc[12][2];
#pragma unroll
    for (int dt = 0; dt < 12; ++dt) { acc[dt][0] = f32x4{}; acc[dt][1] = f32x4{}; pin(acc[dt][0]); pin(acc[dt][1]); }
    auto adr = [&](int t, int K) { return aW[t / 5][(K >> 1) & 1][K & 1] + (t % 5) * PIECE + (K >> 2) * 128; };
    auto xwait = [&](int d) {
      waitvm<4>();
#pragma unroll
      for (int q = 0; q < 4; ++q) pin(xr[d][q & 1][q >> 1]);
      FENCE();
    };
    need(have, ardy, 1, wacc);
    STAMP(29);
#pragma unroll
    for (int k = 0; k < 7; ++k) { a[k] = trd2(adr(0, k), 1536); FENCE(); }
    xwait(0);
#pragma unroll
    for (int n = 0; n < 16; ++n) trade(0, n);
    if (XL == 3) xfirst(2);
    nops<2>();
    unsigned pos = 0, last = 14;
    auto step = [&](auto tc) {
      constexpr int t = decltype(tc)::value, T = t & 1, d = t >> 1, r = (d + XL) % 5;
#pragma unroll
      for (int k = 0; k < 12; ++k) {
        const int b = (12 * t + k) & 7, f = (b + 7) & 7;
        const int n = k < 3 ? 2 * k - 2 : k == 3 ? 4 : k == 4 ? 5 : 2 * k - 5;
        if (k == 5) { need(have, ardy, t < 9 ? pos + t + 2 : last, wacc); FENCE(); }
        if (T && k == 1) xwait((d + 1) % 5);
        const unsigned ra = k < 5 ? adr(t, k + 7) : adr((t + 1) % 10, k - 5);
        if (!(k & 1)) pin(a[(b + 1) & 7]);
        mfma(acc[k][0], a[b], T ? xr[d][1][0] : xa[0]); FENCE();
        trlo(a[f], ra);
        if (k == 10) { pin(pre); pre = cread(ardy); }
        if (k == 5) pre = bump<C_G1 + 4 * t>(c.ctl);
        if (T && k >= 1 && k <= 9 && k != 5) trade((d + 1) % 5, n);
        FENCE();
        mfma(acc[k][1], a[b], T ? xr[d][1][1] : xa[1]); FENCE();
        trhi(a[f], ra + 1536);
        if (k == 3) { have = rfl(pre); pins(have); }
        if (T && k >= 1 && k <= 10 && k != 3 && k != 5) trade((d + 1) % 5, k == 10 ? 15 : n + 1);
        if (XL == 2 ? !T && (k == 1 || k == 2 || k == 7 || k == 8) : T && (k == 3 || k == 5 || k >= 10)) {
          const int q = XL == 2 ? (k < 5 ? k - 1 : k - 5) : (k < 10 ? k >> 2 : k - 8);
          XLOAD("+v", xr[r][q & 1][q >> 1], vx[q >> 1], sx[(d + XL) & 1], d * 128 + (q & 1) * (DM * 2));
        }
        if (t == (XL == 2 ? 5 : 4) && k == 8) { srdX[2] = pos ? 0u : srdX[2]; pins(srdX); }
        FENCE();
      }
    };
#define IC(n) std::integral_constant<int, n>{}
#pragma unroll 1
    for (int ps = 0; ps < 2; ++ps) {
      step(IC(0)); step(IC(1)); step(IC(2)); step(IC(3));
      G1ST { STAMP(24 + 2 * ps); CSTAMP(50 + 2 * ps); }
      step(IC(4)); step(IC(5)); step(IC(6)); step(IC(7));
      G1ST { STAMP(25 + 2 * ps); CSTAMP(51 + 2 * ps); }
      step(IC(8)); step(IC(9));
      pos = 13 + XE; last = XE ? 24 + XE : 23;
      const unsigned u = sx[0]; sx[0] = sx[1] + 640u; sx[1] = u + 640u;
    }
    waitvm<0>();
#pragma unroll
    for (int d = 0; d < 3; ++d)
#pragma unroll
      for (int q = 0; q < 4; ++q) pin(xr[d][q & 1][q >> 1]);
    nops<8>();
    relane();
    const bool odd = g & 1;
#pragma unroll
    for (int i = 0; i < 2; ++i)
#pragma unroll
      for (int s = 0; s < 6; ++s) {
        const f32x4 t0 = acc[2 * s][i] * QSCALE, t1 = acc[2 * s + 1][i] * QSCALE;
        const unsigned u0 = cvtpk(t0[0], t0[1]), u1 = cvtpk(t0[2], t0[3]), u2 = cvtpk(t1[0], t1[1]), u3 = cvtpk(t1[2], t1[3]);
        const u32x4 w = {odd ? u2 : u0, odd ? u3 : u1, odd ? u0 : u2, odd ? u1 : u3};
        (i ? st6[s] : qf[s]) = __builtin_bit_cast(bf16x8, w);
      }
  }
  STAMP(1); PUT(40, wacc); CSTAMP(34);

  const int item = (c.br * 4 + c.bp) * 32 + c.h;
  const unsigned vl = (unsigned)lane * 16u;
  const unsigned adone = lds0 + CTL + C_DONE + wid * 256 + lane * 4;
  auto sibo = [&](int rel) { return (unsigned)WS_O + (unsigned)((item & ~3) | ((hq + rel) & 3)) * OBLK + (unsigned)wid * 12288u; };
  bf16x8 sa[3][6];
#pragma unroll
  for (int r = 0; r < 3; ++r)
#pragma unroll
    for (int q = 0; q < 6; ++q) asm volatile("" : "=v"(sa[r][q]));
  {
    unsigned bK[2], bV[4], aK[2], aV[4];
    bK[0] = l15 * 384 + ((g ^ (((l15 >> 1) & 3) << 1)) << 4);
    bK[1] = bK[0] ^ 64;
#pragma unroll
    for (int d4 = 0; d4 < 4; ++d4) bV[d4] = (4 * g + tq) * 384 + (((2 * d4 + (tp >> 1)) ^ (((2 * g + (tq >> 1)) & 3) << 1)) << 4) + 8 * (tp & 1);
    unsigned slot = lds0, gp = D_ATT;
    auto next = [&]() {
      seen(have, pre);
      need(have, ardy, ++gp, wacc);
      const unsigned s = slot;
      slot = slot == lds0 + (NP - 1) * PIECE ? lds0 : slot + PIECE;
      return s;
    };
    auto nextK = [&]() { const unsigned s = next(); aK[0] = bK[0] + s; aK[1] = bK[1] + s; };
    auto nextV = [&]() {
      const unsigned s = next();
#pragma unroll
      for (int d4 = 0; d4 < 4; ++d4) aV[d4] = bV[d4] + s;
    };
    f32x4 o[12], s[2], negm;
    float lsum, sc;
    int mx;
    u32x4 pf;
    bf16x8 w[8];
    bool resc;
    auto rdK = [&](int n) { w[n & 7] = ldsr128(aK[(n % 6) & 1] + ((n % 6) >> 1) * 128 + (n / 6) * 6144); };
    auto vadr = [&](int dt) { return aV[dt & 3] + (dt >> 2) * 128; };
    auto rda = [&](int n) { if (n < 12) rdK(n); else if (n < 24) w[n & 7] = trd2(vadr(n - 12), 6144); else rdK(n - 24); };
    auto mfa = [&](int n) {
      if (n >= 12) mfma(o[n - 12], w[n & 7], __builtin_bit_cast(bf16x8, pf));
      else if (n % 6) mfma(s[n / 6], w[n & 7], qf[n % 6]);
      else mfmac(s[n / 6], w[n & 7], qf[0], negm);
    };
    auto ex = [&](int e) { s[e >> 2][e & 3] = __builtin_amdgcn_exp2f(s[e >> 2][e & 3]); };
    auto ad = [&](int e) { lsum = addf(lsum, s[e >> 2][e & 3]); };
    auto cv = [&]() { pf[0] = cvtpk(s[0][0], s[0][1]); pf[1] = cvtpk(s[0][2], s[0][3]); pf[2] = cvtpk(s[1][0], s[1][1]); pf[3] = cvtpk(s[1][2], s[1][3]); pin(pf); };
    auto mxs = [&](int kt, int hf) { mx = max3i((kt | hf) ? mx : 0, s[kt][2 * hf], s[kt][2 * hf + 1]); };
    auto lanemax = [&]() { return fmaxf(fmaxf(fmaxf(s[0][0], s[0][1]), fmaxf(s[0][2], s[0][3])), fmaxf(fmaxf(s[1][0], s[1][1]), fmaxf(s[1][2], s[1][3]))); };
#define SLOT(n, A) do { mfa(n); FENCE(); rda(n + 8); A; FENCE(); } while (0)
#pragma unroll 1
    for (int it = 0; it < 2; ++it) {
      {
        nextK();
#pragma unroll
        for (int n = 0; n < 8; ++n) rdK(n);
        FENCE();
#pragma unroll
        for (int n = 0; n < 12; ++n) {
          if (n % 6) mfma(s[n / 6], w[n & 7], qf[n % 6]); else mfma0(s[n / 6], w[n & 7], qf[0]);
          FENCE();
          if (n < 4) rdK(n + 8);
          o[n] = f32x4{};
          FENCE();
        }
        cwrite(adone, gp);
        nextK();
#pragma unroll
        for (int n = 0; n < 8; ++n) rdK(n);
        nops<8>();
        const float m = xg_max(lanemax());
        negm = (f32x4){-m, -m, -m, -m};
        s[0] -= m; s[1] -= m;
        lsum = 0.f; sc = 1.f; resc = false;
#pragma unroll
        for (int e = 0; e < 8; ++e) ex(e);
#pragma unroll
        for (int e = 0; e < 8; ++e) ad(e);
        cv();
        nops<2>();
      }
#pragma unroll 1
      for (int st = 1; st < 16; ++st) {
        SLOT(0, pre = cread(ardy));
        SLOT(1, ); SLOT(2, ); SLOT(3, );
        nextV(); FENCE();
        SLOT(4, ); SLOT(5, );
        if (resc) {
#pragma unroll
          for (int dt = 0; dt < 12; ++dt) o[dt] *= sc;
        }
        FENCE();
        SLOT(6, ); SLOT(7, );
        SLOT(8, mxs(0, 0)); SLOT(9, mxs(0, 1));
        SLOT(10, ); SLOT(11, );
        SLOT(12, pre = cread(ardy));
        SLOT(13, );
        SLOT(14, mxs(1, 0); mxs(1, 1));
        resc = __any(mx > __builtin_bit_cast(int, RESC_THR));
        if (resc) {
          const float d = fmaxf(xg_max(lanemax()), 0.f);
          s[0] -= d; s[1] -= d; negm -= d;
          sc = __builtin_amdgcn_exp2f(-d);
          lsum *= sc;
        }
        FENCE();
        SLOT(15, ex(0));
        cwrite(adone, gp);
        if (st < 15) nextK();
        FENCE();
        SLOT(16, ex(1); ad(0)); SLOT(17, ex(2); ad(1)); SLOT(18, ex(3); ad(2)); SLOT(19, ex(4); ad(3));
        SLOT(20, ex(5); ad(4)); SLOT(21, ex(6); ad(5)); SLOT(22, ex(7); ad(6)); SLOT(23, ad(7));
        cv(); FENCE();
        __builtin_amdgcn_s_setprio(0);
        if (it && st == 2) arrive(c, 0, flag0 + F_OA + (unsigned)c.h * 8u);
        if (it && st == SPF) {
          need(haveO, ardy + (C_RDYO - C_RDY), 1, wacc);
#pragma unroll
          for (int r = 0; r < 3; ++r) {
            const unsigned so = sibo(r + 1);
            bload_sc1_into<0>(sa[r][0], vl, c.srdWs, so); bload_sc1_into<1024>(sa[r][1], vl, c.srdWs, so); bload_sc1_into<2048>(sa[r][2], vl, c.srdWs, so);
            bload_sc1_into<0>(sa[r][3], vl, c.srdWs, so + 3072); bload_sc1_into<1024>(sa[r][4], vl, c.srdWs, so + 3072); bload_sc1_into<2048>(sa[r][5], vl, c.srdWs, so + 3072);
          }
        }
        FENCE();
        if (st == 1 || st == 8) { STAMP(2 + 4 * it + (st >> 3)); CSTAMP(56 + 2 * it + (st >> 3)); }
      }
      {
        const float inv = 1.f / xg_sum(lsum);
        const float fo = resc ? sc * inv : inv;
        s[0] *= inv; s[1] *= inv;
        cv();
        nops<4>();
#pragma unroll
        for (int dt = 0; dt < 12; ++dt) o[dt] *= fo;
        FENCE();
        nextV();
        STAMP(4 + 4 * it);
        bf16x8 v[12];
#pragma unroll
        for (int dt = 0; dt < 12; ++dt) v[dt] = trd2(vadr(dt), 6144);
        cwrite(adone, gp);
        FENCE();
#pragma unroll
        for (int dt = 0; dt < 12; ++dt) mfma(o[dt], v[dt], __builtin_bit_cast(bf16x8, pf));
        FENCE();
#pragma unroll
        for (int q = 0; q < 6; ++q) qf[q] = st6[q];
        FENCE();
        const unsigned so = (unsigned)WS_O + (unsigned)item * OBLK + (unsigned)wid * 12288u + (unsigned)it * 6144u;
#pragma unroll
        for (int q = 0; q < 6; ++q) st6[q] = pack2(o[2 * q], o[2 * q + 1]);
        {
        bstore_sc1<0>(st6[0], vl, c.srdWs, so); bstore_sc1<1024>(st6[1], vl, c.srdWs, so); bstore_sc1<2048>(st6[2], vl, c.srdWs, so);
        bstore_sc1<0>(st6[3], vl, c.srdWs, so + 3072); bstore_sc1<1024>(st6[4], vl, c.srdWs, so + 3072); bstore_sc1<2048>(st6[5], vl, c.srdWs, so + 3072);
        }
      }
      STAMP(5 + 4 * it);
    }
  }

  PUT(41, wacc);
  relane();
  bf16x8 sb[3][6];
  auto loadO = [&](int r, int s) {
    const unsigned so = sibo(r + 1) + 6144 + s * 2048;
    bload_sc1<0>(sb[r][2 * s], vl, c.srdWs, so); bload_sc1<1024>(sb[r][2 * s + 1], vl, c.srdWs, so);
    FENCE();
  };
  auto pinO = [&](int r, int s) { pin(sb[r][2 * s]); pin(sb[r][2 * s + 1]); FENCE(); };
  unsigned aO[2];
  aO[0] = lds0 + (4 * g + tq) * 320 + ((tp ^ (g & 1)) << 4) + 8 * (tp & 1);
  aO[1] = aO[0] ^ 8;
  f32x4 y[5][2][2];
  bf16x8 wf[8];
  auto wadr = [&](int n) { const int j = n / 20, k = n % 20; return aO[k & 1] + (j % NWS % 3) * WSLOT + (k / 10) * 10240 + ((k % 10) >> 1) * 64; };
  auto stage = [&](int j, const bf16x8 (&oA)[6], const bf16x8 (&oB)[6]) {
#pragma unroll
    for (int k = 0; k < 20; ++k) {
      const int n = 20 * j + k, sn = (j + 1) % NWS, dlt = sn % 3 ? 0 : sn ? 3 * WSLOT : -6 * WSLOT;
      if (k == 13 && j < 11) { need(have, ardy, D_G4 + j + 2, wacc); FENCE(); }
      if (!(k & 1)) pin(wf[(n + 1) & 7]);
#pragma unroll
      for (int i = 0; i < 2; ++i) {
        const bf16x8& of = (i ? oB : oA)[2 * (j % 3) + k / 10];
        if (n < 10) mfma0(y[(k % 10) >> 1][k & 1][i], wf[n & 7], of);
        else mfma(y[(k % 10) >> 1][k & 1][i], wf[n & 7], of);
        FENCE();
        if (n + 7 < 240) { if (i) trhi(wf[(n + 7) & 7], wadr(n + 7) + 5120); else trlo(wf[(n + 7) & 7], wadr(n + 7)); }
        if (i == 0 && k == 5 && j < 11) { pin(pre); pre = cread(ardy); }
        if (i == 1 && k == 12 && j < 11) { have = rfl(pre); pins(have); }
        if (i == 0 && k == 15 && j < 5) pre = j == 0 ? bump<C_G4>(c.ctl) : j == 1 ? bump<C_G4 + 4>(c.ctl) : j == 2 ? bump<C_G4 + 8>(c.ctl) : j == 3 ? bump<C_G4 + 12>(c.ctl) : bump<C_G4 + 16>(c.ctl);
        if (i == 0 && (k == 12 || k == 13) && dlt && j < 11) { aO[k & 1] += dlt; pin(aO[k & 1]); }
        FENCE();
      }
    }
  };
  need(have, ardy, D_G4 + 1, wacc);
#pragma unroll
  for (int n = 0; n < 7; ++n) wf[n] = trd2(wadr(n), 5120);
  FENCE();
  STAMP(10);
  stage(0, qf, st6);
  arrive(c, 1, flag0 + F_OB + (unsigned)c.h * 8u);
#pragma unroll
  for (int r = 0; r < 3; ++r)
#pragma unroll
    for (int q = 0; q < 6; ++q) pin(sa[r][q]);
  FENCE();
  STAMP(11);
  stage(1, qf, st6);
  stage(2, qf, st6);
  STAMP(12);
  need(haveO, ardy + (C_RDYO - C_RDY), 2, wacc);
  STAMP(13);
  loadO(0, 0); loadO(0, 1); loadO(0, 2); loadO(1, 0); loadO(1, 1); loadO(1, 2);
  STAMP(14);
  waitvm<10>(); pinO(0, 0); STAMP(15); stage(3, sa[0], sb[0]); loadO(2, 0);
  waitvm<10>(); pinO(0, 1); stage(4, sa[0], sb[0]); loadO(2, 1);
  waitvm<10>(); pinO(0, 2); stage(5, sa[0], sb[0]); loadO(2, 2);
  STAMP(16); CSTAMP(60);
  waitvm<6>(); pinO(1, 0); pinO(1, 1); pinO(1, 2); stage(6, sa[1], sb[1]); stage(7, sa[1], sb[1]); stage(8, sa[1], sb[1]);
  STAMP(17); CSTAMP(61);
  waitvm<0>(); pinO(2, 0); pinO(2, 1); pinO(2, 2); stage(9, sa[2], sb[2]); stage(10, sa[2], sb[2]); stage(11, sa[2], sb[2]);
  nops<8>();
  STAMP(20); PUT(42, wacc); CSTAMP(62);

  const int grp = (c.br * 4 + c.bp) * 4 + hq, k8 = c.h >> 2;
  {
    const unsigned so = (unsigned)WS_P + (unsigned)(grp * 8 + k8) * PBLK + (unsigned)wid * 10240u;
#pragma unroll
    for (int i = 0; i < 2; ++i) {
      bstore_sc1<0>(pack2(y[0][0][i], y[0][1][i]), vl, c.srdWs, so + i * 5120); bstore_sc1<1024>(pack2(y[1][0][i], y[1][1][i]), vl, c.srdWs, so + i * 5120);
      bstore_sc1<2048>(pack2(y[2][0][i], y[2][1][i]), vl, c.srdWs, so + i * 5120); bstore_sc1<3072>(pack2(y[3][0][i], y[3][1][i]), vl, c.srdWs, so + i * 5120);
      bstore_sc1<0>(pack2(y[4][0][i], y[4][1][i]), vl, c.srdWs, so + i * 5120 + 4096);
    }
  }
  relane();
  arrive(c, 2, flag0 + F_P + (unsigned)hq * 64u + (unsigned)k8 * 8u);
  STAMP(21);
  need(haveO, ardy + (C_RDYO - C_RDY), 3, wacc);
  STAMP(22);
  {
    bf16x8 pr[8];
    const unsigned so = (unsigned)WS_P + (unsigned)(grp * 8) * PBLK + (unsigned)wid * 10240u + (unsigned)k8 * 1024u;
#pragma unroll
    for (int k = 0; k < 8; ++k) bload_sc1<0>(pr[k], vl, c.srdWs, so + k * PBLK);
    waitvm<0>();
#pragma unroll
    for (int k = 0; k < 8; ++k) pin(pr[k]);
    const int i = k8 >= 5, cp = k8 - 5 * i;
    reduce_store(pr, c.out + (size_t)((2 * c.bp + i) * SQ + wid * 16 + l15) * (2 * DM) + c.br * DM + hq * 160 + cp * 32 + 8 * g, g & 1);
  }
  asm volatile(".Lcode_end:");
}

typedef unsigned long long u64;
typedef u64 u64x2 __attribute__((ext_vector_type(2)));
typedef u64 u64x4 __attribute__((ext_vector_type(4)));
__global__ __launch_bounds__(512, 2) void fused_kernel(const u16* xA, const u16* kA, const u16* vA, const u16* wqA, const u16* woA,
                                                       const u16* xB, const u16* kB, const u16* vB, const u16* wqB, const u16* woB,
                                                       u16* out_, char* ws_) {
  extern __shared__ __attribute__((aligned(16))) char lds[];
  Ctx c;
  const int xcd = blockIdx.x & 7, idx = blockIdx.x >> 3;
  c.h = 8 * (xcd >> 1) + (idx & 7); c.bp = idx >> 3; c.br = xcd & 1;
  u64x4 p4; u64 p1; u64x2 p2;
  asm volatile("s_load_dwordx8 %0, %3, %4\n\ts_load_dwordx2 %1, %3, %4 offset:32\n\ts_load_dwordx4 %2, %3, 0x50"
               : "=&s"(p4), "=&s"(p1), "=&s"(p2) : "s"(__builtin_amdgcn_kernarg_segment_ptr()), "s"(c.br * 40) : "memory");
  c.lds0 = (unsigned)(uintptr_t)lds - CTL;
  c.ctl = c.lds0 + CTL;
  pins(c.ctl);
  c.wid = rfl(threadIdx.x >> 6);
  {
    unsigned long long pc; unsigned clen;
    asm volatile("s_getpc_b64 %0\n.Lcode_here:\n\ts_mov_b32 %1, .Lcode_end-.Lcode_here" : "=s"(pc), "=s"(clen));
    c.srdC = __builtin_amdgcn_make_buffer_rsrc((void*)pc, (short)0, (int)clen, 0x00020000);
    c.cshare = (unsigned)(idx & 31) * 128u;
    if (c.wid == 0) touch(c.lds0 + CTL + C_JUNK + 1024, threadIdx.x * 4096u, c.srdC, c.cshare);
  }
  cwrite(c.lds0 + CTL + threadIdx.x * 4, 0);
  {
    const int lane = threadIdx.x & 63;
#pragma unroll
    for (int k = 0; k < 3; ++k) {
      const int row = ((8 * k + (lane >> 3)) * 11) >> 5;
      c.vw[0][k] = (unsigned)(row * (HD * 2 - 384) + ((lane ^ ((row >> 1) & 1)) << 4));
      c.vw[1][k] = c.vw[0][k] ^ 64;
      c.vb[k] = (unsigned)((lane ^ (((row >> 1) & 3) << 1)) << 4);
      pin(c.vw[0][k]); pin(c.vw[1][k]); pin(c.vb[k]);
    }
  }
  asm volatile("s_waitcnt lgkmcnt(0)\n\ts_barrier" : "+s"(p4), "+s"(p1), "+s"(p2) :: "memory");
  const u16* x = (const u16*)p4[0]; const u16* kc = (const u16*)p4[1]; const u16* vc = (const u16*)p4[2]; const u16* wq = (const u16*)p4[3]; const u16* wo = (const u16*)p1;
  char* ws = (char*)p2[1];
  c.out = (GAS u16*)p2[0];
  const size_t kvoff = (size_t)(2 * c.bp * NH + c.h) * SK * DH;
  const u64 xb = (u64)(x + (size_t)c.bp * 2 * SQ * DM);
  c.srdX = u32x4{(unsigned)xb, (unsigned)(xb >> 32) & 0xFFFFu, 2 * SQ * DM * 2, 0x00020000};
  c.rot2 = ((c.bp + c.h) & 1) * 2;
  c.srdW = __builtin_amdgcn_make_buffer_rsrc((void*)(wq + c.h * DH), (short)0, (DM * HD - c.h * DH) * 2, 0x00020000);
  c.srdK = __builtin_amdgcn_make_buffer_rsrc((void*)(kc + kvoff), (short)0, KVITEM + SK * DH * 2, 0x00020000);
  c.srdV = __builtin_amdgcn_make_buffer_rsrc((void*)(vc + kvoff), (short)0, KVITEM + SK * DH * 2, 0x00020000);
  c.srdWo = __builtin_amdgcn_make_buffer_rsrc((void*)(wo + (size_t)(c.h >> 2) * 4 * DH * DM + (c.h & 3) * 160), (short)0, (4 * DH * DM - (c.h & 3) * 160) * 2, 0x00020000);
  c.srdWs = __builtin_amdgcn_make_buffer_rsrc((void*)ws, (short)0, (int)WS_END, 0x00020000);
  if (__builtin_expect(c.wid == 7, 1)) {
    __builtin_amdgcn_s_setprio(3);
    loader(c, ws);
    asm volatile("s_endpgm" ::: "memory");
  }
  consumer(c, ws);
}

extern "C" void kernel_launch(void* const* d_in, const int* in_sizes, int n_in, void* d_out, int out_size, void* d_ws, size_t ws_size, hipStream_t stream) {
  (void)in_sizes; (void)n_in; (void)out_size; (void)ws_size;
  (void)hipFuncSetAttribute((const void*)fused_kernel, hipFuncAttributeMaxDynamicSharedMemorySize, LDS_BYTES);
  fused_kernel<<<256, 512, LDS_BYTES, stream>>>((const u16*)d_in[0], (const u16*)d_in[1], (const u16*)d_in[2], (const u16*)d_in[3], (const u16*)d_in[4],
                                                (const u16*)d_in[5], (const u16*)d_in[6], (const u16*)d_in[7], (const u16*)d_in[8], (const u16*)d_in[9],
                                                (u16*)d_out, (char*)d_ws);
}
```

```cpp
#define DUMP_STAMPS
#define NOG1ST
#define PB 200
#define PLO 0
#define PHI 0
#define KV_NT PRG XE 4 SPF=14 PFL=52
#include <hip/hip_runtime.h>
#include <hip/hip_bf16.h>
#include <stdint.h>
#include <type_traits>
#pragma clang diagnostic ignored "-Winline-asm"

typedef unsigned short u16;
typedef short bf16x8 __attribute__((ext_vector_type(8)));
typedef short s16x4 __attribute__((ext_vector_type(4)));
typedef float f32x4 __attribute__((ext_vector_type(4)));
typedef float f32x2 __attribute__((ext_vector_type(2)));
typedef __bf16 bf16x2_t __attribute__((ext_vector_type(2)));
typedef unsigned u32x4 __attribute__((ext_vector_type(4)));
typedef unsigned u32x2 __attribute__((ext_vector_type(2)));
typedef __amdgpu_buffer_rsrc_t rsrc_t;
#define LAS __attribute__((address_space(3)))
#define GAS __attribute__((address_space(1)))
#define DI __device__ __forceinline__

constexpr int SQ = 112, DM = 640, NH = 32, SK = 512, DH = 192, HD = NH * DH;
constexpr int PIECE = 12288, NP = 13;
constexpr int WSLOT = 20480, NWS = 7;
constexpr int CTL = -4096;
constexpr int C_G1 = 0, C_G4 = 40, C_RDY = 64, C_RDYO = 68;
constexpr int C_TAG = 72, C_CNT = 80, C_DONE = 256, C_JUNK = 2048;
constexpr int LDS_BYTES = 163840;
constexpr unsigned FLAG_BYTES = 8192;
constexpr unsigned OBLK = 7 * 12 * 1024;
constexpr unsigned PBLK = 7 * 10 * 1024;
constexpr size_t WS_O = FLAG_BYTES;
constexpr size_t WS_P = WS_O + (size_t)256 * OBLK;
constexpr size_t WS_END = WS_P + (size_t)256 * PBLK;
constexpr float QSCALE = 0.07216878364870322f * 1.4426950408889634f;
constexpr float RESC_THR = 8.0f;
constexpr unsigned KVITEM = NH * SK * DH * 2;
constexpr int D_ATT = 20, D_G4 = 84;
#define NPRE 5
#define SPF 11
#define PFL 48
#define XL 2
#define XE 0

DI void mfma(f32x4& d, const bf16x8& a, const bf16x8& b) { asm("v_mfma_f32_16x16x32_bf16 %0, %1, %2, %0" : "+v"(d) : "v"(a), "v"(b)); }
DI void mfmac(f32x4& d, const bf16x8& a, const bf16x8& b, const f32x4& c) { asm("v_mfma_f32_16x16x32_bf16 %0, %1, %2, %3" : "=v"(d) : "v"(a), "v"(b), "v"(c)); }
DI void mfma0(f32x4& d, const bf16x8& a, const bf16x8& b) { asm("v_mfma_f32_16x16x32_bf16 %0, %1, %2, 0" : "=v"(d) : "v"(a), "v"(b)); }
#define FENCE() __builtin_amdgcn_sched_barrier(0)
template <int N> DI void nops() { FENCE(); asm volatile("s_nop %0" :: "i"(N - 1)); FENCE(); }
DI int lane_id() { int l; asm volatile("v_mbcnt_lo_u32_b32 %0, -1, 0\n\tv_mbcnt_hi_u32_b32 %0, -1, %0" : "=v"(l)); return l; }
DI unsigned rfl(unsigned x) { return __builtin_amdgcn_readfirstlane(x); }
DI unsigned cvtpk(float lo, float hi) { f32x2 v = {lo, hi}; bf16x2_t b = __builtin_convertvector(v, bf16x2_t); return __builtin_bit_cast(unsigned, b); }
DI s16x4 trd(unsigned a) { return __builtin_bit_cast(s16x4, __builtin_amdgcn_ds_read_tr16_b64_v4i16((LAS s16x4*)a)); }
DI void trlo(bf16x8& d, unsigned a) { const s16x4 t = trd(a); d[0] = t[0]; d[1] = t[1]; d[2] = t[2]; d[3] = t[3]; }
DI void trhi(bf16x8& d, unsigned a) { const s16x4 t = trd(a); d[4] = t[0]; d[5] = t[1]; d[6] = t[2]; d[7] = t[3]; }
DI bf16x8 trd2(unsigned a, unsigned d) { s16x4 lo = trd(a), hi = trd(a + d); return (bf16x8){lo[0], lo[1], lo[2], lo[3], hi[0], hi[1], hi[2], hi[3]}; }
DI bf16x8 ldsr128(unsigned a) { return *(const LAS bf16x8*)a; }
DI bf16x8 pack2(const f32x4& a, const f32x4& b) {
  u32x4 w = {cvtpk(a[0], a[1]), cvtpk(a[2], a[3]), cvtpk(b[0], b[1]), cvtpk(b[2], b[3])};
  return __builtin_bit_cast(bf16x8, w);
}
DI float bflo(unsigned w) { return __uint_as_float(w << 16); }
DI float bfhi(unsigned w) { return __uint_as_float(w & 0xFFFF0000u); }
DI float addf(float a, float b) { float d = a + b; asm("" : "+v"(d)); return d; }
DI int max3i(int a, float b, float c) { return max(max(a, __float_as_int(b)), __float_as_int(c)); }
DI f32x2 sw16(float v) { const unsigned u = __float_as_uint(v); const auto r = __builtin_amdgcn_permlane16_swap(u, u, false, false); return f32x2{__uint_as_float(r[0]), __uint_as_float(r[1])}; }
DI f32x2 sw32(float v) { const unsigned u = __float_as_uint(v); const auto r = __builtin_amdgcn_permlane32_swap(u, u, false, false); return f32x2{__uint_as_float(r[0]), __uint_as_float(r[1])}; }
DI float xg_max(float v) { f32x2 r = sw16(v); r = sw32(fmaxf(r[0], r[1])); return fmaxf(r[0], r[1]); }
DI float xg_sum(float v) { f32x2 r = sw16(v); r = sw32(r[0] + r[1]); return r[0] + r[1]; }

#define KVPOL " nt"
DI void dma4(unsigned m0v, unsigned v0, unsigned v1, unsigned v2, unsigned v3, rsrc_t srd, unsigned soff) {
  asm volatile("s_mov_b32 m0, %0\n\ts_nop 0\n\t"
               "buffer_load_dwordx4 %1, %5, %6 offen lds\n\t"
               "buffer_load_dwordx4 %2, %5, %6 offen offset:1024 lds\n\t"
               "buffer_load_dwordx4 %3, %5, %6 offen offset:2048 lds\n\t"
               "buffer_load_dwordx4 %4, %5, %6 offen offset:3072 lds"
               :: "s"(m0v), "v"(v0), "v"(v1), "v"(v2), "v"(v3), "s"(srd), "s"(soff) : "m0", "memory");
}
DI void dma3k(unsigned m0v, unsigned v0, unsigned v1, unsigned v2, rsrc_t srd, unsigned soff) {
  asm volatile("s_mov_b32 m0, %0\n\ts_nop 0\n\t"
               "buffer_load_dwordx4 %1, %4, %5 offen" KVPOL " lds\n\t"
               "buffer_load_dwordx4 %2, %4, %5 offen offset:1024" KVPOL " lds\n\t"
               "buffer_load_dwordx4 %3, %4, %5 offen offset:2048" KVPOL " lds"
               :: "s"(m0v), "v"(v0), "v"(v1), "v"(v2), "s"(srd), "s"(soff) : "m0", "memory");
}
DI void dma3(unsigned m0v, unsigned v0, unsigned v1, unsigned v2, rsrc_t srd, unsigned soff) {
  asm volatile("s_mov_b32 m0, %0\n\ts_nop 0\n\t"
               "buffer_load_dwordx4 %1, %4, %5 offen lds\n\t"
               "buffer_load_dwordx4 %2, %4, %5 offen offset:1024 lds\n\t"
               "buffer_load_dwordx4 %3, %4, %5 offen offset:2048 lds"
               :: "s"(m0v), "v"(v0), "v"(v1), "v"(v2), "s"(srd), "s"(soff) : "m0", "memory");
}
DI void touch(unsigned m0v, unsigned v, rsrc_t srd, unsigned soff) {
  asm volatile("s_mov_b32 m0, %0\n\ts_nop 0\n\tbuffer_load_dword %1, %2, %3 offen lds" :: "s"(m0v), "v"(v), "s"(srd), "s"(soff) : "m0", "memory");
}
template <int OFF> DI void bload_sc1(bf16x8& d, unsigned v, rsrc_t srd, unsigned soff) {
  asm volatile("buffer_load_dwordx4 %0, %1, %2, %3 offen offset:%4 sc1" : "=v"(d) : "v"(v), "s"(srd), "s"(soff), "i"(OFF) : "memory");
}
template <int OFF> DI void bload_sc1_into(bf16x8& d, unsigned v, rsrc_t srd, unsigned soff) {
  asm volatile("buffer_load_dwordx4 %0, %1, %2, %3 offen offset:%4 sc1" : "+v"(d) : "v"(v), "s"(srd), "s"(soff), "i"(OFF) : "memory");
}
template <int OFF> DI void bstore_sc1(const bf16x8& d, unsigned v, rsrc_t srd, unsigned soff) {
  asm volatile("buffer_store_dwordx4 %0, %1, %2, %3 offen offset:%4 sc1\n\ts_nop 1" :: "v"(d), "v"(v), "s"(srd), "s"(soff), "i"(OFF) : "memory");
}
template <int N> DI void waitvm() { asm volatile("s_waitcnt vmcnt(%0)" :: "i"(N) : "memory"); }
template <typename T> DI void pin(T& x) { asm volatile("" : "+v"(x)); }
template <typename T> DI void pins(T& x) { asm volatile("" : "+s"(x)); }
DI void cbar() { asm volatile("" ::: "memory"); }

#define STAMP_(i, ins) do { unsigned long long t_; asm volatile(ins " %0\n\ts_waitcnt lgkmcnt(0)" : "=s"(t_) :: "memory"); \
  asm volatile("s_mov_b32 m0, %2\n\ts_nop 4\n\tv_writelane_b32 %0, %1, m0" : "+v"(stampv) : "s"((unsigned)t_), "s"(i) : "m0"); } while (0)
#define STAMP(i) STAMP_(i, "s_memrealtime")
#define PUT(i, v) asm volatile("s_mov_b32 m0, %2\n\ts_nop 4\n\tv_writelane_b32 %0, %1, m0" : "+v"(stampv) : "s"(v), "s"(i) : "m0")
DI unsigned nowt() { unsigned long long t_; asm volatile("s_memrealtime %0\n\ts_waitcnt lgkmcnt(0)" : "=s"(t_) :: "memory"); return (unsigned)t_; }
#define WACC_BEGIN const unsigned wt0_ = nowt()
#define WACC_END(w) (w) += nowt() - wt0_
#define CSTAMP(i) STAMP_(i, "s_memtime")

#define G1ST if (0)
struct Ctx {
  rsrc_t srdW, srdK, srdV, srdWo, srdWs, srdC;
  u32x4 srdX;
  GAS u16* out;
  unsigned lds0, ctl, cshare;
  unsigned vw[2][3], vb[3];
  int wid, br, bp, h, rot2;
  unsigned long long tent;
};

DI unsigned cread(unsigned a) { return *(const volatile LAS unsigned*)a; }
DI void cwrite(unsigned a, unsigned v) { *(volatile LAS unsigned*)a = v; }
template <int A> DI unsigned bump(unsigned z) { cbar(); const unsigned r = __builtin_amdgcn_ds_append((LAS int*)z + A / 4); cbar(); return r; }
DI void need(unsigned& have, unsigned a, unsigned n, unsigned& wacc) {
  n *= 64;
  if (__builtin_expect(have < n, 0)) {
    WACC_BEGIN;
#pragma unroll 1
    for (unsigned spins = 0;; ++spins) {
      have = rfl(cread(a));
      if (have >= n) break;
      if (spins > (1u << 20)) { have = ~0u; break; }
      __builtin_amdgcn_s_sleep(1);
    }
    WACC_END(wacc);
  }
  cbar();
}
DI void seen(unsigned& have, unsigned pre) { have = max(have, rfl(pre)); }

struct Tag { unsigned lo, hi; };
DI bool match(const u32x2& f, Tag tag) { return __all(f[0] == tag.lo && f[1] == tag.hi); }
DI void poll_flags(rsrc_t srdWs, unsigned vo, Tag tag) {
#pragma unroll 1
  for (unsigned spins = 0; spins < (1u << 20); ++spins) {
    u32x2 f;
    asm volatile("buffer_load_dwordx2 %0, %1, %2, 0 offen sc1\n\ts_waitcnt vmcnt(0)" : "=v"(f) : "v"(vo), "s"(srdWs) : "memory");
    if (match(f, tag)) break;
    __builtin_amdgcn_s_sleep(2);
  }
}
DI void set_flag(rsrc_t srdWs, unsigned off, int lane, Tag tag) {
  if (lane == 0) { const u32x2 t = {tag.lo, tag.hi}; asm volatile("buffer_store_dwordx2 %0, off, %1, %2 sc1\n\ts_nop 1" :: "v"(t), "s"(srdWs), "s"(off) : "memory"); }
}
DI void arrive(const Ctx& c, int which, unsigned off) {
  waitvm<0>();
  const int lane = lane_id();
  unsigned old = 0;
  if (lane == 0) old = __hip_atomic_fetch_add((LAS unsigned*)(c.lds0 + CTL + C_CNT + 4 * which), 1u, __ATOMIC_RELAXED, __HIP_MEMORY_SCOPE_WORKGROUP);
  if (rfl(old) == 6) {
    const Tag tag{rfl(cread(c.lds0 + CTL + C_TAG)), rfl(cread(c.lds0 + CTL + C_TAG + 4))};
    set_flag(c.srdWs, off, lane, tag);
  }
}

DI void reduce_store(const bf16x8 (&pr)[8], GAS u16* dst, bool odd) {
  float a[8] = {0.f, 0.f, 0.f, 0.f, 0.f, 0.f, 0.f, 0.f};
#pragma unroll
  for (int k = 0; k < 8; ++k) {
    const u32x4 w = __builtin_bit_cast(u32x4, pr[k]);
#pragma unroll
    for (int e = 0; e < 4; ++e) { a[2 * e] += bflo(w[e]); a[2 * e + 1] += bfhi(w[e]); }
  }
  const unsigned u0 = cvtpk(a[0], a[1]), u1 = cvtpk(a[2], a[3]), u2 = cvtpk(a[4], a[5]), u3 = cvtpk(a[6], a[7]);
  const u32x4 w = {odd ? u2 : u0, odd ? u3 : u1, odd ? u0 : u2, odd ? u1 : u3};
  *(GAS u32x4*)dst = w;
}
constexpr unsigned F_OA = 256, F_OB = 512, F_P = 768;

DI void loader(const Ctx& c, char* ws) {
  const int lane = lane_id();
  const unsigned lds0 = c.lds0;
  const int hq = c.h & 3;
  unsigned stampv = 0;
  STAMP(0); CSTAMP(32); PUT(30, (unsigned)c.tent);
  int dv = 0, gv = 0;
  unsigned wacc = 1;
  const unsigned adone = lds0 + CTL + C_DONE + (lane < 7 ? lane : 0) * 260;
  auto gate = [&](int p) {
    if (!__all(dv >= p)) {
      WACC_BEGIN;
#pragma unroll 1
      for (unsigned spins = 0;; ++spins) {
        dv = cread(adone);
        if (__all(dv >= p)) break;
        if (spins > (1u << 20)) { dv = 0x7FFFFFFF; break; }
        __builtin_amdgcn_s_sleep(1);
      }
      WACC_END(wacc);
    }
    cbar();
  };
  auto gatec = [&](int i, int n) {
    n *= 7 * 64;
    if (__builtin_amdgcn_readlane(gv, i) < n) {
      WACC_BEGIN;
#pragma unroll 1
      for (unsigned spins = 0;; ++spins) {
        gv = cread(lds0 + CTL + lane * 4);
        if (__builtin_amdgcn_readlane(gv, i) >= n) break;
        if (spins > (1u << 20)) { gv = 0x7FFFFFFF; break; }
        __builtin_amdgcn_s_sleep(1);
      }
      WACC_END(wacc);
    }
    cbar();
  };
  const unsigned (&vw)[2][3] = c.vw; const unsigned (&vb)[3] = c.vb;
  auto wpieces = [&](int t0) {
    unsigned m = lds0 + 3 * PIECE;
#pragma unroll 1
    for (int t = t0; t < t0 + 10; ++t) {
      if (t0) gatec(t - t0, 1);
      const unsigned so = (unsigned)(((t ^ c.rot2) >> 1) * 64 + (t & 1) * 8) * (HD * 2);
#pragma unroll
      for (int q = 0; q < 4; ++q) dma3(m + 3072 * q, vw[q & 1][0], vw[q & 1][1], vw[q & 1][2], c.srdW, so + q * (16 * HD * 2));
      m += PIECE;
      waitvm<36>(); if (t >= 3) bump<C_RDY>(c.ctl);
    }
  };
  const unsigned flag0 = (unsigned)(c.br * 4 + c.bp) * 1024u;
  const unsigned vtc = (unsigned)lane * 4096u;
  unsigned mslot = lds0;
  auto pieces = [&](int p0, int p1, bool tc) {
#pragma unroll 1
    for (int p = p0; p < p1; ++p) {
      const int q = p & 31;
      const bool isv = ((q & 1) == 0 && q) || q == 31;
      const unsigned so = (unsigned)((isv ? q - 1 : q + 1) >> 1) * PIECE + (unsigned)(p >> 5) * KVITEM;
      const rsrc_t srd = isv ? c.srdV : c.srdK;
      if (p0 == 3) gatec(p - 3, 2); else if (p0) gate(p + 8);
#pragma unroll
      for (int k = 0; k < 4; ++k) dma3k(mslot + 3072 * k, vb[0], vb[1], vb[2], srd, so + 3072 * k);
      mslot = mslot == lds0 + (NP - 1) * PIECE ? lds0 : mslot + PIECE;
      waitvm<36>(); bump<C_RDY>(c.ctl);
      if ((p & 7) == 7) { STAMP(10 + (p >> 3)); PUT(42 + (p >> 3), wacc); }
      if (tc && (p & 1) && p < 60) touch(lds0 + CTL + C_JUNK, vtc, c.srdC, c.cshare);
    }
  };
  wpieces(0);
  STAMP(24);
  u32x2 f;
  asm volatile("buffer_load_dwordx2 %0, off, %1, %2 sc1" : "=v"(f) : "s"(c.srdWs), "s"(flag0) : "memory");
  pieces(0, 3, false);
  STAMP(25);
  wpieces(10);
  STAMP(26);
  pin(f);
  const unsigned long long tg = (((unsigned long long)rfl(f[1]) << 32) | rfl(f[0])) + 1ull;
  const Tag tag{(unsigned)tg, (unsigned)(tg >> 32)};
  pieces(3, NP, false);
  STAMP(1); PUT(40, wacc); CSTAMP(34);
  if (lane == 0) { cwrite(lds0 + CTL + C_TAG, tag.lo); cwrite(lds0 + CTL + C_TAG + 4, tag.hi); }
  pieces(NP, PFL, false);
  const unsigned vsib = flag0 + (unsigned)(c.h >> 2) * 32u + (unsigned)(lane & 3) * 8u;
  asm volatile("buffer_load_dwordx2 %0, %1, %2, 0 offen offset:%3 sc1" : "=v"(f) : "v"(vsib), "s"(c.srdWs), "i"(F_OA) : "memory");
  pieces(PFL, PFL + 4, false);
  pin(f);
  if (!match(f, tag)) poll_flags(c.srdWs, vsib + F_OA, tag);
  bump<C_RDYO>(c.ctl);
  STAMP(8);
  pieces(PFL + 4, 64, true);
  STAMP(2); PUT(41, wacc);
  unsigned vo[5];
#pragma unroll
  for (int k = 0; k < 5; ++k) {
    const int row = ((16 * k + (lane >> 2)) * 205) >> 10;
    vo[k] = (unsigned)(row * (DM * 2 - 320) + ((lane ^ ((row >> 2) & 1)) << 4) + (k == 4 ? 4096 : 0));
  }
  unsigned cwo = hq * 3;
  mslot = lds0;
  auto issueWo = [&](int j) {
    if (j < NWS) gate(D_ATT + 53 + (int)((0xB986431u >> (4 * j)) & 15)); else gatec(C_G4 / 4 + j - NWS, 1);
#pragma unroll
    for (int q = 0; q < 4; ++q) {
      const unsigned so = cwo * (64 * DM * 2) + q * (16 * DM * 2);
      dma4(mslot + 5120 * q, vo[0], vo[1], vo[2], vo[3], c.srdWo, so);
      asm volatile("s_mov_b32 m0, %0\n\ts_nop 0\n\tbuffer_load_dwordx4 %1, %2, %3 offen lds" :: "s"(mslot + 5120 * q + 4096), "v"(vo[4]), "s"(c.srdWo), "s"(so) : "m0", "memory");
    }
    cwo = cwo == 11 ? 0 : cwo + 1;
    mslot = mslot == lds0 + (NWS - 1) * WSLOT ? lds0 : mslot + WSLOT;
  };
#pragma unroll 1
  for (int j = 0; j < 12; ++j) {
    issueWo(j);
    if (j == 0) { waitvm<44>(); bump<C_RDY>(c.ctl); continue; }
    if (j == 1) { waitvm<52>(); bump<C_RDY>(c.ctl); }
    waitvm<40>(); if (j < NPRE || j > NPRE + 1) bump<C_RDY>(c.ctl);
    if (j == 1) STAMP(3);
    if (j == NPRE - 1 || j == 11) {
      waitvm<20>(); bump<C_RDY>(c.ctl);
      waitvm<0>(); bump<C_RDY>(c.ctl);
      if (j == 11) break;
      STAMP(4);
      poll_flags(c.srdWs, vsib + F_OB, tag);
      bump<C_RDYO>(c.ctl);
      STAMP(5);
    }
  }
  STAMP(6);
  poll_flags(c.srdWs, flag0 + F_P + (unsigned)hq * 64u + (unsigned)(lane & 7) * 8u, tag);
  bump<C_RDYO>(c.ctl);
  if (c.h == 0) set_flag(c.srdWs, flag0, lane, tag);
  STAMP(7);
  const int k8 = c.h >> 2;
  if (k8 < 7) {
    const unsigned so = (unsigned)WS_P + (unsigned)(((c.br * 4 + c.bp) * 4 + hq) * 8) * PBLK + (unsigned)k8 * 10240u + 8192u;
    bf16x8 pr[2][8];
#pragma unroll
    for (int k = 0; k < 8; ++k) { bload_sc1<0>(pr[0][k], lane * 16, c.srdWs, so + k * PBLK); bload_sc1<1024>(pr[1][k], lane * 16, c.srdWs, so + k * PBLK); }
    waitvm<0>();
#pragma unroll
    for (int p = 0; p < 2; ++p) {
#pragma unroll
      for (int k = 0; k < 8; ++k) pin(pr[p][k]);
      reduce_store(pr[p], c.out + (size_t)((2 * c.bp + 1) * SQ + k8 * 16 + (lane & 15)) * (2 * DM) + c.br * DM + hq * 160 + (3 + p) * 32 + 8 * (lane >> 4), (lane >> 4) & 1);
    }
  }
  waitvm<0>();
  STAMP(23); CSTAMP(33);
  ((GAS unsigned*)(ws + (200u << 20)))[(blockIdx.x * 8 + 7) * 64 + lane] = stampv;
  if (blockIdx.x == PB) { const unsigned a0 = __builtin_amdgcn_readlane(stampv, 0), x = __builtin_amdgcn_readlane(stampv, PHI) - __builtin_amdgcn_readlane(stampv, PLO);
    while ((unsigned)__builtin_amdgcn_s_memrealtime() - a0 < 12000u + 4u * x) __builtin_amdgcn_s_sleep(1); }
}

DI void consumer(const Ctx& c, char* ws) {
  const int wid = c.wid;
  int lane, l15, g, tq, tp;
  auto relane = [&]() { lane = lane_id(); l15 = lane & 15; g = lane >> 4; tq = l15 >> 2; tp = lane & 3; };
  relane();
  const unsigned lds0 = c.lds0;
  unsigned stampv = 0;
  STAMP(0); CSTAMP(32); PUT(30, (unsigned)c.tent);
  const int hq = c.h & 3;
  const unsigned flag0 = (unsigned)(c.br * 4 + c.bp) * 1024u;
  unsigned ardy = lds0 + CTL + C_RDY;
  pin(ardy);
  unsigned have = 0, pre = 0, haveO = 0, wacc = 1;

  bf16x8 qf[6], st6[6];
  {
    bf16x8 xr[5][2][2], xa[2], a[8];
    unsigned vx[2];
    vx[0] = (unsigned)((wid * 16 + (l15 & 14)) * (DM * 2) + g * 32 + (lane & 1) * 16);
    vx[1] = vx[0] + SQ * DM * 2;
    u32x4 srdX = c.srdX;
    unsigned sx[2] = {(unsigned)c.rot2 * 64u + XL * 128u, XL * 128u - (unsigned)c.rot2 * 64u};
#define XLOAD(C, D, V, S, OFF) asm volatile("buffer_load_dwordx4 %0, %1, %2, %3 offen offset:%4" : C(D) : "v"(V), "s"(srdX), "s"(S), "i"(OFF) : "memory")
    auto xfirst = [&](int d) {
#pragma unroll
      for (int q = 0; q < 4; ++q) XLOAD("=v", xr[d][q & 1][q >> 1], vx[q >> 1], sx[d & 1] - XL * 128u + (d & 1 ? 128u : 0u), (d & ~1) * 128 + (q & 1) * (DM * 2));
    };
    unsigned long long even = 0x5555555555555555ull;
    pins(even);
    auto trade = [&](int d, int n) {
      const int i = (n >> 2) & 1, e = n & 3;
      u32x4 r0 = __builtin_bit_cast(u32x4, xr[d][0][i]), r1 = __builtin_bit_cast(u32x4, xr[d][1][i]), t = __builtin_bit_cast(u32x4, xa[i]);
#define XSEL "v_cndmask_b32_dpp %0, %1, %2, vcc quad_perm:[1,0,3,2] row_mask:0xf bank_mask:0xf"
      register unsigned long long m asm("vcc") = n < 8 ? even : ~even;
      if (n < 8) { asm volatile(XSEL : "=v"(t[e]) : "v"(r1[e]), "v"(r0[e]), "s"(m)); xa[i] = __builtin_bit_cast(bf16x8, t); }
      else { asm volatile(XSEL : "=v"(r1[e]) : "v"(r0[e]), "0"(r1[e]), "s"(m)); xr[d][1][i] = __builtin_bit_cast(bf16x8, r1); }
    };
#pragma unroll
    for (int d = 0; d < 5; ++d) {
      if (d < 2) xfirst(d);
      else
#pragma unroll
        for (int q = 0; q < 4; ++q) asm volatile("" : "=v"(xr[d][q & 1][q >> 1]));
    }
    asm volatile("" : "=v"(xa[0])); asm volatile("" : "=v"(xa[1]));
    unsigned aW[2][2][2];
#pragma unroll
    for (int s1 = 0; s1 < 2; ++s1)
#pragma unroll
      for (int T = 0; T < 2; ++T) {
        aW[0][s1][T] = lds0 + 3 * PIECE + (8 * g + tq) * 384 + (((4 * s1 + tp) ^ ((tq >> 1) | ((g & 1) << 2))) << 4) + 8 * (T ^ (tp & 1));
        aW[1][s1][T] = aW[0][s1][T] + 5 * PIECE;
        pin(aW[0][s1][T]); pin(aW[1][s1][T]);
      }
    f32x4 acc[12][2];
#pragma unroll
    for (int dt = 0; dt < 12; ++dt) { acc[dt][0] = f32x4{}; acc[dt][1] = f32x4{}; pin(acc[dt][0]); pin(acc[dt][1]); }
    auto adr = [&](int t, int K) { return aW[t / 5][(K >> 1) & 1][K & 1] + (t % 5) * PIECE + (K >> 2) * 128; };
    auto xwait = [&](int d) {
      waitvm<4>();
#pragma unroll
      for (int q = 0; q < 4; ++q) pin(xr[d][q & 1][q >> 1]);
      FENCE();
    };
    need(have, ardy, 1, wacc);
    STAMP(29);
#pragma unroll
    for (int k = 0; k < 7; ++k) { a[k] = trd2(adr(0, k), 1536); FENCE(); }
    xwait(0);
#pragma unroll
    for (int n = 0; n < 16; ++n) trade(0, n);
    if (XL == 3) xfirst(2);
    nops<2>();
    unsigned pos = 0, last = 14;
    auto step = [&](auto tc) {
      constexpr int t = decltype(tc)::value, T = t & 1, d = t >> 1, r = (d + XL) % 5;
#pragma unroll
      for (int k = 0; k < 12; ++k) {
        const int b = (12 * t + k) & 7, f = (b + 7) & 7;
        const int n = k < 3 ? 2 * k - 2 : k == 3 ? 4 : k == 4 ? 5 : 2 * k - 5;
        if (k == 5) { need(have, ardy, t < 9 ? pos + t + 2 : last, wacc); FENCE(); }
        if (T && k == 1) xwait((d + 1) % 5);
        const unsigned ra = k < 5 ? adr(t, k + 7) : adr((t + 1) % 10, k - 5);
        if (!(k & 1)) pin(a[(b + 1) & 7]);
        mfma(acc[k][0], a[b], T ? xr[d][1][0] : xa[0]); FENCE();
        trlo(a[f], ra);
        if (k == 10) { pin(pre); pre = cread(ardy); }
        if (k == 5) pre = bump<C_G1 + 4 * t>(c.ctl);
        if (T && k >= 1 && k <= 9 && k != 5) trade((d + 1) % 5, n);
        FENCE();
        mfma(acc[k][1], a[b], T ? xr[d][1][1] : xa[1]); FENCE();
        trhi(a[f], ra + 1536);
        if (k == 3) { have = rfl(pre); pins(have); }
        if (T && k >= 1 && k <= 10 && k != 3 && k != 5) trade((d + 1) % 5, k == 10 ? 15 : n + 1);
        if (XL == 2 ? !T && (k == 1 || k == 2 || k == 7 || k == 8) : T && (k == 3 || k == 5 || k >= 10)) {
          const int q = XL == 2 ? (k < 5 ? k - 1 : k - 5) : (k < 10 ? k >> 2 : k - 8);
          XLOAD("+v", xr[r][q & 1][q >> 1], vx[q >> 1], sx[(d + XL) & 1], d * 128 + (q & 1) * (DM * 2));
        }
        if (t == (XL == 2 ? 5 : 4) && k == 8) { srdX[2] = pos ? 0u : srdX[2]; pins(srdX); }
        FENCE();
      }
    };
#define IC(n) std::integral_constant<int, n>{}
#pragma unroll 1
    for (int ps = 0; ps < 2; ++ps) {
      step(IC(0)); step(IC(1)); step(IC(2)); step(IC(3));
      G1ST { STAMP(24 + 2 * ps); CSTAMP(50 + 2 * ps); }
      step(IC(4)); step(IC(5)); step(IC(6)); step(IC(7));
      G1ST { STAMP(25 + 2 * ps); CSTAMP(51 + 2 * ps); }
      step(IC(8)); step(IC(9));
      pos = 13 + XE; last = XE ? 24 + XE : 23;
      const unsigned u = sx[0]; sx[0] = sx[1] + 640u; sx[1] = u + 640u;
    }
    waitvm<0>();
#pragma unroll
    for (int d = 0; d < 3; ++d)
#pragma unroll
      for (int q = 0; q < 4; ++q) pin(xr[d][q & 1][q >> 1]);
    nops<8>();
    relane();
    const bool odd = g & 1;
#pragma unroll
    for (int i = 0; i < 2; ++i)
#pragma unroll
      for (int s = 0; s < 6; ++s) {
        const f32x4 t0 = acc[2 * s][i] * QSCALE, t1 = acc[2 * s + 1][i] * QSCALE;
        const unsigned u0 = cvtpk(t0[0], t0[1]), u1 = cvtpk(t0[2], t0[3]), u2 = cvtpk(t1[0], t1[1]), u3 = cvtpk(t1[2], t1[3]);
        const u32x4 w = {odd ? u2 : u0, odd ? u3 : u1, odd ? u0 : u2, odd ? u1 : u3};
        (i ? st6[s] : qf[s]) = __builtin_bit_cast(bf16x8, w);
      }
  }
  STAMP(1); PUT(40, wacc); CSTAMP(34);

  const int item = (c.br * 4 + c.bp) * 32 + c.h;
  const unsigned vl = (unsigned)lane * 16u;
  const unsigned adone = lds0 + CTL + C_DONE + wid * 256 + lane * 4;
  auto sibo = [&](int rel) { return (unsigned)WS_O + (unsigned)((item & ~3) | ((hq + rel) & 3)) * OBLK + (unsigned)wid * 12288u; };
  bf16x8 sa[3][6];
#pragma unroll
  for (int r = 0; r < 3; ++r)
#pragma unroll
    for (int q = 0; q < 6; ++q) asm volatile("" : "=v"(sa[r][q]));
  {
    unsigned bK[2], bV[4], aK[2], aV[4];
    bK[0] = l15 * 384 + ((g ^ (((l15 >> 1) & 3) << 1)) << 4);
    bK[1] = bK[0] ^ 64;
#pragma unroll
    for (int d4 = 0; d4 < 4; ++d4) bV[d4] = (4 * g + tq) * 384 + (((2 * d4 + (tp >> 1)) ^ (((2 * g + (tq >> 1)) & 3) << 1)) << 4) + 8 * (tp & 1);
    unsigned slot = lds0, gp = D_ATT;
    auto next = [&]() {
      seen(have, pre);
      need(have, ardy, ++gp, wacc);
      const unsigned s = slot;
      slot = slot == lds0 + (NP - 1) * PIECE ? lds0 : slot + PIECE;
      return s;
    };
    auto nextK = [&]() { const unsigned s = next(); aK[0] = bK[0] + s; aK[1] = bK[1] + s; };
    auto nextV = [&]() {
      const unsigned s = next();
#pragma unroll
      for (int d4 = 0; d4 < 4; ++d4) aV[d4] = bV[d4] + s;
    };
    f32x4 o[12], s[2], negm;
    float lsum, sc;
    int mx;
    u32x4 pf;
    bf16x8 w[8];
    bool resc;
    auto rdK = [&](int n) { w[n & 7] = ldsr128(aK[(n % 6) & 1] + ((n % 6) >> 1) * 128 + (n / 6) * 6144); };
    auto vadr = [&](int dt) { return aV[dt & 3] + (dt >> 2) * 128; };
    auto rda = [&](int n) { if (n < 12) rdK(n); else if (n < 24) w[n & 7] = trd2(vadr(n - 12), 6144); else rdK(n - 24); };
    auto mfa = [&](int n) {
      if (n >= 12) mfma(o[n - 12], w[n & 7], __builtin_bit_cast(bf16x8, pf));
      else if (n % 6) mfma(s[n / 6], w[n & 7], qf[n % 6]);
      else mfmac(s[n / 6], w[n & 7], qf[0], negm);
    };
    auto ex = [&](int e) { s[e >> 2][e & 3] = __builtin_amdgcn_exp2f(s[e >> 2][e & 3]); };
    auto ad = [&](int e) { lsum = addf(lsum, s[e >> 2][e & 3]); };
    auto cv = [&]() { pf[0] = cvtpk(s[0][0], s[0][1]); pf[1] = cvtpk(s[0][2], s[0][3]); pf[2] = cvtpk(s[1][0], s[1][1]); pf[3] = cvtpk(s[1][2], s[1][3]); pin(pf); };
    auto mxs = [&](int kt, int hf) { mx = max3i((kt | hf) ? mx : 0, s[kt][2 * hf], s[kt][2 * hf + 1]); };
    auto lanemax = [&]() { return fmaxf(fmaxf(fmaxf(s[0][0], s[0][1]), fmaxf(s[0][2], s[0][3])), fmaxf(fmaxf(s[1][0], s[1][1]), fmaxf(s[1][2], s[1][3]))); };
#define SLOT(n, A) do { mfa(n); FENCE(); rda(n + 8); A; FENCE(); } while (0)
#pragma unroll 1
    for (int it = 0; it < 2; ++it) {
      {
        nextK();
#pragma unroll
        for (int n = 0; n < 8; ++n) rdK(n);
        FENCE();
#pragma unroll
        for (int n = 0; n < 12; ++n) {
          if (n % 6) mfma(s[n / 6], w[n & 7], qf[n % 6]); else mfma0(s[n / 6], w[n & 7], qf[0]);
          FENCE();
          if (n < 4) rdK(n + 8);
          o[n] = f32x4{};
          FENCE();
        }
        cwrite(adone, gp);
        nextK();
#pragma unroll
        for (int n = 0; n < 8; ++n) rdK(n);
        nops<8>();
        const float m = xg_max(lanemax());
        negm = (f32x4){-m, -m, -m, -m};
        s[0] -= m; s[1] -= m;
        lsum = 0.f; sc = 1.f; resc = false;
#pragma unroll
        for (int e = 0; e < 8; ++e) ex(e);
#pragma unroll
        for (int e = 0; e < 8; ++e) ad(e);
        cv();
        nops<2>();
      }
#pragma unroll 1
      for (int st = 1; st < 16; ++st) {
        SLOT(0, pre = cread(ardy));
        SLOT(1, ); SLOT(2, ); SLOT(3, );
        nextV(); FENCE();
        SLOT(4, ); SLOT(5, );
        if (resc) {
#pragma unroll
          for (int dt = 0; dt < 12; ++dt) o[dt] *= sc;
        }
        FENCE();
        SLOT(6, ); SLOT(7, );
        SLOT(8, mxs(0, 0)); SLOT(9, mxs(0, 1));
        SLOT(10, ); SLOT(11, );
        SLOT(12, pre = cread(ardy));
        SLOT(13, );
        SLOT(14, mxs(1, 0); mxs(1, 1));
        resc = __any(mx > __builtin_bit_cast(int, RESC_THR));
        if (resc) {
          const float d = fmaxf(xg_max(lanemax()), 0.f);
          s[0] -= d; s[1] -= d; negm -= d;
          sc = __builtin_amdgcn_exp2f(-d);
          lsum *= sc;
        }
        FENCE();
        SLOT(15, ex(0));
        cwrite(adone, gp);
        if (st < 15) nextK();
        FENCE();
        SLOT(16, ex(1); ad(0)); SLOT(17, ex(2); ad(1)); SLOT(18, ex(3); ad(2)); SLOT(19, ex(4); ad(3));
        SLOT(20, ex(5); ad(4)); SLOT(21, ex(6); ad(5)); SLOT(22, ex(7); ad(6)); SLOT(23, ad(7));
        cv(); FENCE();
        if (it && st == 2) arrive(c, 0, flag0 + F_OA + (unsigned)c.h * 8u);
        if (it && st == SPF) {
          need(haveO, ardy + (C_RDYO - C_RDY), 1, wacc);
#pragma unroll
          for (int r = 0; r < 3; ++r) {
            const unsigned so = sibo(r + 1);
            bload_sc1_into<0>(sa[r][0], vl, c.srdWs, so); bload_sc1_into<1024>(sa[r][1], vl, c.srdWs, so); bload_sc1_into<2048>(sa[r][2], vl, c.srdWs, so);
            bload_sc1_into<0>(sa[r][3], vl, c.srdWs, so + 3072); bload_sc1_into<1024>(sa[r][4], vl, c.srdWs, so + 3072); bload_sc1_into<2048>(sa[r][5], vl, c.srdWs, so + 3072);
          }
        }
        FENCE();
        if (st == 1 || st == 8) { STAMP(2 + 4 * it + (st >> 3)); CSTAMP(56 + 2 * it + (st >> 3)); }
      }
      {
        const float inv = 1.f / xg_sum(lsum);
        const float fo = resc ? sc * inv : inv;
        s[0] *= inv; s[1] *= inv;
        cv();
        nops<4>();
#pragma unroll
        for (int dt = 0; dt < 12; ++dt) o[dt] *= fo;
        FENCE();
        nextV();
        STAMP(4 + 4 * it);
        bf16x8 v[12];
#pragma unroll
        for (int dt = 0; dt < 12; ++dt) v[dt] = trd2(vadr(dt), 6144);
        cwrite(adone, gp);
        FENCE();
#pragma unroll
        for (int dt = 0; dt < 12; ++dt) mfma(o[dt], v[dt], __builtin_bit_cast(bf16x8, pf));
        FENCE();
#pragma unroll
        for (int q = 0; q < 6; ++q) qf[q] = st6[q];
        FENCE();
        const unsigned so = (unsigned)WS_O + (unsigned)item * OBLK + (unsigned)wid * 12288u + (unsigned)it * 6144u;
#pragma unroll
        for (int q = 0; q < 6; ++q) st6[q] = pack2(o[2 * q], o[2 * q + 1]);
        {
        bstore_sc1<0>(st6[0], vl, c.srdWs, so); bstore_sc1<1024>(st6[1], vl, c.srdWs, so); bstore_sc1<2048>(st6[2], vl, c.srdWs, so);
        bstore_sc1<0>(st6[3], vl, c.srdWs, so + 3072); bstore_sc1<1024>(st6[4], vl, c.srdWs, so + 3072); bstore_sc1<2048>(st6[5], vl, c.srdWs, so + 3072);
        }
      }
      STAMP(5 + 4 * it);
    }
  }

  PUT(41, wacc);
  relane();
  bf16x8 sb[3][6];
  auto loadO = [&](int r, int s) {
    const unsigned so = sibo(r + 1) + 6144 + s * 2048;
    bload_sc1<0>(sb[r][2 * s], vl, c.srdWs, so); bload_sc1<1024>(sb[r][2 * s + 1], vl, c.srdWs, so);
    FENCE();
  };
  auto pinO = [&](int r, int s) { pin(sb[r][2 * s]); pin(sb[r][2 * s + 1]); FENCE(); };
  unsigned aO[2];
  aO[0] = lds0 + (4 * g + tq) * 320 + ((tp ^ (g & 1)) << 4) + 8 * (tp & 1);
  aO[1] = aO[0] ^ 8;
  f32x4 y[5][2][2];
  bf16x8 wf[8];
  auto wadr = [&](int n) { const int j = n / 20, k = n % 20; return aO[k & 1] + (j % NWS % 3) * WSLOT + (k / 10) * 10240 + ((k % 10) >> 1) * 64; };
  auto stage = [&](int j, const bf16x8 (&oA)[6], const bf16x8 (&oB)[6]) {
#pragma unroll
    for (int k = 0; k < 20; ++k) {
      const int n = 20 * j + k, sn = (j + 1) % NWS, dlt = sn % 3 ? 0 : sn ? 3 * WSLOT : -6 * WSLOT;
      if (k == 13 && j < 11) { need(have, ardy, D_G4 + j + 2, wacc); FENCE(); }
      if (!(k & 1)) pin(wf[(n + 1) & 7]);
#pragma unroll
      for (int i = 0; i < 2; ++i) {
        const bf16x8& of = (i ? oB : oA)[2 * (j % 3) + k / 10];
        if (n < 10) mfma0(y[(k % 10) >> 1][k & 1][i], wf[n & 7], of);
        else mfma(y[(k % 10) >> 1][k & 1][i], wf[n & 7], of);
        FENCE();
        if (n + 7 < 240) { if (i) trhi(wf[(n + 7) & 7], wadr(n + 7) + 5120); else trlo(wf[(n + 7) & 7], wadr(n + 7)); }
        if (i == 0 && k == 5 && j < 11) { pin(pre); pre = cread(ardy); }
        if (i == 1 && k == 12 && j < 11) { have = rfl(pre); pins(have); }
        if (i == 0 && k == 15 && j < 5) pre = j == 0 ? bump<C_G4>(c.ctl) : j == 1 ? bump<C_G4 + 4>(c.ctl) : j == 2 ? bump<C_G4 + 8>(c.ctl) : j == 3 ? bump<C_G4 + 12>(c.ctl) : bump<C_G4 + 16>(c.ctl);
        if (i == 0 && (k == 12 || k == 13) && dlt && j < 11) { aO[k & 1] += dlt; pin(aO[k & 1]); }
        FENCE();
      }
    }
  };
  need(have, ardy, D_G4 + 1, wacc);
#pragma unroll
  for (int n = 0; n < 7; ++n) wf[n] = trd2(wadr(n), 5120);
  FENCE();
  STAMP(10);
  stage(0, qf, st6);
  arrive(c, 1, flag0 + F_OB + (unsigned)c.h * 8u);
#pragma unroll
  for (int r = 0; r < 3; ++r)
#pragma unroll
    for (int q = 0; q < 6; ++q) pin(sa[r][q]);
  FENCE();
  STAMP(11);
  stage(1, qf, st6);
  stage(2, qf, st6);
  STAMP(12);
  need(haveO, ardy + (C_RDYO - C_RDY), 2, wacc);
  STAMP(13);
  loadO(0, 0); loadO(0, 1); loadO(0, 2); loadO(1, 0); loadO(1, 1); loadO(1, 2);
  STAMP(14);
  waitvm<10>(); pinO(0, 0); STAMP(15); stage(3, sa[0], sb[0]); loadO(2, 0);
  waitvm<10>(); pinO(0, 1); stage(4, sa[0], sb[0]); loadO(2, 1);
  waitvm<10>(); pinO(0, 2); stage(5, sa[0], sb[0]); loadO(2, 2);
  STAMP(16); CSTAMP(60);
  waitvm<6>(); pinO(1, 0); pinO(1, 1); pinO(1, 2); stage(6, sa[1], sb[1]); stage(7, sa[1], sb[1]); stage(8, sa[1], sb[1]);
  STAMP(17); CSTAMP(61);
  waitvm<0>(); pinO(2, 0); pinO(2, 1); pinO(2, 2); stage(9, sa[2], sb[2]); stage(10, sa[2], sb[2]); stage(11, sa[2], sb[2]);
  nops<8>();
  STAMP(20); PUT(42, wacc); CSTAMP(62);

  const int grp = (c.br * 4 + c.bp) * 4 + hq, k8 = c.h >> 2;
  {
    const unsigned so = (unsigned)WS_P + (unsigned)(grp * 8 + k8) * PBLK + (unsigned)wid * 10240u;
#pragma unroll
    for (int i = 0; i < 2; ++i) {
      bstore_sc1<0>(pack2(y[0][0][i], y[0][1][i]), vl, c.srdWs, so + i * 5120); bstore_sc1<1024>(pack2(y[1][0][i], y[1][1][i]), vl, c.srdWs, so + i * 5120);
      bstore_sc1<2048>(pack2(y[2][0][i], y[2][1][i]), vl, c.srdWs, so + i * 5120); bstore_sc1<3072>(pack2(y[3][0][i], y[3][1][i]), vl, c.srdWs, so + i * 5120);
      bstore_sc1<0>(pack2(y[4][0][i], y[4][1][i]), vl, c.srdWs, so + i * 5120 + 4096);
    }
  }
  relane();
  arrive(c, 2, flag0 + F_P + (unsigned)hq * 64u + (unsigned)k8 * 8u);
  STAMP(21);
  need(haveO, ardy + (C_RDYO - C_RDY), 3, wacc);
  STAMP(22);
  {
    bf16x8 pr[8];
    const unsigned so = (unsigned)WS_P + (unsigned)(grp * 8) * PBLK + (unsigned)wid * 10240u + (unsigned)k8 * 1024u;
#pragma unroll
    for (int k = 0; k < 8; ++k) bload_sc1<0>(pr[k], vl, c.srdWs, so + k * PBLK);
    waitvm<0>();
#pragma unroll
    for (int k = 0; k < 8; ++k) pin(pr[k]);
    const int i = k8 >= 5, cp = k8 - 5 * i;
    reduce_store(pr, c.out + (size_t)((2 * c.bp + i) * SQ + wid * 16 + l15) * (2 * DM) + c.br * DM + hq * 160 + cp * 32 + 8 * g, g & 1);
  }
  waitvm<0>();
  STAMP(23); CSTAMP(33);
  ((GAS unsigned*)(ws + (200u << 20)))[(blockIdx.x * 8 + wid) * 64 + lane] = stampv;
  asm volatile(".Lcode_end:");
}

typedef unsigned long long u64;
typedef u64 u64x2 __attribute__((ext_vector_type(2)));
typedef u64 u64x4 __attribute__((ext_vector_type(4)));
__global__ __launch_bounds__(512, 2) void fused_kernel(const u16* xA, const u16* kA, const u16* vA, const u16* wqA, const u16* woA,
                                                       const u16* xB, const u16* kB, const u16* vB, const u16* wqB, const u16* woB,
                                                       u16* out_, char* ws_) {
  extern __shared__ __attribute__((aligned(16))) char lds[];
  Ctx c;
  asm volatile("s_memrealtime %0\n\ts_waitcnt lgkmcnt(0)" : "=s"(c.tent) :: "memory");
  const int xcd = blockIdx.x & 7, idx = blockIdx.x >> 3;
  c.h = 8 * (xcd >> 1) + (idx & 7); c.bp = idx >> 3; c.br = xcd & 1;
  u64x4 p4; u64 p1; u64x2 p2;
  asm volatile("s_load_dwordx8 %0, %3, %4\n\ts_load_dwordx2 %1, %3, %4 offset:32\n\ts_load_dwordx4 %2, %3, 0x50"
               : "=&s"(p4), "=&s"(p1), "=&s"(p2) : "s"(__builtin_amdgcn_kernarg_segment_ptr()), "s"(c.br * 40) : "memory");
  c.lds0 = (unsigned)(uintptr_t)lds - CTL;
  c.ctl = c.lds0 + CTL;
  pins(c.ctl);
  c.wid = rfl(threadIdx.x >> 6);
  {
    unsigned long long pc; unsigned clen;
    asm volatile("s_getpc_b64 %0\n.Lcode_here:\n\ts_mov_b32 %1, .Lcode_end-.Lcode_here" : "=s"(pc), "=s"(clen));
    c.srdC = __builtin_amdgcn_make_buffer_rsrc((void*)pc, (short)0, (int)clen, 0x00020000);
    c.cshare = (unsigned)(idx & 31) * 128u;
    if (c.wid == 0) touch(c.lds0 + CTL + C_JUNK + 1024, threadIdx.x * 4096u, c.srdC, c.cshare);
  }
  cwrite(c.lds0 + CTL + threadIdx.x * 4, 0);
  {
    const int lane = threadIdx.x & 63;
#pragma unroll
    for (int k = 0; k < 3; ++k) {
      const int row = ((8 * k + (lane >> 3)) * 11) >> 5;
      c.vw[0][k] = (unsigned)(row * (HD * 2 - 384) + ((lane ^ ((row >> 1) & 1)) << 4));
      c.vw[1][k] = c.vw[0][k] ^ 64;
      c.vb[k] = (unsigned)((lane ^ (((row >> 1) & 3) << 1)) << 4);
      pin(c.vw[0][k]); pin(c.vw[1][k]); pin(c.vb[k]);
    }
  }
  asm volatile("s_waitcnt lgkmcnt(0)\n\ts_barrier" : "+s"(p4), "+s"(p1), "+s"(p2) :: "memory");
  const u16* x = (const u16*)p4[0]; const u16* kc = (const u16*)p4[1]; const u16* vc = (const u16*)p4[2]; const u16* wq = (const u16*)p4[3]; const u16* wo = (const u16*)p1;
  char* ws = (char*)p2[1];
  c.out = (GAS u16*)p2[0];
  const size_t kvoff = (size_t)(2 * c.bp * NH + c.h) * SK * DH;
  const u64 xb = (u64)(x + (size_t)c.bp * 2 * SQ * DM);
  c.srdX = u32x4{(unsigned)xb, (unsigned)(xb >> 32) & 0xFFFFu, 2 * SQ * DM * 2, 0x00020000};
  c.rot2 = ((c.bp + c.h) & 1) * 2;
  c.srdW = __builtin_amdgcn_make_buffer_rsrc((void*)(wq + c.h * DH), (short)0, (DM * HD - c.h * DH) * 2, 0x00020000);
  c.srdK = __builtin_amdgcn_make_buffer_rsrc((void*)(kc + kvoff), (short)0, KVITEM + SK * DH * 2, 0x00020000);
  c.srdV = __builtin_amdgcn_make_buffer_rsrc((void*)(vc + kvoff), (short)0, KVITEM + SK * DH * 2, 0x00020000);
  c.srdWo = __builtin_amdgcn_make_buffer_rsrc((void*)(wo + (size_t)(c.h >> 2) * 4 * DH * DM + (c.h & 3) * 160), (short)0, (4 * DH * DM - (c.h & 3) * 160) * 2, 0x00020000);
  c.srdWs = __builtin_amdgcn_make_buffer_rsrc((void*)ws, (short)0, (int)WS_END, 0x00020000);
  if (__builtin_expect(c.wid == 7, 1)) {
    __builtin_amdgcn_s_setprio(3);
    loader(c, ws);
    asm volatile("s_endpgm" ::: "memory");
  }
  consumer(c, ws);
}

extern "C" void kernel_launch(void* const* d_in, const int* in_sizes, int n_in, void* d_out, int out_size, void* d_ws, size_t ws_size, hipStream_t stream) {
  (void)in_sizes; (void)n_in; (void)out_size; (void)ws_size;
  (void)hipFuncSetAttribute((const void*)fused_kernel, hipFuncAttributeMaxDynamicSharedMemorySize, LDS_BYTES);
  fused_kernel<<<256, 512, LDS_BYTES, stream>>>((const u16*)d_in[0], (const u16*)d_in[1], (const u16*)d_in[2], (const u16*)d_in[3], (const u16*)d_in[4],
                                                (const u16*)d_in[5], (const u16*)d_in[6], (const u16*)d_in[7], (const u16*)d_in[8], (const u16*)d_in[9],
                                                (u16*)d_out, (char*)d_ws);
}
```

```cpp
#include <hip/hip_runtime.h>
#include <hip/hip_bf16.h>
#include <stdint.h>
#include <type_traits>
#pragma clang diagnostic ignored "-Winline-asm"

typedef unsigned short u16;
typedef short bf16x8 __attribute__((ext_vector_type(8)));
typedef short s16x4 __attribute__((ext_vector_type(4)));
typedef float f32x4 __attribute__((ext_vector_type(4)));
typedef float f32x2 __attribute__((ext_vector_type(2)));
typedef __bf16 bf16x2_t __attribute__((ext_vector_type(2)));
typedef unsigned u32x4 __attribute__((ext_vector_type(4)));
typedef unsigned u32x2 __attribute__((ext_vector_type(2)));
typedef unsigned u32x8 __attribute__((ext_vector_type(8)));
typedef unsigned u32x16 __attribute__((ext_vector_type(16)));
typedef __amdgpu_buffer_rsrc_t rsrc_t;
#define LAS __attribute__((address_space(3)))
#define GAS __attribute__((address_space(1)))
#define DI __device__ __forceinline__

constexpr int SQ = 112, DM = 640, NH = 32, SK = 512, DH = 192, HD = NH * DH;
constexpr int PIECE = 12288;
constexpr int NK = 8;
constexpr int WPC = 10240;
constexpr int CTL = -4096;
constexpr int C_G1 = 0, C_G4 = 40, C_RK = 160, C_RW = 164;
constexpr int C_TAG = 168, C_CNT = 176, C_ZERO = 252, C_DONE = 256, C_JUNK = 2048;
constexpr int LDS_BYTES = 163840;
#define FS 128u
constexpr unsigned F_GRP = 129 * FS < 2048 ? 2048 : 129 * FS;
constexpr unsigned FLAG_BYTES = (8 * F_GRP + 4095) & ~4095u;
constexpr unsigned OBLK = 7 * 12 * 1024;
constexpr unsigned PBLK = 7 * 10 * 1024;
constexpr size_t WS_O = FLAG_BYTES;
constexpr size_t WS_P = WS_O + (size_t)256 * OBLK;
constexpr size_t WS_END = WS_P + (size_t)256 * PBLK;
constexpr float QSCALE = 0.07216878364870322f * 1.4426950408889634f;
constexpr float RESC_THR = 8.0f;
constexpr unsigned KVITEM = NH * SK * DH * 2;
#define XL 2
#define YDLY 3
#define ARR 4
#define PRA 2
#define PW 48
#define PWS 1
#define WOT -2
#define WOS 1
#define TLBS 32768u
#define OST 60
#define PAT2 0xAAAAAAAAu

DI void mfma(f32x4& d, const bf16x8& a, const bf16x8& b) { asm("v_mfma_f32_16x16x32_bf16 %0, %1, %2, %0" : "+v"(d) : "v"(a), "v"(b)); }
DI void mfmac(f32x4& d, const bf16x8& a, const bf16x8& b, const f32x4& c) { asm("v_mfma_f32_16x16x32_bf16 %0, %1, %2, %3" : "=v"(d) : "v"(a), "v"(b), "v"(c)); }
DI void mfma0(f32x4& d, const bf16x8& a, const bf16x8& b) { asm("v_mfma_f32_16x16x32_bf16 %0, %1, %2, 0" : "=v"(d) : "v"(a), "v"(b)); }
#define FENCE() __builtin_amdgcn_sched_barrier(0)
template <int N> DI void nops() { FENCE(); asm volatile("s_nop %0" :: "i"(N - 1)); FENCE(); }
DI int lane_id() { int l; asm volatile("v_mbcnt_lo_u32_b32 %0, -1, 0\n\tv_mbcnt_hi_u32_b32 %0, -1, %0" : "=v"(l)); return l; }
DI unsigned rfl(unsigned x) { return __builtin_amdgcn_readfirstlane(x); }
DI unsigned cvtpk(float lo, float hi) { f32x2 v = {lo, hi}; bf16x2_t b = __builtin_convertvector(v, bf16x2_t); return __builtin_bit_cast(unsigned, b); }
DI s16x4 trd(unsigned a) { return __builtin_bit_cast(s16x4, __builtin_amdgcn_ds_read_tr16_b64_v4i16((LAS s16x4*)a)); }
DI void trlo(bf16x8& d, unsigned a) { const s16x4 t = trd(a); d[0] = t[0]; d[1] = t[1]; d[2] = t[2]; d[3] = t[3]; }
DI void trhi(bf16x8& d, unsigned a) { const s16x4 t = trd(a); d[4] = t[0]; d[5] = t[1]; d[6] = t[2]; d[7] = t[3]; }
DI bf16x8 trd2(unsigned a, unsigned d) { s16x4 lo = trd(a), hi = trd(a + d); return (bf16x8){lo[0], lo[1], lo[2], lo[3], hi[0], hi[1], hi[2], hi[3]}; }
DI bf16x8 ldsr128(unsigned a) { return *(const LAS bf16x8*)a; }
DI bf16x8 pack2(const f32x4& a, const f32x4& b) {
  u32x4 w = {cvtpk(a[0], a[1]), cvtpk(a[2], a[3]), cvtpk(b[0], b[1]), cvtpk(b[2], b[3])};
  return __builtin_bit_cast(bf16x8, w);
}
DI float bflo(unsigned w) { return __uint_as_float(w << 16); }
DI float bfhi(unsigned w) { return __uint_as_float(w & 0xFFFF0000u); }
DI float addf(float a, float b) { float d = a + b; asm("" : "+v"(d)); return d; }
DI int max3i(int a, float b, float c) { return max(max(a, __float_as_int(b)), __float_as_int(c)); }
DI f32x2 sw16(float v) { const unsigned u = __float_as_uint(v); const auto r = __builtin_amdgcn_permlane16_swap(u, u, false, false); return f32x2{__uint_as_float(r[0]), __uint_as_float(r[1])}; }
DI f32x2 sw32(float v) { const unsigned u = __float_as_uint(v); const auto r = __builtin_amdgcn_permlane32_swap(u, u, false, false); return f32x2{__uint_as_float(r[0]), __uint_as_float(r[1])}; }
DI float xg_max(float v) { f32x2 r = sw16(v); r = sw32(fmaxf(r[0], r[1])); return fmaxf(r[0], r[1]); }
DI float xg_sum(float v) { f32x2 r = sw16(v); r = sw32(r[0] + r[1]); return r[0] + r[1]; }

#define KVPOL " nt"
DI void dma4(unsigned m0v, unsigned v0, unsigned v1, unsigned v2, unsigned v3, rsrc_t srd, unsigned soff) {
  asm volatile("s_mov_b32 m0, %0\n\ts_nop 0\n\t"
               "buffer_load_dwordx4 %1, %5, %6 offen lds\n\t"
               "buffer_load_dwordx4 %2, %5, %6 offen offset:1024 lds\n\t"
               "buffer_load_dwordx4 %3, %5, %6 offen offset:2048 lds\n\t"
               "buffer_load_dwordx4 %4, %5, %6 offen offset:3072 lds"
               :: "s"(m0v), "v"(v0), "v"(v1), "v"(v2), "v"(v3), "s"(srd), "s"(soff) : "m0", "memory");
}
DI void dma3k(unsigned m0v, unsigned v0, unsigned v1, unsigned v2, u32x4 srd, unsigned soff) {
  asm volatile("s_mov_b32 m0, %0\n\ts_nop 0\n\t"
               "buffer_load_dwordx4 %1, %4, %5 offen" KVPOL " lds\n\t"
               "buffer_load_dwordx4 %2, %4, %5 offen offset:1024" KVPOL " lds\n\t"
               "buffer_load_dwordx4 %3, %4, %5 offen offset:2048" KVPOL " lds"
               :: "s"(m0v), "v"(v0), "v"(v1), "v"(v2), "s"(srd), "s"(soff) : "m0", "memory");
}
DI void dma3(unsigned m0v, unsigned v0, unsigned v1, unsigned v2, rsrc_t srd, unsigned soff) {
  asm volatile("s_mov_b32 m0, %0\n\ts_nop 0\n\t"
               "buffer_load_dwordx4 %1, %4, %5 offen lds\n\t"
               "buffer_load_dwordx4 %2, %4, %5 offen offset:1024 lds\n\t"
               "buffer_load_dwordx4 %3, %4, %5 offen offset:2048 lds"
               :: "s"(m0v), "v"(v0), "v"(v1), "v"(v2), "s"(srd), "s"(soff) : "m0", "memory");
}
DI void touch(unsigned m0v, unsigned v, rsrc_t srd, unsigned soff) {
  asm volatile("s_mov_b32 m0, %0\n\ts_nop 0\n\tbuffer_load_dword %1, %2, %3 offen lds" :: "s"(m0v), "v"(v), "s"(srd), "s"(soff) : "m0", "memory");
}
template <int OFF> DI void bload_sc1(bf16x8& d, unsigned v, rsrc_t srd, unsigned soff) {
  asm volatile("buffer_load_dwordx4 %0, %1, %2, %3 offen offset:%4 sc1" : "=v"(d) : "v"(v), "s"(srd), "s"(soff), "i"(OFF) : "memory");
}
template <int OFF> DI void bload_sc1_into(bf16x8& d, unsigned v, rsrc_t srd, unsigned soff) {
  asm volatile("buffer_load_dwordx4 %0, %1, %2, %3 offen offset:%4 sc1" : "+v"(d) : "v"(v), "s"(srd), "s"(soff), "i"(OFF) : "memory");
}
template <int OFF> DI void bstore_sc1(const bf16x8& d, unsigned v, rsrc_t srd, unsigned soff) {
  asm volatile("buffer_store_dwordx4 %0, %1, %2, %3 offen offset:%4 sc1\n\ts_nop 1" :: "v"(d), "v"(v), "s"(srd), "s"(soff), "i"(OFF) : "memory");
}
template <int N> DI void waitvm() { asm volatile("s_waitcnt vmcnt(%0)" :: "i"(N) : "memory"); }
template <typename T> DI void pin(T& x) { asm volatile("" : "+v"(x)); }
template <typename T> DI void pins(T& x) { asm volatile("" : "+s"(x)); }
DI void cbar() { asm volatile("" ::: "memory"); }

#define STAMP(i)
#define CSTAMP(i)
#define PUT(i, v)
#define WACC_BEGIN
#define WACC_END(w)

#define G1ST
struct Ctx {
  rsrc_t srdW, srdWo, srdWs, srdC;
  unsigned long long baseK, baseV;
  u32x4 srdX;
  GAS u16* out;
  unsigned lds0, ctl, cshare;
  unsigned vw[2][3], vb[3];
  int wid, br, bp, h, rot2;
};

DI unsigned cread(unsigned a) { return *(const volatile LAS unsigned*)a; }
DI void cwrite(unsigned a, unsigned v) { *(volatile LAS unsigned*)a = v; }
template <int A> DI unsigned bump(unsigned z) { cbar(); const unsigned r = __builtin_amdgcn_ds_append((LAS int*)z + A / 4); cbar(); return r; }
template <int SL> DI void need(unsigned& have, unsigned a, unsigned n, unsigned& wacc) {
  n *= 64;
  if (__builtin_expect(have < n, 0)) {
    WACC_BEGIN;
#pragma unroll 1
    for (unsigned spins = 0;; ++spins) {
      have = rfl(cread(a));
      if (have >= n) break;
      if (spins > (1u << 20)) { have = ~0u; break; }
      __builtin_amdgcn_s_sleep(SL);
    }
    WACC_END(wacc);
  }
  cbar();
}
DI void seen(unsigned& have, unsigned pre) { have = max(have, rfl(pre)); }

struct Tag { unsigned lo, hi; };
DI bool match(const u32x2& f, Tag tag) { return __all(f[0] == tag.lo && f[1] == tag.hi); }
DI void poll_flags(rsrc_t srdWs, unsigned vo, Tag tag) {
#pragma unroll 1
  for (unsigned spins = 0; spins < (1u << 20); ++spins) {
    u32x2 f;
    asm volatile("buffer_load_dwordx2 %0, %1, %2, 0 offen sc1\n\ts_waitcnt vmcnt(0)" : "=v"(f) : "v"(vo), "s"(srdWs) : "memory");
    if (match(f, tag)) break;
    __builtin_amdgcn_s_sleep(2);
  }
}
DI void set_flag(rsrc_t srdWs, unsigned off, int lane, Tag tag) {
  if (lane == 0) { const u32x2 t = {tag.lo, tag.hi}; asm volatile("buffer_store_dwordx2 %0, off, %1, %2 sc1\n\ts_nop 1" :: "v"(t), "s"(srdWs), "s"(off) : "memory"); }
}
DI Tag get_tag(const Ctx& c) { return Tag{rfl(cread(c.lds0 + CTL + C_TAG)), rfl(cread(c.lds0 + CTL + C_TAG + 4))}; }
DI void arrive(const Ctx& c, int which, unsigned last, unsigned off, Tag tag) {
  waitvm<0>();
  const int lane = lane_id();
  unsigned old = 0;
  if (lane == 0) old = __hip_atomic_fetch_add((LAS unsigned*)(c.lds0 + CTL + C_CNT + 4 * which), 1u, __ATOMIC_RELAXED, __HIP_MEMORY_SCOPE_WORKGROUP);
  if (rfl(old) == last) set_flag(c.srdWs, off, lane, tag);
}

DI void reduce_store(const bf16x8 (&pr)[8], GAS u16* dst, bool odd) {
  float a[8] = {0.f, 0.f, 0.f, 0.f, 0.f, 0.f, 0.f, 0.f};
#pragma unroll
  for (int k = 0; k < 8; ++k) {
    const u32x4 w = __builtin_bit_cast(u32x4, pr[k]);
#pragma unroll
    for (int e = 0; e < 4; ++e) { a[2 * e] += bflo(w[e]); a[2 * e + 1] += bfhi(w[e]); }
  }
  const unsigned u0 = cvtpk(a[0], a[1]), u1 = cvtpk(a[2], a[3]), u2 = cvtpk(a[4], a[5]), u3 = cvtpk(a[6], a[7]);
  const u32x4 w = {odd ? u2 : u0, odd ? u3 : u1, odd ? u0 : u2, odd ? u1 : u3};
  *(GAS u32x4*)dst = w;
}
constexpr unsigned F_O = FS < 128 ? 256 : FS, F_P = F_O + 32 * FS, F_TEAM = 64 * FS;

DI void loader(const Ctx& c, char* ws) {
  const int lane = lane_id();
  const unsigned lds0 = c.lds0;
  const int hq = c.h & 3;
  STAMP(0); CSTAMP(32); PUT(30, (unsigned)c.tent);
  const unsigned (&vw)[2][3] = c.vw; const unsigned (&vb)[3] = c.vb;
  auto wqpiece = [&](int t, unsigned m) {
    const unsigned so = (unsigned)(((t ^ c.rot2) >> 1) * 64 + (t & 1) * 8) * (HD * 2);
#pragma unroll
    for (int q = 0; q < 4; ++q) dma3(m + 3072 * q, vw[q & 1][0], vw[q & 1][1], vw[q & 1][2], c.srdW, so + q * (16 * HD * 2));
  };
  u32x4 srdT = {0u, 0u, KVITEM + SK * DH * 2, 0x00020000};
  auto pickKV = [&](unsigned p) {
    const unsigned long long b = (0xD5555554D5555554ull >> p) & 1 ? c.baseV : c.baseK;
    srdT[0] = (unsigned)b; srdT[1] = (unsigned)(b >> 32);
  };
#pragma unroll 1
  for (int t = 0; t < 10; ++t) {
    wqpiece(t, lds0 + (unsigned)(3 + t) * PIECE);
    waitvm<36>(); if (t >= 3) bump<C_RW>(c.ctl);
  }
  STAMP(24);
  const unsigned flag0 = (unsigned)(c.br * 4 + c.bp) * F_GRP;
  u32x2 f;
  asm volatile("buffer_load_dwordx2 %0, off, %1, %2 sc1" : "=v"(f) : "s"(c.srdWs), "s"(flag0) : "memory");
#pragma unroll 1
  for (int p = 0; p < 3; ++p) {
    pickKV(p);
#pragma unroll
    for (int k = 0; k < 4; ++k) dma3k(lds0 + p * PIECE + 3072 * k, vb[0], vb[1], vb[2], srdT, (p & 1) * PIECE + 3072 * k);
    waitvm<36>(); bump<C_RW>(c.ctl);
  }
  pin(f);
  const unsigned long long tg = (((unsigned long long)rfl(f[1]) << 32) | rfl(f[0])) + 1ull;
  const Tag tag{(unsigned)tg, (unsigned)(tg >> 32)};
  if (lane == 0) { cwrite(lds0 + CTL + C_TAG, tag.lo); cwrite(lds0 + CTL + C_TAG + 4, tag.hi); }
  STAMP(25);
  u32x4 st = {0x15u, 3u, 0u, 0u};
  unsigned tK;
  {
    const int q = lane & 31;
    const bool isv = ((q & 1) == 0 && q) || q == 31;
    tK = (unsigned)((isv ? q - 1 : q + 1) >> 1) * PIECE + (unsigned)(lane >> 5) * KVITEM + (unsigned)(lane < 12 ? lane : lane < 24 ? lane - 12 : lane & 7);
  }
  const unsigned atop = lds0 + CTL + (lane > 56 ? C_DONE + (lane - 57) * 260 : C_ZERO);
#define ANNOUNCE(W, B0, B1) "s_mov_b32 m0, 0\n\ts_waitcnt " W "\n\ts_bitcmp1_b32 s60, " B0 "\n\ts_cselect_b64 exec, -1, 0\n\tds_append v54 offset:160\n\t" \
                         "s_bitcmp1_b32 s60, " B1 "\n\ts_cselect_b64 exec, -1, 0\n\tds_append v54 offset:164\n\ts_mov_b64 exec, -1\n\t"
  static_assert(C_RK == 160 && C_RW == 164 && NK == 8 && PIECE == 0x3000, "");
#define LD4(A, B, C, D, R, P) "buffer_load_dwordx4 " A ", " R " offen" P " lds\n\tbuffer_load_dwordx4 " B ", " R " offen offset:1024" P " lds\n\t" \
                              "buffer_load_dwordx4 " C ", " R " offen offset:2048" P " lds\n\tbuffer_load_dwordx4 " D ", " R " offen offset:3072" P " lds\n\t"
#define ASTAMP(N, B)
  const unsigned stampv = 0;
#define STREAM(WTAIL, N3, N2, N1) \
      "s_mov_b32 s69, 0\n\tds_read_b32 v53, v47\n\ts_waitcnt lgkmcnt(0)\n" \
      "1:\tv_cmp_ge_i32_e64 s[64:65], v53, v48\n\t" \
      "v_readlane_b32 s66, v50, s61\n\tv_readlane_b32 s67, v51, s69\n\tv_readlane_b32 s68, v52, s69\n\t" \
      "s_add_u32 s71, s61, s69\n\ts_cmp_eq_u32 s71, s56\n\ts_cbranch_scc1 9f\n\t" \
      "s_and_b32 s70, s66, 15\n\ts_mulk_i32 s70, 0x3000\n\ts_andn2_b32 s66, s66, 15\n\ts_bitcmp1_b32 s58, s71\n\ts_cbranch_scc0 4f\n\t" \
      "s_bitcmp1_b64 s[64:65], s69\n\ts_cbranch_scc1 2f\n" \
      "4:\ts_cmp_ge_u32 s65, s62\n\ts_cbranch_scc0 3f\n\t" \
      "s_add_u32 m0, s70, s54\n\ts_bitcmp1_b64 s[52:53], s61\n\ts_cselect_b64 s[44:45], s[50:51], s[48:49]\n\t" \
      "v_add_u32 v48, v48, v49\n\ts_ashr_i32 s62, s62, s55\n\ts_add_u32 s61, s61, 1\n\ts_cmp_gt_u32 s61, 64\n\ts_cbranch_scc1 1b\n\t" \
      LD4("v44", "v45", "v46", "v44", "s[44:47], s66", KVPOL) \
      "s_add_u32 m0, m0, 0x1000\n\ts_add_u32 s66, s66, 0x1000\n\t" \
      LD4("v45", "v46", "v44", "v45", "s[44:47], s66", KVPOL) \
      "s_add_u32 m0, m0, 0x1000\n\ts_add_u32 s66, s66, 0x1000\n\tds_read_b32 v53, v47\n\t" \
      LD4("v46", "v44", "v45", "v46", "s[44:47], s66", KVPOL) \
      "s_lshl2_add_u32 s60, s60, 1\n\t" \
      ANNOUNCE("vmcnt(" N3 ") lgkmcnt(0)", "6", "7") ASTAMP("s61", "9") \
      "s_branch 1b\n" \
      "2:\ts_mov_b32 m0, s68\n\ts_lshl2_add_u32 s60, s60, 2\n\t" \
      LD4("v32", "v33", "v34", "v35", "s[40:43], s67", "") \
      "s_add_u32 m0, m0, 0x1000\n\ts_add_u32 s69, s69, 1\n\t" \
      LD4("v36", "v37", "v38", "v39", "s[40:43], s67", "") \
      "s_add_u32 m0, m0, 0x1000\n\tds_read_b32 v53, v47\n\t" \
      WTAIL \
      ANNOUNCE("vmcnt(" N3 ") lgkmcnt(0)", "6", "7") ASTAMP("s69", "s57") \
      "s_branch 1b\n" \
      "3:\ts_bitcmp1_b64 s[64:65], s69\n\ts_cbranch_scc1 2b\n\t" \
        \
      "ds_read_b32 v53, v47\n\ts_getreg_b32 s70, hwreg(HW_REG_IB_STS)\n\ts_bfe_u32 s71, s70, 0x20016\n\ts_and_b32 s70, s70, 15\n\ts_lshl4_add_u32 s70, s71, s70\n\t" \
      "s_and_b32 s71, s60, 0x30\n\ts_cbranch_scc0 5f\n\ts_cmp_gt_u32 s70, " N2 "\n\ts_cbranch_scc1 8f\n\t" \
      ANNOUNCE("vmcnt(" N2 ")", "4", "5") "s_and_b32 s60, s60, 15\n\ts_branch 8f\n" \
      "5:\ts_and_b32 s71, s60, 12\n\ts_cbranch_scc0 6f\n\ts_cmp_gt_u32 s70, " N1 "\n\ts_cbranch_scc1 8f\n\t" \
      ANNOUNCE("vmcnt(" N1 ")", "2", "3") "s_and_b32 s60, s60, 3\n\ts_branch 8f\n" \
      "6:\ts_and_b32 s71, s60, 3\n\ts_cbranch_scc0 8f\n\ts_cmp_lg_u32 s70, 0\n\ts_cbranch_scc1 8f\n\t" \
      ANNOUNCE("vmcnt(0)", "0", "1") "s_mov_b32 s60, 0\n" \
      "8:\ts_add_u32 s63, s63, 1\n\ts_waitcnt lgkmcnt(0)\n\ts_cmp_lt_u32 s63, 0x100000\n\ts_cbranch_scc1 1b\n\t" \
      "v_mov_b32 v53, 0x7ffffffe\n\ts_nop 0\n\tv_writelane_b32 v53, 0, 56\n\ts_branch 1b\n" \
      "9:\ts_waitcnt lgkmcnt(0)" \
      : "+{s[60:63]}"(st), "+{v[48:55]}"(vt), "+{s[44:47]}"(sk) \
      : "{v[32:47]}"(va), "{s[40:43]}"(srd), "{s[48:55]}"(sa), "{s[56:59]}"(sc) \
      : "s64", "s65", "s66", "s67", "s68", "s69", "s70", "s71", "m0", "scc", "memory"
  auto stream = [&](auto wo, rsrc_t srd, const unsigned (&vo)[12], unsigned aG, int thr, int inc, unsigned tW, unsigned tM, unsigned total, unsigned req, unsigned sb, unsigned pat) {
    if (lane >= 56) { aG = atop; thr = (int)st[1] - (lane == 56 ? 71 : wo ? 7 : 11); inc = 1; }
    st[2] = req;
    const u32x16 va = {vo[0], vo[1], vo[2], vo[3], vo[4], vo[5], vo[6], vo[7], vo[8], vo[9], vo[10], vo[11], vb[0], vb[1], vb[2], aG};
    u32x8 vt = {(unsigned)thr, (unsigned)inc, tK, tW, tM, 0u, 0u, stampv};
    const u32x8 sa = {(unsigned)c.baseK, (unsigned)(c.baseK >> 32), (unsigned)c.baseV, (unsigned)(c.baseV >> 32), 0xD5555554u, 0xD5555554u, lds0, wo ? 0u : 1u};
    const u32x4 sc = {total, sb, pat, 0u};
    u32x4 sk = {0u, 0u, KVITEM + SK * DH * 2, 0x00020000};
    if constexpr (wo) asm volatile(STREAM("buffer_load_dwordx4 v40, s[40:43], s67 offen lds\n\tbuffer_load_dwordx4 v41, s[40:43], s67 offen offset:1024 lds\n\t", "30", "20", "10"));
    else asm volatile(STREAM(LD4("v40", "v41", "v42", "v43", "s[40:43], s67", ""), "36", "24", "12"));
    st = u32x4{rfl(st[0]), rfl(st[1]), 0u, rfl(st[3])};
  };
  {
    unsigned vo[12];
#pragma unroll
    for (int n = 0; n < 12; ++n) vo[n] = vw[(n / 3) & 1][n % 3] + (unsigned)(1024 * (n % 3) + (n / 3) * (16 * HD * 2) - 1024 * (n % 4));
    const int i = lane, t = i + 10;
    const int gl = i < 10 ? i : i > 46 ? 55 - i : 0;
    const int gt = i < 10 ? 7 * 64 : i == 10 ? 0x7FFFFFFF : i > 46 ? 14 * 64 : 0;
    stream(std::false_type{}, c.srdW, vo, lds0 + CTL + C_G1 + gl * 4, gt, 0, (unsigned)(((t ^ c.rot2) >> 1) * 64 + (t & 1) * 8) * (HD * 2),
           lds0 + (unsigned)(3 + i) * PIECE, 32 + 10, 0xFF800000u, 36, ~0u);
  }
  STAMP(1); CSTAMP(34);
  {
    unsigned vo[12];
#pragma unroll
    for (int n = 0; n < 10; ++n) {
      const int row = ((16 * (n % 5) + (lane >> 2)) * 205) >> 10;
      vo[n] = (unsigned)(row * (DM * 2 - 320) + ((lane ^ ((row >> 2) & 1)) << 4) + 1024 * (n % 5) + (n / 5) * (16 * DM * 2) - 1024 * (n % 4));
    }
    vo[10] = vo[11] = 0;
    const int j = lane, jj = j < 14 ? j : j - 14;
    static_assert(PW > 40, "K/V piece 40 has left: wq and the first two rounds of K/V are through");
    const int ga = j < 14 ? C_ZERO : C_G4 + 4 * jj;
    const int gt = j < 6 ? PW + PWS * j - 32 : j < 14 ? 59 + j - 32 : j < 24 ? 7 * 64 : 0x7FFFFFFF;
    stream(std::true_type{}, c.srdWo, vo, lds0 + CTL + ga, gt, j < 14 ? -1 : 0, (unsigned)((hq * 6 + j) % 24) * (32 * DM * 2),
           lds0 + (unsigned)(jj < 6 ? NK * PIECE + jj * WPC : (jj - 6) * PIECE), 72 + 24, 0xFF000000u, 41, PAT2);
    asm volatile(ANNOUNCE("vmcnt(20)", "4", "5") ANNOUNCE("vmcnt(10)", "2", "3") ANNOUNCE("vmcnt(0)", "0", "1") "s_waitcnt lgkmcnt(0)"
                 :: "{s[60:63]}"(st) : "v54", "m0", "scc", "memory");
  }
  PUT(8, (unsigned)c.tent + st[3]);
  STAMP(6);
  poll_flags(c.srdWs, flag0 + F_TEAM + F_P + (unsigned)(hq * 8 + (lane & 7)) * FS, tag);
  if (c.h == 0) set_flag(c.srdWs, flag0, lane, tag);
  STAMP(7);
  {
    const int k8 = c.h >> 2, w = 3 + (k8 >> 1), u = k8 & 1;
    const unsigned so = (unsigned)WS_P + (unsigned)(((c.br * 4 + c.bp) * 4 + hq) * 8) * PBLK + (unsigned)w * 10240u + 8192u + (unsigned)u * 1024u;
    bf16x8 pr[8];
#pragma unroll
    for (int k = 0; k < 8; ++k) bload_sc1<0>(pr[k], lane * 16, c.srdWs, so + k * PBLK);
    waitvm<0>();
#pragma unroll
    for (int k = 0; k < 8; ++k) pin(pr[k]);
    reduce_store(pr, c.out + (size_t)(2 * c.bp * SQ + (w == 3 ? 208 : 32 * w) + (lane & 15)) * (2 * DM) + c.br * DM + hq * 160 + (3 + u) * 32 + 8 * (lane >> 4), (lane >> 4) & 1);
  }
}

DI void consumer(const Ctx& c, char* ws) {
  const int wid = c.wid;
  int lane, l15, g, tq, tp;
  auto relane = [&]() { lane = lane_id(); l15 = lane & 15; g = lane >> 4; tq = l15 >> 2; tp = lane & 3; };
  relane();
  const unsigned lds0 = c.lds0;
  STAMP(0); CSTAMP(32); PUT(30, (unsigned)c.tent);
  const int hq = c.h & 3;
  const bool late = wid > 3;
  const int row0 = 32 * wid - (late ? 16 : 0), drow = wid == 3 ? SQ : 16;
  unsigned ardy = lds0 + CTL + C_RW;
  pin(ardy);
  unsigned have = 0, pre = 0, wacc = 1;
  auto touchWo = [&](int n) {
    touch(lds0 + CTL + C_JUNK, (unsigned)lane_id() * 128u, c.srdWo, (unsigned)(c.bp * 4 + hq) * 61440u - (unsigned)hq * 320u + (unsigned)n * 8192u);
  };

  bf16x8 qf[6], st6[6];
  {
    bf16x8 xr[5][2][2], xa[2], a[8];
    unsigned vx[2];
    vx[0] = (unsigned)((row0 + (l15 & 14)) * (DM * 2) + g * 32 + (lane & 1) * 16);
    vx[1] = vx[0] + drow * (DM * 2);
    u32x4 srdX = c.srdX;
    unsigned sx[2] = {(unsigned)c.rot2 * 64u + XL * 128u, XL * 128u - (unsigned)c.rot2 * 64u};
#define XLOAD(C, D, V, S, OFF) asm volatile("buffer_load_dwordx4 %0, %1, %2, %3 offen offset:%4" : C(D) : "v"(V), "s"(srdX), "s"(S), "i"(OFF) : "memory")
    auto xfirst = [&](int d) {
#pragma unroll
      for (int q = 0; q < 4; ++q) XLOAD("=v", xr[d][q & 1][q >> 1], vx[q >> 1], sx[d & 1] - XL * 128u + (d & 1 ? 128u : 0u), (d & ~1) * 128 + (q & 1) * (DM * 2));
    };
    unsigned long long even = 0x5555555555555555ull;
    pins(even);
    auto trade = [&](int d, int n) {
      const int i = (n >> 2) & 1, e = n & 3;
      u32x4 r0 = __builtin_bit_cast(u32x4, xr[d][0][i]), r1 = __builtin_bit_cast(u32x4, xr[d][1][i]), t = __builtin_bit_cast(u32x4, xa[i]);
#define XSEL "v_cndmask_b32_dpp %0, %1, %2, vcc quad_perm:[1,0,3,2] row_mask:0xf bank_mask:0xf"
      register unsigned long long m asm("vcc") = n < 8 ? even : ~even;
      if (n < 8) { asm volatile(XSEL : "=v"(t[e]) : "v"(r1[e]), "v"(r0[e]), "s"(m)); xa[i] = __builtin_bit_cast(bf16x8, t); }
      else { asm volatile(XSEL : "=v"(r1[e]) : "v"(r0[e]), "0"(r1[e]), "s"(m)); xr[d][1][i] = __builtin_bit_cast(bf16x8, r1); }
    };
    unsigned pos = 0, last = 11, lap = 0;
    if (late) need<8>(have, ardy, YDLY, wacc); else __builtin_amdgcn_s_setprio(1);
#pragma unroll
    for (int d = 0; d < 5; ++d) {
      if (d < XL) xfirst(d);
      else
#pragma unroll
        for (int q = 0; q < 4; ++q) asm volatile("" : "=v"(xr[d][q & 1][q >> 1]));
    }
    asm volatile("" : "=v"(xa[0])); asm volatile("" : "=v"(xa[1]));
    unsigned aW[2][2][2];
#pragma unroll
    for (int s1 = 0; s1 < 2; ++s1)
#pragma unroll
      for (int T = 0; T < 2; ++T) {
        aW[1][s1][T] = lds0 + NK * PIECE + (8 * g + tq) * 384 + (((4 * s1 + tp) ^ ((tq >> 1) | ((g & 1) << 2))) << 4) + 8 * (T ^ (tp & 1));
        aW[0][s1][T] = aW[1][s1][T] - 5 * PIECE;
        pin(aW[0][s1][T]); pin(aW[1][s1][T]);
      }
    f32x4 acc[12][2];
#pragma unroll
    for (int dt = 0; dt < 12; ++dt) { acc[dt][0] = f32x4{}; acc[dt][1] = f32x4{}; pin(acc[dt][0]); pin(acc[dt][1]); }
    auto adr = [&](int t, int K) { return aW[t / 5][(K >> 1) & 1][K & 1] + (t % 5) * PIECE + (K >> 2) * 128; };
    auto xwait = [&](int d) {
      waitvm<4 * XL - 4>();
#pragma unroll
      for (int q = 0; q < 4; ++q) pin(xr[d][q & 1][q >> 1]);
      FENCE();
    };
    need<1>(have, ardy, pos + 1, wacc);
    STAMP(29);
#pragma unroll
    for (int k = 0; k < 7; ++k) { a[k] = trd2(adr(0, k), 1536); FENCE(); }
    xwait(0);
#pragma unroll
    for (int n = 0; n < 16; ++n) trade(0, n);
    nops<2>();
    auto step = [&](auto tc) {
      constexpr int t = decltype(tc)::value, T = t & 1, d = t >> 1, r = (d + XL) % 5;
#pragma unroll
      for (int k = 0; k < 12; ++k) {
        const int b = (12 * t + k) & 7, f = (b + 7) & 7;
        const int n = k < 3 ? 2 * k - 2 : k == 3 ? 4 : k == 4 ? 5 : 2 * k - 5;
        if (k == 5) { pre = bump<C_G1 + 4 * t>(c.ctl); need<1>(have, ardy, t < 9 ? pos + t + 2 : last, wacc); FENCE(); }
        if (T && k == 1) xwait((d + 1) % 5);
        const unsigned ra = k < 5 ? adr(t, k + 7) : adr((t + 1) % 10, k - 5);
        if (!(k & 1)) pin(a[(b + 1) & 7]);
        mfma(acc[k][0], a[b], T ? xr[d][1][0] : xa[0]); FENCE();
        trlo(a[f], ra);
        if (k == 10) { pin(pre); pre = cread(ardy); }
        if (T && k >= 1 && k <= 9 && k != 5) trade((d + 1) % 5, n);
        FENCE();
        mfma(acc[k][1], a[b], T ? xr[d][1][1] : xa[1]); FENCE();
        trhi(a[f], ra + 1536);
        if (k == 3) { have = rfl(pre); pins(have); }
        if (T && k >= 1 && k <= 10 && k != 3 && k != 5) trade((d + 1) % 5, k == 10 ? 15 : n + 1);
        if (!T && (k == 1 || k == 2 || k == 7 || k == 8)) {
          const int q = k < 5 ? k - 1 : k - 5;
          XLOAD("+v", xr[r][q & 1][q >> 1], vx[q >> 1], sx[(d + XL) & 1], d * 128 + (q & 1) * (DM * 2));
        }
        if (t == 9 - 2 * XL && k == 8) { srdX[2] = lap ? 0u : srdX[2]; pins(srdX); }
        FENCE();
      }
    };
#define IC(n) std::integral_constant<int, n>{}
#pragma unroll 1
    for (; lap < 2; ++lap) {
      step(IC(0)); step(IC(1)); step(IC(2)); step(IC(3)); step(IC(4));
      step(IC(5)); step(IC(6)); step(IC(7)); step(IC(8)); step(IC(9));
      G1ST { STAMP(24 + lap); }
      if (WOT < 0 && late && lap == -1 - WOT) { touchWo(wid - 4); touchWo(wid - 1); if (wid < 6) touchWo(wid + 2); }
      pos += 10; last = 0;
      const unsigned u = sx[0]; sx[0] = sx[1] + 640u; sx[1] = u + 640u;
    }
    waitvm<0>();
#pragma unroll
    for (int d = 0; d < 5; ++d)
#pragma unroll
      for (int q = 0; q < 4; ++q) pin(xr[d][q & 1][q >> 1]);
    nops<8>();
    relane();
    const bool odd = g & 1;
#pragma unroll
    for (int i = 0; i < 2; ++i)
#pragma unroll
      for (int s = 0; s < 6; ++s) {
        const f32x4 t0 = acc[2 * s][i] * QSCALE, t1 = acc[2 * s + 1][i] * QSCALE;
        const unsigned u0 = cvtpk(t0[0], t0[1]), u1 = cvtpk(t0[2], t0[3]), u2 = cvtpk(t1[0], t1[1]), u3 = cvtpk(t1[2], t1[3]);
        const u32x4 w = {odd ? u2 : u0, odd ? u3 : u1, odd ? u0 : u2, odd ? u1 : u3};
        (i ? st6[s] : qf[s]) = __builtin_bit_cast(bf16x8, w);
      }
  }
  STAMP(1); PUT(40, wacc); CSTAMP(34);
  __builtin_amdgcn_s_setprio(PRA);

  const int item = (c.br * 4 + c.bp) * 32 + c.h;
  const unsigned vl = (unsigned)lane * 16u;
  const unsigned sown = (unsigned)WS_O + (unsigned)item * OBLK + (unsigned)wid * 12288u;
  auto storeO = [&](const bf16x8 (&q)[6], unsigned so) {
    bstore_sc1<0>(q[0], vl, c.srdWs, so); bstore_sc1<1024>(q[1], vl, c.srdWs, so); bstore_sc1<2048>(q[2], vl, c.srdWs, so);
    bstore_sc1<0>(q[3], vl, c.srdWs, so + 3072); bstore_sc1<1024>(q[4], vl, c.srdWs, so + 3072); bstore_sc1<2048>(q[5], vl, c.srdWs, so + 3072);
  };
  {
    const unsigned adone = lds0 + CTL + C_DONE + wid * 256 + lane * 4;
    const unsigned ardk = ardy + (C_RK - C_RW);
    unsigned bK[2], bV[4], aK[2], aV[4];
    bK[0] = l15 * 384 + ((g ^ (((l15 >> 1) & 3) << 1)) << 4);
    bK[1] = bK[0] ^ 64;
#pragma unroll
    for (int d4 = 0; d4 < 4; ++d4) bV[d4] = (4 * g + tq) * 384 + (((2 * d4 + (tp >> 1)) ^ (((2 * g + (tq >> 1)) & 3) << 1)) << 4) + 8 * (tp & 1);
    unsigned slot = lds0, gp = late ? 32 : 0;
    have = 0; pre = 0;
    auto next = [&]() {
      seen(have, pre);
      need<1>(have, ardk, ++gp, wacc);
      const unsigned s = slot;
      slot = slot == lds0 + (gp > 24 ? NK - 1 : 11) * PIECE ? lds0 : slot + PIECE;
      return s;
    };
    auto nextK = [&]() { const unsigned s = next(); aK[0] = bK[0] + s; aK[1] = bK[1] + s; };
    auto nextV = [&]() {
      const unsigned s = next();
#pragma unroll
      for (int d4 = 0; d4 < 4; ++d4) aV[d4] = bV[d4] + s;
    };
    int mx;
    bf16x8 w[8];
    bool resc;
    auto rdK = [&](int n) { w[n & 7] = ldsr128(aK[(n % 6) & 1] + ((n % 6) >> 1) * 128 + (n / 6) * 6144); };
    auto vadr = [&](int dt) { return aV[dt & 3] + (dt >> 2) * 128; };
    auto lanemax = [&](const f32x4 (&s)[2]) { return fmaxf(fmaxf(fmaxf(s[0][0], s[0][1]), fmaxf(s[0][2], s[0][3])), fmaxf(fmaxf(s[1][0], s[1][1]), fmaxf(s[1][2], s[1][3]))); };
    auto cvp = [&](u32x4& pf, const f32x4 (&s)[2]) { pf[0] = cvtpk(s[0][0], s[0][1]); pf[1] = cvtpk(s[0][2], s[0][3]); pf[2] = cvtpk(s[1][0], s[1][1]); pf[3] = cvtpk(s[1][2], s[1][3]); pin(pf); };
    if (wid != 3) {
      f32x4 o[2][12], s[2][2], negm[2];
      float lsum[2], sc[2];
      u32x4 pf[2];
      auto rdh = [&](int m, int hf) {
        if (m >= 12 && m < 24) { if (hf) trhi(w[m & 7], vadr(m - 12) + 6144); else trlo(w[m & 7], vadr(m - 12)); }
        else if (!hf) rdK(m % 24);
      };
      auto mfa = [&](int n, int i) {
        if (n >= 12) mfma(o[i][n - 12], w[n & 7], __builtin_bit_cast(bf16x8, pf[i]));
        else if (n % 6) mfma(s[i][n / 6], w[n & 7], (i ? st6 : qf)[n % 6]);
        else mfmac(s[i][n / 6], w[n & 7], (i ? st6 : qf)[0], negm[i]);
      };
      auto ex = [&](int e) { s[e >> 3][(e >> 2) & 1][e & 3] = __builtin_amdgcn_exp2f(s[e >> 3][(e >> 2) & 1][e & 3]); };
      auto ad = [&](int e) { lsum[e >> 3] = addf(lsum[e >> 3], s[e >> 3][(e >> 2) & 1][e & 3]); };
      auto mxs = [&](int i, int kt, int hf) { mx = max3i((i | kt | hf) ? mx : 0, s[i][kt][2 * hf], s[i][kt][2 * hf + 1]); };
#define SLOT2(n, A, B) do { if (!((n) & 1)) pin(w[((n) + 1) & 7]); mfa(n, 0); FENCE(); rdh(n + 7, 0); A; FENCE(); mfa(n, 1); FENCE(); rdh(n + 7, 1); B; FENCE(); } while (0)
      {
        nextK();
#pragma unroll
        for (int n = 0; n < 7; ++n) rdK(n);
        FENCE();
#pragma unroll
        for (int n = 0; n < 12; ++n) {
#pragma unroll
          for (int i = 0; i < 2; ++i) {
            if (n % 6) mfma(s[i][n / 6], w[n & 7], (i ? st6 : qf)[n % 6]); else mfma0(s[i][n / 6], w[n & 7], (i ? st6 : qf)[0]);
            FENCE();
            if (!i && n < 5) rdK(n + 7);
            o[i][n] = f32x4{};
            FENCE();
          }
        }
        cwrite(adone, gp);
        nextK();
#pragma unroll
        for (int n = 0; n < 7; ++n) rdK(n);
        nops<8>();
#pragma unroll
        for (int i = 0; i < 2; ++i) {
          const float m = xg_max(lanemax(s[i]));
          negm[i] = (f32x4){-m, -m, -m, -m};
          s[i][0] -= m; s[i][1] -= m;
          lsum[i] = 0.f; sc[i] = 1.f;
        }
        resc = false;
#pragma unroll
        for (int e = 0; e < 16; ++e) ex(e);
#pragma unroll
        for (int e = 0; e < 16; ++e) ad(e);
        cvp(pf[0], s[0]); cvp(pf[1], s[1]);
        nops<2>();
      }
#pragma unroll 1
      for (int st = 1; st < 16; ++st) {
        SLOT2(0, , ); SLOT2(1, , ); SLOT2(2, , pre = cread(ardk)); SLOT2(3, , ); SLOT2(4, , cwrite(adone, gp));
        nextV(); FENCE();
        SLOT2(5, , );
        if (resc) {
#pragma unroll
          for (int dt = 0; dt < 12; ++dt) { o[0][dt] *= sc[0]; o[1][dt] *= sc[1]; }
        }
        FENCE();
        SLOT2(6, , );
        SLOT2(7, mxs(0, 0, 0), mxs(0, 0, 1)); SLOT2(8, mxs(1, 0, 0), mxs(1, 0, 1));
        SLOT2(9, , ); SLOT2(10, , ); SLOT2(11, , ); SLOT2(12, , );
        SLOT2(13, mxs(0, 1, 0), mxs(0, 1, 1)); SLOT2(14, mxs(1, 1, 0); pre = cread(ardk), mxs(1, 1, 1));
        resc = __any(mx > __builtin_bit_cast(int, RESC_THR));
        if (resc) {
#pragma unroll
          for (int i = 0; i < 2; ++i) {
            const float d = fmaxf(xg_max(lanemax(s[i])), 0.f);
            s[i][0] -= d; s[i][1] -= d; negm[i] -= d;
            sc[i] = __builtin_amdgcn_exp2f(-d);
            lsum[i] *= sc[i];
          }
        }
        FENCE();
        SLOT2(15, ex(0), ex(1); ad(0)); SLOT2(16, ex(2); ad(1), ex(3); ad(2));
        cwrite(adone, gp);
        if (st < 15) nextK();
        FENCE();
        SLOT2(17, ex(4); ad(3), ex(5); ad(4)); SLOT2(18, ex(6); ad(5), ex(7); ad(6)); SLOT2(19, ex(8); ad(7), ex(9); ad(8));
        SLOT2(20, ex(10); ad(9), ex(11); ad(10)); SLOT2(21, ex(12); ad(11), ex(13); ad(12)); SLOT2(22, ex(14); ad(13), ex(15); ad(14));
        SLOT2(23, ad(15); cvp(pf[0], s[0]), cvp(pf[1], s[1]));
        if (st == 1 || st == 8) { STAMP(2 + (st >> 3)); CSTAMP(56 + (st >> 3)); }
      }
      {
        float fo[2];
#pragma unroll
        for (int i = 0; i < 2; ++i) {
          const float inv = 1.f / xg_sum(lsum[i]);
          fo[i] = resc ? sc[i] * inv : inv;
          s[i][0] *= inv; s[i][1] *= inv;
          cvp(pf[i], s[i]);
        }
        nops<4>();
#pragma unroll
        for (int dt = 0; dt < 12; ++dt) { o[0][dt] *= fo[0]; o[1][dt] *= fo[1]; }
        FENCE();
        nextV();
        STAMP(8);
        bf16x8 v[12];
#pragma unroll
        for (int dt = 0; dt < 12; ++dt) v[dt] = trd2(vadr(dt), 6144);
        cwrite(adone, 64);
        FENCE();
#pragma unroll
        for (int i = 0; i < 2; ++i)
#pragma unroll
          for (int dt = 0; dt < 12; ++dt) mfma(o[i][dt], v[dt], __builtin_bit_cast(bf16x8, pf[i]));
        FENCE();
#pragma unroll
        for (int q = 0; q < 6; ++q) qf[q] = pack2(o[0][2 * q], o[0][2 * q + 1]);
        need<8>(have, ardk, OST, wacc);
        storeO(qf, sown);
        FENCE();
#pragma unroll
        for (int q = 0; q < 6; ++q) st6[q] = pack2(o[1][2 * q], o[1][2 * q + 1]);
        storeO(st6, sown + 6144);
      }
    } else {
      f32x4 o[12], s[2], negm;
      float lsum, sc;
      u32x4 pf;
      auto rda = [&](int n) { if (n < 12) rdK(n); else if (n < 24) w[n & 7] = trd2(vadr(n - 12), 6144); else rdK(n - 24); };
      auto mfa = [&](int n) {
        if (n >= 12) mfma(o[n - 12], w[n & 7], __builtin_bit_cast(bf16x8, pf));
        else if (n % 6) mfma(s[n / 6], w[n & 7], qf[n % 6]);
        else mfmac(s[n / 6], w[n & 7], qf[0], negm);
      };
      auto ex = [&](int e) { s[e >> 2][e & 3] = __builtin_amdgcn_exp2f(s[e >> 2][e & 3]); };
      auto ad = [&](int e) { lsum = addf(lsum, s[e >> 2][e & 3]); };
      auto mxs = [&](int kt, int hf) { mx = max3i((kt | hf) ? mx : 0, s[kt][2 * hf], s[kt][2 * hf + 1]); };
#define SLOT(n, A) do { mfa(n); FENCE(); rda(n + 8); A; FENCE(); } while (0)
#pragma unroll 1
      for (int it = 0; it < 2; ++it) {
        {
          nextK();
#pragma unroll
          for (int n = 0; n < 8; ++n) rdK(n);
          FENCE();
#pragma unroll
          for (int n = 0; n < 12; ++n) {
            if (n % 6) mfma(s[n / 6], w[n & 7], qf[n % 6]); else mfma0(s[n / 6], w[n & 7], qf[0]);
            FENCE();
            if (n < 4) rdK(n + 8);
            o[n] = f32x4{};
            FENCE();
          }
          cwrite(adone, gp);
          nextK();
#pragma unroll
          for (int n = 0; n < 8; ++n) rdK(n);
          nops<8>();
          const float m = xg_max(lanemax(s));
          negm = (f32x4){-m, -m, -m, -m};
          s[0] -= m; s[1] -= m;
          lsum = 0.f; sc = 1.f; resc = false;
#pragma unroll
          for (int e = 0; e < 8; ++e) ex(e);
#pragma unroll
          for (int e = 0; e < 8; ++e) ad(e);
          cvp(pf, s);
          nops<2>();
        }
#pragma unroll 1
        for (int st = 1; st < 16; ++st) {
          SLOT(0, pre = cread(ardk));
          SLOT(1, ); SLOT(2, ); SLOT(3, cwrite(adone, gp));
          nextV(); FENCE();
          SLOT(4, ); SLOT(5, );
          if (resc) {
#pragma unroll
            for (int dt = 0; dt < 12; ++dt) o[dt] *= sc;
          }
          FENCE();
          SLOT(6, ); SLOT(7, );
          SLOT(8, mxs(0, 0)); SLOT(9, mxs(0, 1));
          SLOT(10, ); SLOT(11, );
          SLOT(12, pre = cread(ardk));
          SLOT(13, );
          SLOT(14, mxs(1, 0); mxs(1, 1));
          resc = __any(mx > __builtin_bit_cast(int, RESC_THR));
          if (resc) {
            const float d = fmaxf(xg_max(lanemax(s)), 0.f);
            s[0] -= d; s[1] -= d; negm -= d;
            sc = __builtin_amdgcn_exp2f(-d);
            lsum *= sc;
          }
          FENCE();
          SLOT(15, ex(0));
          cwrite(adone, gp);
          if (st < 15) nextK();
          FENCE();
          SLOT(16, ex(1); ad(0)); SLOT(17, ex(2); ad(1)); SLOT(18, ex(3); ad(2)); SLOT(19, ex(4); ad(3));
          SLOT(20, ex(5); ad(4)); SLOT(21, ex(6); ad(5)); SLOT(22, ex(7); ad(6)); SLOT(23, ad(7));
          cvp(pf, s); FENCE();
          if (st == 1 || st == 8) { STAMP(2 + 4 * it + (st >> 3)); }
        }
        {
          const float inv = 1.f / xg_sum(lsum);
          const float fo = resc ? sc * inv : inv;
          s[0] *= inv; s[1] *= inv;
          cvp(pf, s);
          nops<4>();
#pragma unroll
          for (int dt = 0; dt < 12; ++dt) o[dt] *= fo;
          FENCE();
          nextV();
          STAMP(4 + 4 * it);
          bf16x8 v[12];
#pragma unroll
          for (int dt = 0; dt < 12; ++dt) v[dt] = trd2(vadr(dt), 6144);
          cwrite(adone, gp);
          FENCE();
#pragma unroll
          for (int dt = 0; dt < 12; ++dt) mfma(o[dt], v[dt], __builtin_bit_cast(bf16x8, pf));
          FENCE();
#pragma unroll
          for (int q = 0; q < 6; ++q) qf[q] = st6[q];
          FENCE();
#pragma unroll
          for (int q = 0; q < 6; ++q) st6[q] = pack2(o[2 * q], o[2 * q + 1]);
        }
      }
      storeO(qf, sown); storeO(st6, sown + 6144);
    }
  }
  STAMP(9);
  __builtin_amdgcn_s_setprio(0);

  PUT(41, wacc);
  relane();
  const bool team = wid >= 3;
  const unsigned flagt = (unsigned)(c.br * 4 + c.bp) * F_GRP + (team ? F_TEAM : 0u);
  constexpr unsigned wbase = 20;
  const Tag tag = get_tag(c);
  bf16x8 ob[12][2];
#pragma unroll
  for (int q = 0; q < 6; ++q) { ob[q][0] = qf[q]; ob[q][1] = st6[q]; }
  auto loadO = [&](int j) {
    const unsigned so = (unsigned)WS_O + (unsigned)((item & ~3) | ((hq + j / 6) & 3)) * OBLK + (unsigned)wid * 12288u + (j % 6 / 3) * 3072;
    if (j % 3 == 0) { bload_sc1<0>(ob[j % 12][0], vl, c.srdWs, so); bload_sc1<0>(ob[j % 12][1], vl, c.srdWs, so + 6144); }
    if (j % 3 == 1) { bload_sc1<1024>(ob[j % 12][0], vl, c.srdWs, so); bload_sc1<1024>(ob[j % 12][1], vl, c.srdWs, so + 6144); }
    if (j % 3 == 2) { bload_sc1<2048>(ob[j % 12][0], vl, c.srdWs, so); bload_sc1<2048>(ob[j % 12][1], vl, c.srdWs, so + 6144); }
    FENCE();
  };
  unsigned aO[2];
  aO[0] = lds0 + NK * PIECE + (4 * g + tq) * 320 + ((tp ^ (g & 1)) << 4) + 8 * (tp & 1);
  aO[1] = aO[0] ^ 8;
  f32x4 y[5][2][2];
  bf16x8 wf[8];
  auto wadr = [&](int n) { const int j = n / 10, k = n % 10; return aO[k & 1] + (j % 14 < 6 ? j % 14 * WPC : (j % 14 - 6) % 5 * PIECE) + (k >> 1) * 64; };
  auto stage = [&](auto jc) {
    constexpr int j = decltype(jc)::value;
    if (j >= 6) { waitvm<(j < 12 ? 22 : 46 - 2 * j)>(); pin(ob[j % 12][0]); pin(ob[j % 12][1]); FENCE(); }
#pragma unroll
    for (int k = 0; k < 10; ++k) {
      const int n = 10 * j + k;
      if (k == 3) { if (j < 10) pre = bump<C_G4 + 4 * (j % 10)>(c.ctl); if (j < 23) need<1>(have, ardy, wbase + j + 2, wacc); FENCE(); }
      if (!(k & 1)) pin(wf[(n + 1) & 7]);
#pragma unroll
      for (int i = 0; i < 2; ++i) {
        if (n < 10) mfma0(y[k >> 1][k & 1][i], wf[n & 7], ob[j % 12][i]);
        else mfma(y[k >> 1][k & 1][i], wf[n & 7], ob[j % 12][i]);
        FENCE();
        if (n + 7 < 240) { if (i) trhi(wf[(n + 7) & 7], wadr(n + 7) + 5120); else trlo(wf[(n + 7) & 7], wadr(n + 7)); }
        if (i == 0 && k == 7 && j < 22) { pin(pre); pre = cread(ardy); }
        if (i == 1 && k == 1 && j && j < 23) { have = rfl(pre); pins(have); }
        if (i == 0 && (k == 2 || k == 3) && (j % 14 == 5 || j == 10 || j == 13)) { aO[k & 1] += (j == 10 ? 5 : j == 13 ? 3 : -8) * PIECE; pin(aO[k & 1]); }
        FENCE();
      }
    }
    if (j >= 6 && j < 12) loadO(j + 12);
    if (j == ARR - 1) { arrive(c, team, team ? 3 : 2, flagt + F_O + (unsigned)c.h * FS, tag); STAMP(11); }
  };
  have = 0;
  need<1>(have, ardy, wbase + 1, wacc);
#pragma unroll
  for (int n = 0; n < 7; ++n) wf[n] = trd2(wadr(n), 5120);
  FENCE();
  STAMP(10);
  stage(IC(0)); stage(IC(1)); stage(IC(2)); stage(IC(3)); stage(IC(4)); stage(IC(5));
  STAMP(12);
  poll_flags(c.srdWs, flagt + F_O + (unsigned)(c.h & ~3) * FS + (vl & 48u) * (FS / 8) / 2u, tag);
  STAMP(13);
#pragma unroll
  for (int j = 6; j < 18; ++j) loadO(j);
  stage(IC(6));
  STAMP(15);
  stage(IC(7)); stage(IC(8)); stage(IC(9)); stage(IC(10)); stage(IC(11));
  STAMP(16); CSTAMP(60);
  stage(IC(12)); stage(IC(13)); stage(IC(14)); stage(IC(15)); stage(IC(16)); stage(IC(17));
  STAMP(17); CSTAMP(61);
  stage(IC(18)); stage(IC(19)); stage(IC(20)); stage(IC(21)); stage(IC(22)); stage(IC(23));
  nops<8>();
  STAMP(20); PUT(42, wacc); CSTAMP(62);

  const int grp = (c.br * 4 + c.bp) * 4 + hq, k8 = c.h >> 2;
  {
    const unsigned so = (unsigned)WS_P + (unsigned)(grp * 8 + k8) * PBLK + (unsigned)wid * 10240u;
#pragma unroll
    for (int i = 0; i < 2; ++i) {
      bstore_sc1<0>(pack2(y[0][0][i], y[0][1][i]), vl, c.srdWs, so + i * 5120); bstore_sc1<1024>(pack2(y[1][0][i], y[1][1][i]), vl, c.srdWs, so + i * 5120);
      bstore_sc1<2048>(pack2(y[2][0][i], y[2][1][i]), vl, c.srdWs, so + i * 5120); bstore_sc1<3072>(pack2(y[3][0][i], y[3][1][i]), vl, c.srdWs, so + i * 5120);
      bstore_sc1<0>(pack2(y[4][0][i], y[4][1][i]), vl, c.srdWs, so + i * 5120 + 4096);
    }
  }
  relane();
  arrive(c, 2 + team, team ? 3 : 2, flagt + F_P + (unsigned)(hq * 8 + k8) * FS, tag);
  STAMP(21);
  poll_flags(c.srdWs, flagt + F_P + (unsigned)(hq * 8 + (lane & 7)) * FS, tag);
  STAMP(22);
#pragma unroll 1
  for (int u = k8;; u = 8 + (k8 & 1)) {
    bf16x8 pr[8];
    const unsigned so = (unsigned)WS_P + (unsigned)(grp * 8) * PBLK + (unsigned)wid * 10240u + (unsigned)u * 1024u;
#pragma unroll
    for (int k = 0; k < 8; ++k) bload_sc1<0>(pr[k], vl, c.srdWs, so + k * PBLK);
    waitvm<0>();
#pragma unroll
    for (int k = 0; k < 8; ++k) pin(pr[k]);
    const int i = u >= 5, cp = u - 5 * i;
    reduce_store(pr, c.out + (size_t)(2 * c.bp * SQ + row0 + i * drow + l15) * (2 * DM) + c.br * DM + hq * 160 + cp * 32 + 8 * g, g & 1);
    if (u >= 8 || team || (k8 >> 1) != wid) break;
  }
  asm volatile(".Lcode_end:");
}

typedef unsigned long long u64;
typedef u64 u64x2 __attribute__((ext_vector_type(2)));
typedef u64 u64x4 __attribute__((ext_vector_type(4)));
__global__ __launch_bounds__(512, 2) void fused_kernel(const u16* xA, const u16* kA, const u16* vA, const u16* wqA, const u16* woA,
                                                       const u16* xB, const u16* kB, const u16* vB, const u16* wqB, const u16* woB,
                                                       u16* out_, char* ws_) {
  extern __shared__ __attribute__((aligned(16))) char lds[];
  Ctx c;
  const int xcd = blockIdx.x & 7, idx = blockIdx.x >> 3;
  c.h = 8 * (xcd >> 1) + (idx & 7); c.bp = idx >> 3; c.br = xcd & 1;
  u64x4 p4; u64 p1; u64x2 p2;
  asm volatile("s_load_dwordx8 %0, %3, %4\n\ts_load_dwordx2 %1, %3, %4 offset:32\n\ts_load_dwordx4 %2, %3, 0x50"
               : "=&s"(p4), "=&s"(p1), "=&s"(p2) : "s"(__builtin_amdgcn_kernarg_segment_ptr()), "s"(c.br * 40) : "memory");
  c.lds0 = (unsigned)(uintptr_t)lds - CTL;
  c.ctl = c.lds0 + CTL;
  pins(c.ctl);
  c.wid = rfl(threadIdx.x >> 6);
  {
    unsigned long long pc; unsigned clen;
    asm volatile("s_getpc_b64 %0\n.Lcode_here:\n\ts_mov_b32 %1, .Lcode_end-.Lcode_here" : "=s"(pc), "=s"(clen));
    c.srdC = __builtin_amdgcn_make_buffer_rsrc((void*)pc, (short)0, (int)clen, 0x00020000);
    c.cshare = (unsigned)(idx & 31) * 128u;
    if (c.wid == 0) touch(c.lds0 + CTL + C_JUNK + 1024, threadIdx.x * 4096u, c.srdC, c.cshare);
  }
  cwrite(c.lds0 + CTL + threadIdx.x * 4, c.wid > 4 ? 32 : 0);
  {
    const int lane = threadIdx.x & 63;
#pragma unroll
    for (int k = 0; k < 3; ++k) {
      const int row = ((8 * k + (lane >> 3)) * 11) >> 5;
      c.vw[0][k] = (unsigned)(row * (HD * 2 - 384) + ((lane ^ ((row >> 1) & 1)) << 4));
      c.vw[1][k] = c.vw[0][k] ^ 64;
      c.vb[k] = (unsigned)((lane ^ (((row >> 1) & 3) << 1)) << 4);
      pin(c.vw[0][k]); pin(c.vw[1][k]); pin(c.vb[k]);
    }
  }
  asm volatile("s_waitcnt lgkmcnt(0)\n\ts_barrier" : "+s"(p4), "+s"(p1), "+s"(p2) :: "memory");
  const u16* x = (const u16*)p4[0]; const u16* kc = (const u16*)p4[1]; const u16* vc = (const u16*)p4[2]; const u16* wq = (const u16*)p4[3]; const u16* wo = (const u16*)p1;
  char* ws = (char*)p2[1];
  c.out = (GAS u16*)p2[0];
  const size_t kvoff = (size_t)(2 * c.bp * NH + c.h) * SK * DH;
  const u64 xb = (u64)(x + (size_t)c.bp * 2 * SQ * DM);
  c.srdX = u32x4{(unsigned)xb, (unsigned)(xb >> 32) & 0xFFFFu, 2 * SQ * DM * 2, 0x00020000};
  c.rot2 = ((c.bp + c.h) & 1) * 2;
  c.srdW = __builtin_amdgcn_make_buffer_rsrc((void*)(wq + c.h * DH), (short)0, (DM * HD - c.h * DH) * 2, 0x00020000);
  c.baseK = (u64)(kc + kvoff) & 0xFFFFFFFFFFFFull;
  c.baseV = (u64)(vc + kvoff) & 0xFFFFFFFFFFFFull;
  c.srdWo = __builtin_amdgcn_make_buffer_rsrc((void*)(wo + (size_t)(c.h >> 2) * 4 * DH * DM + (c.h & 3) * 160), (short)0, (4 * DH * DM - (c.h & 3) * 160) * 2, 0x00020000);
  c.srdWs = __builtin_amdgcn_make_buffer_rsrc((void*)ws, (short)0, (int)WS_END, 0x00020000);
  if (__builtin_expect(c.wid == 7, 1)) {
    __builtin_amdgcn_s_setprio(3);
    loader(c, ws);
    asm volatile("s_endpgm" ::: "memory");
  }
  consumer(c, ws);
}

extern "C" void kernel_launch(void* const* d_in, const int* in_sizes, int n_in, void* d_out, int out_size, void* d_ws, size_t ws_size, hipStream_t stream) {
  (void)in_sizes; (void)n_in; (void)out_size; (void)ws_size;
  (void)hipFuncSetAttribute((const void*)fused_kernel, hipFuncAttributeMaxDynamicSharedMemorySize, LDS_BYTES);
  fused_kernel<<<256, 512, LDS_BYTES, stream>>>((const u16*)d_in[0], (const u16*)d_in[1], (const u16*)d_in[2], (const u16*)d_in[3], (const u16*)d_in[4],
                                                (const u16*)d_in[5], (const u16*)d_in[6], (const u16*)d_in[7], (const u16*)d_in[8], (const u16*)d_in[9],
                                                (u16*)d_out, (char*)d_ws);
}
```

```cpp
#include <hip/hip_runtime.h>
#include <hip/hip_bf16.h>
#include <stdint.h>
#include <type_traits>
#pragma clang diagnostic ignored "-Winline-asm"

typedef unsigned short u16;
typedef short bf16x8 __attribute__((ext_vector_type(8)));
typedef short s16x4 __attribute__((ext_vector_type(4)));
typedef float f32x4 __attribute__((ext_vector_type(4)));
typedef float f32x2 __attribute__((ext_vector_type(2)));
typedef __bf16 bf16x2_t __attribute__((ext_vector_type(2)));
typedef unsigned u32x4 __attribute__((ext_vector_type(4)));
typedef unsigned u32x2 __attribute__((ext_vector_type(2)));
typedef unsigned u32x8 __attribute__((ext_vector_type(8)));
typedef unsigned u32x16 __attribute__((ext_vector_type(16)));
typedef __amdgpu_buffer_rsrc_t rsrc_t;
#define LAS __attribute__((address_space(3)))
#define GAS __attribute__((address_space(1)))
#define DI __device__ __forceinline__

constexpr int SQ = 112, DM = 640, NH = 32, SK = 512, DH = 192, HD = NH * DH;
constexpr int PIECE = 12288;
constexpr int NK = 8;
constexpr int WPC = 10240;
constexpr int CTL = -4096;
constexpr int C_G1 = 0, C_G4 = 40, C_RK = 160, C_RW = 164;
constexpr int C_TAG = 168, C_CNT = 176, C_ZERO = 252, C_DONE = 256, C_JUNK = 2048;
constexpr int LDS_BYTES = 163840;
#define FS 128u
constexpr unsigned F_GRP = 129 * FS < 2048 ? 2048 : 129 * FS;
constexpr unsigned FLAG_BYTES = (8 * F_GRP + 4095) & ~4095u;
constexpr unsigned OBLK = 7 * 12 * 1024;
constexpr unsigned PBLK = 7 * 10 * 1024;
constexpr size_t WS_O = FLAG_BYTES;
constexpr size_t WS_P = WS_O + (size_t)256 * OBLK;
constexpr size_t WS_END = WS_P + (size_t)256 * PBLK;
constexpr float QSCALE = 0.07216878364870322f * 1.4426950408889634f;
constexpr float RESC_THR = 8.0f;
constexpr unsigned KVITEM = NH * SK * DH * 2;
#define XL 2
#define YDLY 3
#define ARR 4
#define PRA 2
#define PW 48
#define PWS 1
#define OST 60
#define PT0 1800
#define PTAU 455
#define PP0 16
#define PAT2 0xAAAAAAAAu

DI void mfma(f32x4& d, const bf16x8& a, const bf16x8& b) { asm("v_mfma_f32_16x16x32_bf16 %0, %1, %2, %0" : "+v"(d) : "v"(a), "v"(b)); }
DI void mfmac(f32x4& d, const bf16x8& a, const bf16x8& b, const f32x4& c) { asm("v_mfma_f32_16x16x32_bf16 %0, %1, %2, %3" : "=v"(d) : "v"(a), "v"(b), "v"(c)); }
DI void mfma0(f32x4& d, const bf16x8& a, const bf16x8& b) { asm("v_mfma_f32_16x16x32_bf16 %0, %1, %2, 0" : "=v"(d) : "v"(a), "v"(b)); }
#define FENCE() __builtin_amdgcn_sched_barrier(0)
template <int N> DI void nops() { FENCE(); asm volatile("s_nop %0" :: "i"(N - 1)); FENCE(); }
DI int lane_id() { int l; asm volatile("v_mbcnt_lo_u32_b32 %0, -1, 0\n\tv_mbcnt_hi_u32_b32 %0, -1, %0" : "=v"(l)); return l; }
DI unsigned rfl(unsigned x) { return __builtin_amdgcn_readfirstlane(x); }
DI unsigned cvtpk(float lo, float hi) { f32x2 v = {lo, hi}; bf16x2_t b = __builtin_convertvector(v, bf16x2_t); return __builtin_bit_cast(unsigned, b); }
DI s16x4 trd(unsigned a) { return __builtin_bit_cast(s16x4, __builtin_amdgcn_ds_read_tr16_b64_v4i16((LAS s16x4*)a)); }
DI void trlo(bf16x8& d, unsigned a) { const s16x4 t = trd(a); d[0] = t[0]; d[1] = t[1]; d[2] = t[2]; d[3] = t[3]; }
DI void trhi(bf16x8& d, unsigned a) { const s16x4 t = trd(a); d[4] = t[0]; d[5] = t[1]; d[6] = t[2]; d[7] = t[3]; }
DI bf16x8 trd2(unsigned a, unsigned d) { s16x4 lo = trd(a), hi = trd(a + d); return (bf16x8){lo[0], lo[1], lo[2], lo[3], hi[0], hi[1], hi[2], hi[3]}; }
DI bf16x8 ldsr128(unsigned a) { return *(const LAS bf16x8*)a; }
DI bf16x8 pack2(const f32x4& a, const f32x4& b) {
  u32x4 w = {cvtpk(a[0], a[1]), cvtpk(a[2], a[3]), cvtpk(b[0], b[1]), cvtpk(b[2], b[3])};
  return __builtin_bit_cast(bf16x8, w);
}
DI float bflo(unsigned w) { return __uint_as_float(w << 16); }
DI float bfhi(unsigned w) { return __uint_as_float(w & 0xFFFF0000u); }
DI float addf(float a, float b) { float d = a + b; asm("" : "+v"(d)); return d; }
DI f32x4 mul4(const f32x4& a, float b) { f32x4 d; for (int e = 0; e < 4; ++e) { float t = a[e] * b; asm("" : "+v"(t)); d[e] = t; } return d; }
DI int max3i(int a, float b, float c) { return max(max(a, __float_as_int(b)), __float_as_int(c)); }
DI f32x2 sw16(float v) { const unsigned u = __float_as_uint(v); const auto r = __builtin_amdgcn_permlane16_swap(u, u, false, false); return f32x2{__uint_as_float(r[0]), __uint_as_float(r[1])}; }
DI f32x2 sw32(float v) { const unsigned u = __float_as_uint(v); const auto r = __builtin_amdgcn_permlane32_swap(u, u, false, false); return f32x2{__uint_as_float(r[0]), __uint_as_float(r[1])}; }
DI float xg_max(float v) { f32x2 r = sw16(v); r = sw32(fmaxf(r[0], r[1])); return fmaxf(r[0], r[1]); }
DI float xg_sum(float v) { f32x2 r = sw16(v); r = sw32(r[0] + r[1]); return r[0] + r[1]; }

#define KVPOL " nt"
DI void dma4(unsigned m0v, unsigned v0, unsigned v1, unsigned v2, unsigned v3, rsrc_t srd, unsigned soff) {
  asm volatile("s_mov_b32 m0, %0\n\ts_nop 0\n\t"
               "buffer_load_dwordx4 %1, %5, %6 offen lds\n\t"
               "buffer_load_dwordx4 %2, %5, %6 offen offset:1024 lds\n\t"
               "buffer_load_dwordx4 %3, %5, %6 offen offset:2048 lds\n\t"
               "buffer_load_dwordx4 %4, %5, %6 offen offset:3072 lds"
               :: "s"(m0v), "v"(v0), "v"(v1), "v"(v2), "v"(v3), "s"(srd), "s"(soff) : "m0", "memory");
}
DI void dma3k(unsigned m0v, unsigned v0, unsigned v1, unsigned v2, u32x4 srd, unsigned soff) {
  asm volatile("s_mov_b32 m0, %0\n\ts_nop 0\n\t"
               "buffer_load_dwordx4 %1, %4, %5 offen" KVPOL " lds\n\t"
               "buffer_load_dwordx4 %2, %4, %5 offen offset:1024" KVPOL " lds\n\t"
               "buffer_load_dwordx4 %3, %4, %5 offen offset:2048" KVPOL " lds"
               :: "s"(m0v), "v"(v0), "v"(v1), "v"(v2), "s"(srd), "s"(soff) : "m0", "memory");
}
DI void dma3(unsigned m0v, unsigned v0, unsigned v1, unsigned v2, rsrc_t srd, unsigned soff) {
  asm volatile("s_mov_b32 m0, %0\n\ts_nop 0\n\t"
               "buffer_load_dwordx4 %1, %4, %5 offen lds\n\t"
               "buffer_load_dwordx4 %2, %4, %5 offen offset:1024 lds\n\t"
               "buffer_load_dwordx4 %3, %4, %5 offen offset:2048 lds"
               :: "s"(m0v), "v"(v0), "v"(v1), "v"(v2), "s"(srd), "s"(soff) : "m0", "memory");
}
DI void touch(unsigned m0v, unsigned v, rsrc_t srd, unsigned soff) {
  asm volatile("s_mov_b32 m0, %0\n\ts_nop 0\n\tbuffer_load_dword %1, %2, %3 offen lds" :: "s"(m0v), "v"(v), "s"(srd), "s"(soff) : "m0", "memory");
}
template <int OFF> DI void bload_sc1(bf16x8& d, unsigned v, rsrc_t srd, unsigned soff) {
  asm volatile("buffer_load_dwordx4 %0, %1, %2, %3 offen offset:%4 sc1" : "=v"(d) : "v"(v), "s"(srd), "s"(soff), "i"(OFF) : "memory");
}
template <int OFF> DI void bload_sc1_into(bf16x8& d, unsigned v, rsrc_t srd, unsigned soff) {
  asm volatile("buffer_load_dwordx4 %0, %1, %2, %3 offen offset:%4 sc1" : "+v"(d) : "v"(v), "s"(srd), "s"(soff), "i"(OFF) : "memory");
}
template <int OFF> DI void bstore_sc1(const bf16x8& d, unsigned v, rsrc_t srd, unsigned soff) {
  asm volatile("buffer_store_dwordx4 %0, %1, %2, %3 offen offset:%4 sc1\n\ts_nop 1" :: "v"(d), "v"(v), "s"(srd), "s"(soff), "i"(OFF) : "memory");
}
template <int N> DI void waitvm() { asm volatile("s_waitcnt vmcnt(%0)" :: "i"(N) : "memory"); }
template <typename T> DI void pin(T& x) { asm volatile("" : "+v"(x)); }
template <typename T> DI void pins(T& x) { asm volatile("" : "+s"(x)); }
DI void cbar() { asm volatile("" ::: "memory"); }

#define STAMP(i)
#define CSTAMP(i)
#define PUT(i, v)
#define WACC_BEGIN
#define WACC_END(w)

#define G1ST
struct Ctx {
  rsrc_t srdW, srdWo, srdWs, srdC;
  unsigned long long baseK, baseV, baseT;
  u32x4 srdX;
  GAS u16* out;
  unsigned lds0, ctl, cshare;
  unsigned vw[2][3], vb[3];
  int wid, br, bp, h, rot2;
};

DI unsigned cread(unsigned a) { return *(const volatile LAS unsigned*)a; }
DI void cwrite(unsigned a, unsigned v) { *(volatile LAS unsigned*)a = v; }
DI void done(unsigned a) { cbar(); __hip_atomic_fetch_add((LAS unsigned*)a, a, __ATOMIC_RELAXED, __HIP_MEMORY_SCOPE_WORKGROUP); cbar(); }
template <int A> DI unsigned bump(unsigned z) { cbar(); const unsigned r = __builtin_amdgcn_ds_append((LAS int*)z + A / 4); cbar(); return r; }
template <int SL> DI void await(unsigned& have, unsigned a, unsigned n, unsigned& wacc) {
  WACC_BEGIN;
#pragma unroll 1
  for (unsigned spins = 0;; ++spins) {
    have = rfl(cread(a));
    if (have >= n) break;
    if (spins > (1u << 20)) { have = ~0u; break; }
    __builtin_amdgcn_s_sleep(SL);
  }
  WACC_END(wacc);
}
template <int SL> DI void need(unsigned& have, unsigned a, unsigned n, unsigned& wacc) {
  if (__builtin_expect(have < n * 64, 0)) await<SL>(have, a, n * 64, wacc);
  cbar();
}

struct Tag { unsigned lo, hi; };
DI bool match(const u32x2& f, Tag tag) { return __all(f[0] == tag.lo && f[1] == tag.hi); }
DI void poll_flags(rsrc_t srdWs, unsigned vo, Tag tag) {
#pragma unroll 1
  for (unsigned spins = 0; spins < (1u << 20); ++spins) {
    u32x2 f;
    asm volatile("buffer_load_dwordx2 %0, %1, %2, 0 offen sc1\n\ts_waitcnt vmcnt(0)" : "=v"(f) : "v"(vo), "s"(srdWs) : "memory");
    if (match(f, tag)) break;
    __builtin_amdgcn_s_sleep(2);
  }
}
DI void set_flag(rsrc_t srdWs, unsigned off, int lane, Tag tag) {
  if (lane == 0) { const u32x2 t = {tag.lo, tag.hi}; asm volatile("buffer_store_dwordx2 %0, off, %1, %2 sc1\n\ts_nop 1" :: "v"(t), "s"(srdWs), "s"(off) : "memory"); }
}
DI Tag get_tag(const Ctx& c) { return Tag{rfl(cread(c.lds0 + CTL + C_TAG)), rfl(cread(c.lds0 + CTL + C_TAG + 4))}; }
DI void arrive(const Ctx& c, int which, unsigned last, unsigned off, Tag tag) {
  waitvm<0>();
  const int lane = lane_id();
  unsigned old = 0;
  if (lane == 0) old = __hip_atomic_fetch_add((LAS unsigned*)(c.lds0 + CTL + C_CNT + 4 * which), 1u, __ATOMIC_RELAXED, __HIP_MEMORY_SCOPE_WORKGROUP);
  if (rfl(old) == last) set_flag(c.srdWs, off, lane, tag);
}

DI void reduce_store(const bf16x8 (&pr)[8], GAS u16* dst, bool odd) {
  float a[8] = {0.f, 0.f, 0.f, 0.f, 0.f, 0.f, 0.f, 0.f};
#pragma unroll
  for (int k = 0; k < 8; ++k) {
    const u32x4 w = __builtin_bit_cast(u32x4, pr[k]);
#pragma unroll
    for (int e = 0; e < 4; ++e) { a[2 * e] += bflo(w[e]); a[2 * e + 1] += bfhi(w[e]); }
  }
  const unsigned u0 = cvtpk(a[0], a[1]), u1 = cvtpk(a[2], a[3]), u2 = cvtpk(a[4], a[5]), u3 = cvtpk(a[6], a[7]);
  const u32x4 w = {odd ? u2 : u0, odd ? u3 : u1, odd ? u0 : u2, odd ? u1 : u3};
  *(GAS u32x4*)dst = w;
}
constexpr unsigned F_O = FS < 128 ? 256 : FS, F_P = F_O + 32 * FS, F_TEAM = 64 * FS;

DI void loader(const Ctx& c, char* ws) {
  const int lane = lane_id();
  const unsigned lds0 = c.lds0;
  const int hq = c.h & 3;
  STAMP(0); CSTAMP(32); PUT(30, (unsigned)c.tent);
  unsigned long long tl0;
  asm volatile("s_memrealtime %0" : "=s"(tl0) :: "memory");
  const unsigned (&vw)[2][3] = c.vw; const unsigned (&vb)[3] = c.vb;
  auto wqpiece = [&](int t, unsigned m) {
    const unsigned so = (unsigned)(((t ^ c.rot2) >> 1) * 64 + (t & 1) * 8) * (HD * 2);
#pragma unroll
    for (int q = 0; q < 4; ++q) dma3(m + 3072 * q, vw[q & 1][0], vw[q & 1][1], vw[q & 1][2], c.srdW, so + q * (16 * HD * 2));
  };
  u32x4 srdT = {0u, 0u, KVITEM + SK * DH * 2, 0x00020000};
  auto pickKV = [&](unsigned p) {
    const unsigned long long b = (0xD5555554D5555554ull >> p) & 1 ? c.baseV : c.baseK;
    srdT[0] = (unsigned)b; srdT[1] = (unsigned)(b >> 32);
  };
#pragma unroll 1
  for (int t = 0; t < 10; ++t) {
    wqpiece(t, lds0 + (unsigned)(3 + t) * PIECE);
    waitvm<36>(); if (t >= 3) bump<C_RW>(c.ctl);
  }
  STAMP(24);
  const unsigned flag0 = (unsigned)(c.br * 4 + c.bp) * F_GRP;
  u32x2 f;
  asm volatile("buffer_load_dwordx2 %0, off, %1, %2 sc1" : "=v"(f) : "s"(c.srdWs), "s"(flag0) : "memory");
#pragma unroll 1
  for (int p = 0; p < 3; ++p) {
    pickKV(p);
#pragma unroll
    for (int k = 0; k < 4; ++k) dma3k(lds0 + p * PIECE + 3072 * k, vb[0], vb[1], vb[2], srdT, (p & 1) * PIECE + 3072 * k);
    waitvm<36>(); bump<C_RW>(c.ctl);
  }
  pin(f);
  const unsigned long long tg = (((unsigned long long)rfl(f[1]) << 32) | rfl(f[0])) + 1ull;
  const Tag tag{(unsigned)tg, (unsigned)(tg >> 32)};
  if (lane == 0) { cwrite(lds0 + CTL + C_TAG, tag.lo); cwrite(lds0 + CTL + C_TAG + 4, tag.hi); }
  STAMP(25);
  u32x4 st = {0x15u, 3u, 0u, 0u};
  unsigned tK;
  {
    const int q = lane & 31;
    const bool isv = ((q & 1) == 0 && q) || q == 31;
    tK = (unsigned)((isv ? q - 1 : q + 1) >> 1) * PIECE + (unsigned)(lane >> 5) * KVITEM + (unsigned)(lane < 12 ? lane : lane < 24 ? lane - 12 : lane & 7);
  }
  asm volatile("s_waitcnt lgkmcnt(0)" : "+s"(tl0) :: "memory");
  const unsigned tP = (unsigned)tl0 + (lane < PP0 ? 0u : PT0 + (unsigned)((lane - 16) * PTAU) / 10u);
  const unsigned atop = lds0 + CTL + (lane > 56 ? C_DONE + (lane - 57) * 260 : C_ZERO);
#define ANNOUNCE(W, B0, B1) "s_mov_b32 m0, 0\n\ts_waitcnt " W "\n\ts_bitcmp1_b32 s60, " B0 "\n\ts_cselect_b64 exec, -1, 0\n\tds_append v54 offset:160\n\t" \
                         "s_bitcmp1_b32 s60, " B1 "\n\ts_cselect_b64 exec, -1, 0\n\tds_append v54 offset:164\n\ts_mov_b64 exec, -1\n\t"
  static_assert(C_RK == 160 && C_RW == 164 && NK == 8 && PIECE == 0x3000, "");
#define LD4(A, B, C, D, R, P) "buffer_load_dwordx4 " A ", " R " offen" P " lds\n\tbuffer_load_dwordx4 " B ", " R " offen offset:1024" P " lds\n\t" \
                              "buffer_load_dwordx4 " C ", " R " offen offset:2048" P " lds\n\tbuffer_load_dwordx4 " D ", " R " offen offset:3072" P " lds\n\t"
#define ASTAMP(N, B)
  const unsigned stampv = 0;
#define LOOK "ds_read_b32 v53, v47\n\t"
#define PACED
#define STR_(x) #x
#define STR(x) STR_(x)
#define WOL 48
#define WT1 "buffer_load_dword v57, s[76:79], 0 offen lds\n\tv_add_u32 v57, " STR(WOL) "*128, v57\n\t"
#define WTOUCH "s_mov_b32 m0, 0x800\n\ts_nop 0\n\t" WT1
#define KTOUCH
  static_assert(C_JUNK == 0x800, "");
  unsigned tT = lane < WOL ? (unsigned)lane * 128u : 0x80000000u;
  const u32x4 sT = {(unsigned)c.baseT, (unsigned)(c.baseT >> 32), 61440u, 0x00020000};
#define STREAM(WTAIL, KTAIL, N3, N2, N1) \
      "s_mov_b32 s69, 0\n\t" LOOK "s_waitcnt lgkmcnt(0)\n" \
      "1:\tv_cmp_ge_i32_e64 s[64:65], v53, v48\n\t" \
      "v_readlane_b32 s66, v50, s61\n\tv_readlane_b32 s67, v51, s69\n\tv_readlane_b32 s68, v52, s69\n\tv_readlane_b32 s74, v56, s61\n\t" \
      "s_add_u32 s71, s61, s69\n\ts_cmp_eq_u32 s71, s56\n\ts_cbranch_scc1 9f\n\t" \
      "s_and_b32 s70, s66, 15\n\ts_mulk_i32 s70, 0x3000\n\ts_andn2_b32 s66, s66, 15\n\ts_bitcmp1_b32 s58, s71\n\ts_cbranch_scc0 4f\n\t" \
      "s_bitcmp1_b64 s[64:65], s69\n\ts_cbranch_scc1 2f\n" \
      "4:\ts_cmp_ge_u32 s65, s62\n\ts_cbranch_scc0 3f\n\t" PACED \
      "s_add_u32 m0, s70, s54\n\ts_bitcmp1_b64 s[52:53], s61\n\ts_cselect_b64 s[44:45], s[50:51], s[48:49]\n\t" \
      "v_add_u32 v48, v48, v49\n\ts_ashr_i32 s62, s62, s55\n\ts_add_u32 s61, s61, 1\n\ts_cmp_gt_u32 s61, 64\n\ts_cbranch_scc1 1b\n\t" \
      LD4("v44", "v45", "v46", "v44", "s[44:47], s66", KVPOL) \
      "s_add_u32 m0, m0, 0x1000\n\ts_add_u32 s66, s66, 0x1000\n\t" \
      LD4("v45", "v46", "v44", "v45", "s[44:47], s66", KVPOL) \
      "s_add_u32 m0, m0, 0x1000\n\ts_add_u32 s66, s66, 0x1000\n\t" LOOK \
      LD4("v46", "v44", "v45", "v46", "s[44:47], s66", KVPOL) KTAIL \
      "s_lshl2_add_u32 s60, s60, 1\n\t" \
      ANNOUNCE("vmcnt(" N3 ") lgkmcnt(0)", "6", "7") ASTAMP("s61", "9") \
      "s_branch 1b\n" \
      "2:\ts_mov_b32 m0, s68\n\ts_lshl2_add_u32 s60, s60, 2\n\t" \
      LD4("v32", "v33", "v34", "v35", "s[40:43], s67", "") \
      "s_add_u32 m0, m0, 0x1000\n\ts_add_u32 s69, s69, 1\n\t" \
      LD4("v36", "v37", "v38", "v39", "s[40:43], s67", "") \
      "s_add_u32 m0, m0, 0x1000\n\t" LOOK \
      WTAIL \
      ANNOUNCE("vmcnt(" N3 ") lgkmcnt(0)", "6", "7") ASTAMP("s69", "s57") \
      "s_branch 1b\n" \
      "3:\ts_bitcmp1_b64 s[64:65], s69\n\ts_cbranch_scc1 2b\n\t" \
        \
      LOOK "s_getreg_b32 s70, hwreg(HW_REG_IB_STS)\n\ts_bfe_u32 s71, s70, 0x20016\n\ts_and_b32 s70, s70, 15\n\ts_lshl4_add_u32 s70, s71, s70\n\t" \
      "s_and_b32 s71, s60, 0x30\n\ts_cbranch_scc0 5f\n\ts_cmp_gt_u32 s70, " N2 "\n\ts_cbranch_scc1 8f\n\t" \
      ANNOUNCE("vmcnt(" N2 ")", "4", "5") "s_and_b32 s60, s60, 15\n\ts_branch 8f\n" \
      "5:\ts_and_b32 s71, s60, 12\n\ts_cbranch_scc0 6f\n\ts_cmp_gt_u32 s70, " N1 "\n\ts_cbranch_scc1 8f\n\t" \
      ANNOUNCE("vmcnt(" N1 ")", "2", "3") "s_and_b32 s60, s60, 3\n\ts_branch 8f\n" \
      "6:\ts_and_b32 s71, s60, 3\n\ts_cbranch_scc0 8f\n\ts_cmp_lg_u32 s70, 0\n\ts_cbranch_scc1 8f\n\t" \
      ANNOUNCE("vmcnt(0)", "0", "1") "s_mov_b32 s60, 0\n" \
      "8:\ts_add_u32 s63, s63, 1\n\ts_waitcnt lgkmcnt(0)\n\ts_cmp_lt_u32 s63, 0x100000\n\ts_cbranch_scc1 1b\n\t" \
      "v_mov_b32 v53, 0x7ffffffe\n\ts_nop 0\n\tv_writelane_b32 v53, 0, 56\n\ts_branch 1b\n" \
      "9:\ts_waitcnt lgkmcnt(0)" \
      : "+{s[60:63]}"(st), "+{v[48:55]}"(vt), "+{s[44:47]}"(sk), "+{v57}"(tT) \
      : "{v[32:47]}"(va), "{s[40:43]}"(srd), "{s[48:55]}"(sa), "{s[56:59]}"(sc), "{v56}"(tP), "{s[76:79]}"(sT) \
      : "s64", "s65", "s66", "s67", "s68", "s69", "s70", "s71", "s72", "s73", "s74", "m0", "scc", "memory"
  auto stream = [&](auto wo, rsrc_t srd, const unsigned (&vo)[12], unsigned aG, int thr, int inc, unsigned tW, unsigned tM, unsigned total, unsigned req, unsigned sb, unsigned pat) {
    if (lane >= 56) { aG = atop; thr = ((int)st[1] - (lane == 56 ? 71 : wo ? 7 : 11)) * (int)atop; inc = (int)atop; }
    st[2] = req;
    const u32x16 va = {vo[0], vo[1], vo[2], vo[3], vo[4], vo[5], vo[6], vo[7], vo[8], vo[9], vo[10], vo[11], vb[0], vb[1], vb[2], aG};
    u32x8 vt = {(unsigned)thr, (unsigned)inc, tK, tW, tM, 0u, 0u, stampv};
    const u32x8 sa = {(unsigned)c.baseK, (unsigned)(c.baseK >> 32), (unsigned)c.baseV, (unsigned)(c.baseV >> 32), 0xD5555554u, 0xD5555554u, lds0, wo ? 0u : 1u};
    const u32x4 sc = {total, sb, pat, 0u};
    u32x4 sk = {0u, 0u, KVITEM + SK * DH * 2, 0x00020000};
    if constexpr (wo) asm volatile(STREAM("buffer_load_dwordx4 v40, s[40:43], s67 offen lds\n\tbuffer_load_dwordx4 v41, s[40:43], s67 offen offset:1024 lds\n\t", , "30", "20", "10"));
    else asm volatile(STREAM(LD4("v40", "v41", "v42", "v43", "s[40:43], s67", "") WTOUCH, KTOUCH, "36", "24", "12"));
    st = u32x4{rfl(st[0]), rfl(st[1]), 0u, rfl(st[3])};
  };
  {
    unsigned vo[12];
#pragma unroll
    for (int n = 0; n < 12; ++n) vo[n] = vw[(n / 3) & 1][n % 3] + (unsigned)(1024 * (n % 3) + (n / 3) * (16 * HD * 2) - 1024 * (n % 4));
    const int i = lane, t = i + 10;
    const int gl = i < 10 ? i : i > 46 ? 55 - i : 0;
    const int gt = i < 10 ? 7 * 64 : i == 10 ? 0x7FFFFFFF : i > 46 ? 14 * 64 : 0;
    stream(std::false_type{}, c.srdW, vo, lds0 + CTL + C_G1 + gl * 4, gt, 0, (unsigned)(((t ^ c.rot2) >> 1) * 64 + (t & 1) * 8) * (HD * 2),
           lds0 + (unsigned)(3 + i) * PIECE, 32 + 10, 0xFF800000u, 36, ~0u);
  }
  STAMP(1); CSTAMP(34);
  {
    unsigned vo[12];
#pragma unroll
    for (int n = 0; n < 10; ++n) {
      const int row = ((16 * (n % 5) + (lane >> 2)) * 205) >> 10;
      vo[n] = (unsigned)(row * (DM * 2 - 320) + ((lane ^ ((row >> 2) & 1)) << 4) + 1024 * (n % 5) + (n / 5) * (16 * DM * 2) - 1024 * (n % 4));
    }
    vo[10] = vo[11] = 0;
    const int j = lane, jj = j < 14 ? j : j - 14;
    static_assert(PW > 40, "K/V piece 40 has left: wq and the first two rounds of K/V are through");
    const int ga = j < 14 ? C_ZERO : C_G4 + 4 * jj;
    const int gt = j < 6 ? PW + PWS * j - 32 : j < 14 ? 59 + j - 32 : j < 24 ? 7 * 64 : 0x7FFFFFFF;
    stream(std::true_type{}, c.srdWo, vo, lds0 + CTL + ga, gt, j < 14 ? -1 : 0, (unsigned)((hq * 6 + j) % 24) * (32 * DM * 2),
           lds0 + (unsigned)(jj < 6 ? NK * PIECE + jj * WPC : (jj - 6) * PIECE), 72 + 24, 0xFF000000u, 41, PAT2);
    asm volatile(ANNOUNCE("vmcnt(20)", "4", "5") ANNOUNCE("vmcnt(10)", "2", "3") ANNOUNCE("vmcnt(0)", "0", "1") "s_waitcnt lgkmcnt(0)"
                 :: "{s[60:63]}"(st) : "v54", "m0", "scc", "memory");
  }
  PUT(8, (unsigned)c.tent + st[3]);
  STAMP(6);
  poll_flags(c.srdWs, flag0 + F_TEAM + F_P + (unsigned)(hq * 8 + (lane & 7)) * FS, tag);
  if (c.h == 0) set_flag(c.srdWs, flag0, lane, tag);
  STAMP(7);
  {
    const int k8 = c.h >> 2, w = 3 + (k8 >> 1), u = k8 & 1;
    const unsigned so = (unsigned)WS_P + (unsigned)(((c.br * 4 + c.bp) * 4 + hq) * 8) * PBLK + (unsigned)w * 10240u + 8192u + (unsigned)u * 1024u;
    bf16x8 pr[8];
#pragma unroll
    for (int k = 0; k < 8; ++k) bload_sc1<0>(pr[k], lane * 16, c.srdWs, so + k * PBLK);
    waitvm<0>();
#pragma unroll
    for (int k = 0; k < 8; ++k) pin(pr[k]);
    reduce_store(pr, c.out + (size_t)(2 * c.bp * SQ + (w == 3 ? 208 : 32 * w) + (lane & 15)) * (2 * DM) + c.br * DM + hq * 160 + (3 + u) * 32 + 8 * (lane >> 4), (lane >> 4) & 1);
  }
}

DI void consumer(const Ctx& c, char* ws) {
  const int wid = c.wid;
  int lane, l15, g, tq, tp;
  auto relane = [&]() { lane = lane_id(); l15 = lane & 15; g = lane >> 4; tq = l15 >> 2; tp = lane & 3; };
  relane();
  const unsigned lds0 = c.lds0;
  STAMP(0); CSTAMP(32); PUT(30, (unsigned)c.tent);
  const int hq = c.h & 3;
  const bool late = wid > 3;
  const int row0 = 32 * wid - (late ? 16 : 0), drow = wid == 3 ? SQ : 16;
  unsigned ardy = lds0 + CTL + C_RW;
  pin(ardy);
  unsigned have = 0, pre = 0, wacc = 1;

  bf16x8 qf[6], st6[6];
  {
    bf16x8 xr[5][2][2], xa[2], a[8];
    unsigned vx[2];
    vx[0] = (unsigned)((row0 + (l15 & 14)) * (DM * 2) + g * 32 + (lane & 1) * 16);
    vx[1] = vx[0] + drow * (DM * 2);
    u32x4 srdX = c.srdX;
    unsigned sx[2] = {(unsigned)c.rot2 * 64u + XL * 128u, XL * 128u - (unsigned)c.rot2 * 64u};
#define XLOAD(C, D, V, S, OFF) asm volatile("buffer_load_dwordx4 %0, %1, %2, %3 offen offset:%4" : C(D) : "v"(V), "s"(srdX), "s"(S), "i"(OFF) : "memory")
    auto xfirst = [&](int d) {
#pragma unroll
      for (int q = 0; q < 4; ++q) XLOAD("=v", xr[d][q & 1][q >> 1], vx[q >> 1], sx[d & 1] - XL * 128u + (d & 1 ? 128u : 0u), (d & ~1) * 128 + (q & 1) * (DM * 2));
    };
    unsigned long long even = 0x5555555555555555ull;
    pins(even);
    auto trade = [&](int d, int n) {
      const int i = (n >> 2) & 1, e = n & 3;
      u32x4 r0 = __builtin_bit_cast(u32x4, xr[d][0][i]), r1 = __builtin_bit_cast(u32x4, xr[d][1][i]), t = __builtin_bit_cast(u32x4, xa[i]);
#define XSEL "v_cndmask_b32_dpp %0, %1, %2, vcc quad_perm:[1,0,3,2] row_mask:0xf bank_mask:0xf"
      register unsigned long long m asm("vcc") = n < 8 ? even : ~even;
      if (n < 8) { asm volatile(XSEL : "=v"(t[e]) : "v"(r1[e]), "v"(r0[e]), "s"(m)); xa[i] = __builtin_bit_cast(bf16x8, t); }
      else { asm volatile(XSEL : "=v"(r1[e]) : "v"(r0[e]), "0"(r1[e]), "s"(m)); xr[d][1][i] = __builtin_bit_cast(bf16x8, r1); }
    };
    unsigned pos = 0, last = 11, lap = 0;
    if (late) need<8>(have, ardy, YDLY, wacc); else __builtin_amdgcn_s_setprio(1);
#pragma unroll
    for (int d = 0; d < 5; ++d) {
      if (d < XL) xfirst(d);
      else
#pragma unroll
        for (int q = 0; q < 4; ++q) asm volatile("" : "=v"(xr[d][q & 1][q >> 1]));
    }
    asm volatile("" : "=v"(xa[0])); asm volatile("" : "=v"(xa[1]));
    unsigned aW[2][2][2];
#pragma unroll
    for (int s1 = 0; s1 < 2; ++s1)
#pragma unroll
      for (int T = 0; T < 2; ++T) {
        aW[1][s1][T] = lds0 + NK * PIECE + (8 * g + tq) * 384 + (((4 * s1 + tp) ^ ((tq >> 1) | ((g & 1) << 2))) << 4) + 8 * (T ^ (tp & 1));
        aW[0][s1][T] = aW[1][s1][T] - 5 * PIECE;
        pin(aW[0][s1][T]); pin(aW[1][s1][T]);
      }
    f32x4 acc[12][2];
#pragma unroll
    for (int dt = 0; dt < 12; ++dt) { acc[dt][0] = f32x4{}; acc[dt][1] = f32x4{}; pin(acc[dt][0]); pin(acc[dt][1]); }
    auto adr = [&](int t, int K) { return aW[t / 5][(K >> 1) & 1][K & 1] + (t % 5) * PIECE + (K >> 2) * 128; };
    constexpr int XT = 0;
    auto xwait = [&](int d, auto first) {
      waitvm<4 * XL - 4 + (first ? 0 : XT)>();
#pragma unroll
      for (int q = 0; q < 4; ++q) pin(xr[d][q & 1][q >> 1]);
      FENCE();
    };
    need<1>(have, ardy, pos + 1, wacc);
    STAMP(29);
#pragma unroll
    for (int k = 0; k < 7; ++k) { a[k] = trd2(adr(0, k), 1536); FENCE(); }
    xwait(0, std::true_type{});
#pragma unroll
    for (int n = 0; n < 16; ++n) trade(0, n);
    nops<2>();
    auto step = [&](auto tc) {
      constexpr int t = decltype(tc)::value, T = t & 1, d = t >> 1, r = (d + XL) % 5;
#pragma unroll
      for (int k = 0; k < 12; ++k) {
        const int b = (12 * t + k) & 7, f = (b + 7) & 7;
        const int n = k < 3 ? 2 * k - 2 : k == 3 ? 4 : k == 4 ? 5 : 2 * k - 5;
        if (k == 5) { pre = bump<C_G1 + 4 * t>(c.ctl); need<1>(have, ardy, t < 9 ? pos + t + 2 : last, wacc); FENCE(); }
        if (T && k == 1) xwait((d + 1) % 5, std::false_type{});
        const unsigned ra = k < 5 ? adr(t, k + 7) : adr((t + 1) % 10, k - 5);
        if (!(k & 1)) pin(a[(b + 1) & 7]);
        mfma(acc[k][0], a[b], T ? xr[d][1][0] : xa[0]); FENCE();
        trlo(a[f], ra);
        if (k == 10) { pin(pre); pre = cread(ardy); }
        if (T && k >= 1 && k <= 9 && k != 5) trade((d + 1) % 5, n);
        FENCE();
        mfma(acc[k][1], a[b], T ? xr[d][1][1] : xa[1]); FENCE();
        trhi(a[f], ra + 1536);
        if (k == 3) { have = rfl(pre); pins(have); }
        if (T && k >= 1 && k <= 10 && k != 3 && k != 5) trade((d + 1) % 5, k == 10 ? 15 : n + 1);
        if (!T && (k == 1 || k == 2 || k == 7 || k == 8)) {
          const int q = k < 5 ? k - 1 : k - 5;
          XLOAD("+v", xr[r][q & 1][q >> 1], vx[q >> 1], sx[(d + XL) & 1], d * 128 + (q & 1) * (DM * 2));
        }
        if (t == 9 - 2 * XL && k == 8) { srdX[2] = lap ? 0u : srdX[2]; pins(srdX); }
        FENCE();
      }
    };
#define IC(n) std::integral_constant<int, n>{}
#pragma unroll 1
    for (; lap < 2; ++lap) {
      step(IC(0)); step(IC(1)); step(IC(2)); step(IC(3)); step(IC(4));
      step(IC(5)); step(IC(6)); step(IC(7)); step(IC(8)); step(IC(9));
      G1ST { STAMP(24 + lap); }
      pos += 10; last = 0;
      const unsigned u = sx[0]; sx[0] = sx[1] + 640u; sx[1] = u + 640u;
    }
    waitvm<0>();
    STAMP(26);
#pragma unroll
    for (int d = 0; d < 5; ++d)
#pragma unroll
      for (int q = 0; q < 4; ++q) pin(xr[d][q & 1][q >> 1]);
    nops<8>();
    relane();
    const bool odd = g & 1;
#pragma unroll
    for (int i = 0; i < 2; ++i)
#pragma unroll
      for (int s = 0; s < 6; ++s) {
        const f32x4 t0 = mul4(acc[2 * s][i], QSCALE), t1 = mul4(acc[2 * s + 1][i], QSCALE);
        const unsigned u0 = cvtpk(t0[0], t0[1]), u1 = cvtpk(t0[2], t0[3]), u2 = cvtpk(t1[0], t1[1]), u3 = cvtpk(t1[2], t1[3]);
        const u32x4 w = {odd ? u2 : u0, odd ? u3 : u1, odd ? u0 : u2, odd ? u1 : u3};
        (i ? st6[s] : qf[s]) = __builtin_bit_cast(bf16x8, w);
      }
  }
  STAMP(1); PUT(40, wacc); CSTAMP(34);
  __builtin_amdgcn_s_setprio(PRA);

  const int item = (c.br * 4 + c.bp) * 32 + c.h;
  const unsigned vl = (unsigned)lane * 16u;
  const unsigned sown = (unsigned)WS_O + (unsigned)item * OBLK + (unsigned)wid * 12288u;
  auto storeO = [&](const bf16x8 (&q)[6], unsigned so) {
    bstore_sc1<0>(q[0], vl, c.srdWs, so); bstore_sc1<1024>(q[1], vl, c.srdWs, so); bstore_sc1<2048>(q[2], vl, c.srdWs, so);
    bstore_sc1<0>(q[3], vl, c.srdWs, so + 3072); bstore_sc1<1024>(q[4], vl, c.srdWs, so + 3072); bstore_sc1<2048>(q[5], vl, c.srdWs, so + 3072);
  };
  {
    const unsigned adone = lds0 + CTL + C_DONE + wid * 256 + lane * 4;
    const unsigned ardk = ardy + (C_RK - C_RW);
    unsigned aK[2], aV[4];
    unsigned cnt = late ? 32 * 64 : 0;
    unsigned wrap = late ? 0x1110u : 0x1040u;
    int hop, back = late ? -(NK - 2) * PIECE : -10 * PIECE;
    auto start = [&]() {
      relane();
      aK[0] = lds0 + l15 * 384 + ((g ^ (((l15 >> 1) & 3) << 1)) << 4);
      aK[1] = aK[0] ^ 64;
#pragma unroll
      for (int d4 = 0; d4 < 4; ++d4) aV[d4] = lds0 + (d4 ? 0 : 2 * PIECE) + (4 * g + tq) * 384 + (((2 * d4 + (tp >> 1)) ^ (((2 * g + (tq >> 1)) & 3) << 1)) << 4) + 8 * (tp & 1);
      hop = 2 * PIECE;
    };
    have = 0; pre = 0;
    auto landed = [&]() { cnt += 64; if (__builtin_expect(have < cnt, 0)) await<1>(have, ardk, cnt, wacc); cbar(); };
    auto mov = [&](unsigned& a) { a += hop; pin(a); };
    auto look = [&]() { have = rfl(pre); pins(have); };
    int mx;
    bf16x8 w[8];
    auto rdK = [&](int n, int o = PIECE) { w[n & 7] = ldsr128(aK[(n % 6) & 1] + ((n % 6) >> 1) * 128 + (n / 6) * 6144 + o); };
    auto vadr = [&](int dt) { return aV[dt & 3] + (dt >> 2) * 128; };
    auto lanemax = [&](const f32x4 (&s)[2]) { return fmaxf(fmaxf(fmaxf(s[0][0], s[0][1]), fmaxf(s[0][2], s[0][3])), fmaxf(fmaxf(s[1][0], s[1][1]), fmaxf(s[1][2], s[1][3]))); };
    auto cvp = [&](u32x4& pf, const f32x4 (&s)[2]) { pf[0] = cvtpk(s[0][0], s[0][1]); pf[1] = cvtpk(s[0][2], s[0][3]); pf[2] = cvtpk(s[1][0], s[1][1]); pf[3] = cvtpk(s[1][2], s[1][3]); pin(pf); };
    if (wid != 3) {
      f32x4 o[2][12], s[2][2], negm[2];
      float lsum[2], sc[2];
      u32x4 pf[2];
      auto rdh = [&](int m, int hf) {
        if (m >= 12 && m < 24) { if (hf) trhi(w[m & 7], vadr(m - 12) + 6144); else trlo(w[m & 7], vadr(m - 12)); }
        else if (!hf) rdK(m % 24);
      };
      auto mfa = [&](int n, int i) {
        if (n >= 12) mfma(o[i][n - 12], w[n & 7], __builtin_bit_cast(bf16x8, pf[i]));
        else if (n % 6) mfma(s[i][n / 6], w[n & 7], (i ? st6 : qf)[n % 6]);
        else mfmac(s[i][n / 6], w[n & 7], (i ? st6 : qf)[0], negm[i]);
      };
      auto ex = [&](int e) { s[e >> 3][(e >> 2) & 1][e & 3] = __builtin_amdgcn_exp2f(s[e >> 3][(e >> 2) & 1][e & 3]); pin(s[e >> 3][(e >> 2) & 1]); };
      auto ad = [&](int e) { lsum[e >> 3] = addf(lsum[e >> 3], s[e >> 3][(e >> 2) & 1][e & 3]); };
      auto cv = [&](int i, int k) { pf[i][k] = cvtpk(s[i][k >> 1][2 * (k & 1)], s[i][k >> 1][2 * (k & 1) + 1]); pin(pf[i]); };
      auto mxs = [&](int i, int kt, int hf) { mx = max3i((i | kt | hf) ? mx : 0, s[i][kt][2 * hf], s[i][kt][2 * hf + 1]); };
#define SLOT2(n, A, B) do { if (!((n) & 1)) pin(w[((n) + 1) & 7]); mfa(n, 0); FENCE(); rdh(n + 7, 0); A; FENCE(); mfa(n, 1); FENCE(); rdh(n + 7, 1); B; FENCE(); } while (0)
      {
        start();
        landed();
#pragma unroll
        for (int n = 0; n < 7; ++n) rdK(n, 0);
        FENCE();
#pragma unroll
        for (int n = 0; n < 12; ++n) {
#pragma unroll
          for (int i = 0; i < 2; ++i) {
            if (n % 6) mfma(s[i][n / 6], w[n & 7], (i ? st6 : qf)[n % 6]); else mfma0(s[i][n / 6], w[n & 7], (i ? st6 : qf)[0]);
            FENCE();
            if (!i && n < 5) rdK(n + 7, 0);
            o[i][n] = f32x4{};
            FENCE();
          }
        }
        done(adone);
        landed();
#pragma unroll
        for (int n = 0; n < 7; ++n) rdK(n);
        nops<8>();
#pragma unroll
        for (int i = 0; i < 2; ++i) {
          const float m = xg_max(lanemax(s[i]));
          negm[i] = (f32x4){-m, -m, -m, -m};
          s[i][0] -= m; s[i][1] -= m;
          lsum[i] = 0.f; sc[i] = 1.f;
          asm volatile("" : "=v"(pf[i]));
        }
#pragma unroll
        for (int e = 0; e < 12; ++e) if (!(e & 4)) ex(e);
#pragma unroll
        for (int e = 0; e < 12; ++e) if (!(e & 4)) ad(e);
        cv(0, 0); cv(0, 1); cv(1, 0); cv(1, 1);
        cnt += 64;
        nops<2>();
      }
      int st = 1, lim = 16;
#pragma unroll 1
      do {
#pragma unroll 1
        do {
          SLOT2(0, ex(4), ex(5); ad(4)); SLOT2(1, ex(6); pre = cread(ardk), ex(7); ad(5)); SLOT2(2, ad(6); cv(0, 2), ex(12); ad(7));
          SLOT2(3, ex(13), ex(14); ad(12)); SLOT2(4, cv(0, 3); ad(13); look(), ex(15); ad(14); done(adone));
          if (__builtin_expect(have < cnt, 0)) await<1>(have, ardk, cnt, wacc);
          cbar(); FENCE();
          SLOT2(5, cv(1, 2); mov(aV[1]), ad(15); cv(1, 3));
          SLOT2(6, mov(aV[2]), mov(aV[3]));
          SLOT2(7, mxs(0, 0, 0), mxs(0, 0, 1)); SLOT2(8, mxs(1, 0, 0), mxs(1, 0, 1));
          SLOT2(9, mov(aK[0]), mov(aK[1])); SLOT2(10, ++st; pins(st), hop = (wrap >> st) & 1 ? back : 2 * PIECE; pins(hop));
          SLOT2(11, , ); SLOT2(12, , pre = cread(ardk));
          SLOT2(13, mxs(0, 1, 0), mxs(0, 1, 1)); SLOT2(14, mxs(1, 1, 0), mxs(1, 1, 1));
          if (__builtin_expect(__any(mx > __builtin_bit_cast(int, RESC_THR)), 0)) {
#pragma unroll
            for (int i = 0; i < 2; ++i) {
              const float d = fmaxf(xg_max(lanemax(s[i])), 0.f);
              s[i][0] -= d; s[i][1] -= d; negm[i] -= d;
              sc[i] = __builtin_amdgcn_exp2f(-d);
              lsum[i] *= sc[i];
            }
            lim = 0;
          }
          FENCE();
          SLOT2(15, ex(0), ex(1)); SLOT2(16, ex(2); look(), ad(0); done(adone));
          if (__builtin_expect(have <= cnt, 0)) { if (st < 16) await<1>(have, ardk, cnt + 64, wacc); }
          cbar(); FENCE();
          SLOT2(17, ad(1), ex(3)); SLOT2(18, ad(2), ex(8)); SLOT2(19, ad(3), ex(9));
          SLOT2(20, ad(8), ex(10)); SLOT2(21, ad(9), ex(11)); SLOT2(22, ad(10); cnt += 128; pins(cnt), ad(11); mov(aV[0]));
          SLOT2(23, cv(0, 0); cv(0, 1), cv(1, 0); cv(1, 1));
          if (st == 2 || st == 9) { STAMP(2 + (st >> 3)); CSTAMP(56 + (st >> 3)); }
        } while (st < lim);
        if (lim) break;
        nops<8>();
#pragma unroll
        for (int dt = 0; dt < 12; ++dt) { o[0][dt] *= sc[0]; o[1][dt] *= sc[1]; }
        lim = 16;
      } while (st < 16);
      {
        float inv[2];
#pragma unroll
        for (int e = 4; e < 16; ++e) if (e & 4) ex(e);
#pragma unroll
        for (int e = 4; e < 16; ++e) if (e & 4) ad(e);
#pragma unroll
        for (int i = 0; i < 2; ++i) {
          inv[i] = 1.f / xg_sum(lsum[i]);
          s[i][0] = mul4(s[i][0], inv[i]); s[i][1] = mul4(s[i][1], inv[i]);
          cvp(pf[i], s[i]);
        }
        nops<4>();
#pragma unroll
        for (int dt = 0; dt < 12; ++dt) { o[0][dt] = mul4(o[0][dt], inv[0]); o[1][dt] = mul4(o[1][dt], inv[1]); }
        aV[0] -= hop;
        FENCE();
        cnt -= 128;
        landed();
        STAMP(8);
        bf16x8 v[12];
#pragma unroll
        for (int dt = 0; dt < 12; ++dt) v[dt] = trd2(vadr(dt) + PIECE, 6144);
        cwrite(adone, 0x7FFFFFFF);
        FENCE();
#pragma unroll
        for (int i = 0; i < 2; ++i)
#pragma unroll
          for (int dt = 0; dt < 12; ++dt) mfma(o[i][dt], v[dt], __builtin_bit_cast(bf16x8, pf[i]));
        FENCE();
#pragma unroll
        for (int q = 0; q < 6; ++q) qf[q] = pack2(o[0][2 * q], o[0][2 * q + 1]);
        need<8>(have, ardk, OST, wacc);
        storeO(qf, sown);
        FENCE();
#pragma unroll
        for (int q = 0; q < 6; ++q) st6[q] = pack2(o[1][2 * q], o[1][2 * q + 1]);
        storeO(st6, sown + 6144);
      }
    } else {
      f32x4 o[12], s[2], negm;
      float lsum, sc;
      u32x4 pf;
      auto rda = [&](int n) { if (n < 12) rdK(n); else if (n < 24) w[n & 7] = trd2(vadr(n - 12), 6144); else rdK(n - 24); };
      auto mfa = [&](int n) {
        if (n >= 12) mfma(o[n - 12], w[n & 7], __builtin_bit_cast(bf16x8, pf));
        else if (n % 6) mfma(s[n / 6], w[n & 7], qf[n % 6]);
        else mfmac(s[n / 6], w[n & 7], qf[0], negm);
      };
      auto ex = [&](int e) { s[e >> 2][e & 3] = __builtin_amdgcn_exp2f(s[e >> 2][e & 3]); };
      auto ad = [&](int e) { lsum = addf(lsum, s[e >> 2][e & 3]); };
      auto mxs = [&](int kt, int hf) { mx = max3i((kt | hf) ? mx : 0, s[kt][2 * hf], s[kt][2 * hf + 1]); };
#define SLOT(n, A) do { mfa(n); FENCE(); rda(n + 8); A; FENCE(); } while (0)
#pragma unroll 1
      for (int it = 0; it < 2; ++it) {
        {
          start();
          landed();
#pragma unroll
          for (int n = 0; n < 8; ++n) rdK(n, 0);
          FENCE();
#pragma unroll
          for (int n = 0; n < 12; ++n) {
            if (n % 6) mfma(s[n / 6], w[n & 7], qf[n % 6]); else mfma0(s[n / 6], w[n & 7], qf[0]);
            FENCE();
            if (n < 4) rdK(n + 8, 0);
            o[n] = f32x4{};
            FENCE();
          }
          done(adone);
          landed();
#pragma unroll
          for (int n = 0; n < 8; ++n) rdK(n);
          nops<8>();
          const float m = xg_max(lanemax(s));
          negm = (f32x4){-m, -m, -m, -m};
          s[0] -= m; s[1] -= m;
          lsum = 0.f; sc = 1.f;
#pragma unroll
          for (int e = 0; e < 8; ++e) ex(e);
#pragma unroll
          for (int e = 0; e < 8; ++e) ad(e);
          cvp(pf, s);
          cnt += 64;
          nops<2>();
        }
        int st = 1, lim = 16;
#pragma unroll 1
        do {
#pragma unroll 1
          do {
            SLOT(0, pre = cread(ardk));
            SLOT(1, mov(aV[1])); SLOT(2, mov(aV[2]); mov(aV[3])); SLOT(3, look(); done(adone));
            if (__builtin_expect(have < cnt, 0)) await<1>(have, ardk, cnt, wacc);
            cbar(); FENCE();
            SLOT(4, mov(aK[0])); SLOT(5, mov(aK[1]));
            SLOT(6, ++st; pins(st)); SLOT(7, hop = (wrap >> st) & 1 ? back : 2 * PIECE; pins(hop));
            SLOT(8, mxs(0, 0)); SLOT(9, mxs(0, 1));
            SLOT(10, ); SLOT(11, );
            SLOT(12, pre = cread(ardk));
            SLOT(13, );
            SLOT(14, mxs(1, 0); mxs(1, 1));
            if (__builtin_expect(__any(mx > __builtin_bit_cast(int, RESC_THR)), 0)) {
              const float d = fmaxf(xg_max(lanemax(s)), 0.f);
              s[0] -= d; s[1] -= d; negm -= d;
              sc = __builtin_amdgcn_exp2f(-d);
              lsum *= sc;
              lim = 0;
            }
            FENCE();
            SLOT(15, ex(0); look(); done(adone));
            if (__builtin_expect(have <= cnt, 0)) { if (st < 16) await<1>(have, ardk, cnt + 64, wacc); }
            cbar(); FENCE();
            SLOT(16, ex(1); ad(0)); SLOT(17, ex(2); ad(1)); SLOT(18, ex(3); ad(2)); SLOT(19, ex(4); ad(3));
            SLOT(20, ex(5); ad(4)); SLOT(21, ex(6); ad(5)); SLOT(22, ex(7); ad(6); cnt += 128; pins(cnt)); SLOT(23, ad(7); mov(aV[0]));
            cvp(pf, s); FENCE();
            if (st == 2 || st == 9) { STAMP(2 + 4 * it + (st >> 3)); }
          } while (st < lim);
          if (lim) break;
          nops<8>();
#pragma unroll
          for (int dt = 0; dt < 12; ++dt) o[dt] *= sc;
          lim = 16;
        } while (st < 16);
        {
          const float inv = 1.f / xg_sum(lsum);
          s[0] *= inv; s[1] *= inv;
          cvp(pf, s);
          nops<4>();
#pragma unroll
          for (int dt = 0; dt < 12; ++dt) o[dt] *= inv;
          aV[0] -= hop;
          FENCE();
          cnt -= 128;
          landed();
          STAMP(4 + 4 * it);
          bf16x8 v[12];
#pragma unroll
          for (int dt = 0; dt < 12; ++dt) v[dt] = trd2(vadr(dt) + PIECE, 6144);
          done(adone);
          FENCE();
#pragma unroll
          for (int dt = 0; dt < 12; ++dt) mfma(o[dt], v[dt], __builtin_bit_cast(bf16x8, pf));
          FENCE();
#pragma unroll
          for (int q = 0; q < 6; ++q) qf[q] = st6[q];
          FENCE();
#pragma unroll
          for (int q = 0; q < 6; ++q) st6[q] = pack2(o[2 * q], o[2 * q + 1]);
          wrap = 0x1110u; back = -(NK - 2) * PIECE;
        }
      }
      storeO(qf, sown); storeO(st6, sown + 6144);
    }
  }
  STAMP(9);
  __builtin_amdgcn_s_setprio(0);

  PUT(41, wacc);
  relane();
  const bool team = wid >= 3;
  const unsigned flagt = (unsigned)(c.br * 4 + c.bp) * F_GRP + (team ? F_TEAM : 0u);
  constexpr unsigned wbase = 20;
  const Tag tag = get_tag(c);
  bf16x8 ob[12][2];
#pragma unroll
  for (int q = 0; q < 6; ++q) { ob[q][0] = qf[q]; ob[q][1] = st6[q]; }
  auto loadO = [&](int j) {
    const unsigned so = (unsigned)WS_O + (unsigned)((item & ~3) | ((hq + j / 6) & 3)) * OBLK + (unsigned)wid * 12288u + (j % 6 / 3) * 3072;
    if (j % 3 == 0) { bload_sc1<0>(ob[j % 12][0], vl, c.srdWs, so); bload_sc1<0>(ob[j % 12][1], vl, c.srdWs, so + 6144); }
    if (j % 3 == 1) { bload_sc1<1024>(ob[j % 12][0], vl, c.srdWs, so); bload_sc1<1024>(ob[j % 12][1], vl, c.srdWs, so + 6144); }
    if (j % 3 == 2) { bload_sc1<2048>(ob[j % 12][0], vl, c.srdWs, so); bload_sc1<2048>(ob[j % 12][1], vl, c.srdWs, so + 6144); }
    FENCE();
  };
  unsigned aO[2];
  aO[0] = lds0 + NK * PIECE + (4 * g + tq) * 320 + ((tp ^ (g & 1)) << 4) + 8 * (tp & 1);
  aO[1] = aO[0] ^ 8;
  f32x4 y[5][2][2];
  bf16x8 wf[8];
  auto wadr = [&](int n) { const int j = n / 10, k = n % 10; return aO[k & 1] + (j % 14 < 6 ? j % 14 * WPC : (j % 14 - 6) % 5 * PIECE) + (k >> 1) * 64; };
  auto stage = [&](auto jc) {
    constexpr int j = decltype(jc)::value;
    if (j >= 6) { waitvm<(j < 12 ? 22 : 46 - 2 * j)>(); pin(ob[j % 12][0]); pin(ob[j % 12][1]); FENCE(); }
#pragma unroll
    for (int k = 0; k < 10; ++k) {
      const int n = 10 * j + k;
      if (k == 3) { if (j < 10) pre = bump<C_G4 + 4 * (j % 10)>(c.ctl); if (j < 23) need<1>(have, ardy, wbase + j + 2, wacc); FENCE(); }
      if (!(k & 1)) pin(wf[(n + 1) & 7]);
#pragma unroll
      for (int i = 0; i < 2; ++i) {
        if (n < 10) mfma0(y[k >> 1][k & 1][i], wf[n & 7], ob[j % 12][i]);
        else mfma(y[k >> 1][k & 1][i], wf[n & 7], ob[j % 12][i]);
        FENCE();
        if (n + 7 < 240) { if (i) trhi(wf[(n + 7) & 7], wadr(n + 7) + 5120); else trlo(wf[(n + 7) & 7], wadr(n + 7)); }
        if (i == 0 && k == 7 && j < 22) { pin(pre); pre = cread(ardy); }
        if (i == 1 && k == 1 && j && j < 23) { have = rfl(pre); pins(have); }
        if (i == 0 && (k == 2 || k == 3) && (j % 14 == 5 || j == 10 || j == 13)) { aO[k & 1] += (j == 10 ? 5 : j == 13 ? 3 : -8) * PIECE; pin(aO[k & 1]); }
        FENCE();
      }
    }
    if (j >= 6 && j < 12) loadO(j + 12);
    if (j == ARR - 1) { arrive(c, team, team ? 3 : 2, flagt + F_O + (unsigned)c.h * FS, tag); STAMP(11); }
  };
  have = 0;
  need<1>(have, ardy, wbase + 1, wacc);
#pragma unroll
  for (int n = 0; n < 7; ++n) wf[n] = trd2(wadr(n), 5120);
  FENCE();
  STAMP(10);
  stage(IC(0)); stage(IC(1)); stage(IC(2)); stage(IC(3)); stage(IC(4)); stage(IC(5));
  STAMP(12);
  poll_flags(c.srdWs, flagt + F_O + (unsigned)(c.h & ~3) * FS + (vl & 48u) * (FS / 8) / 2u, tag);
  STAMP(13);
#pragma unroll
  for (int j = 6; j < 18; ++j) loadO(j);
  stage(IC(6));
  STAMP(15);
  stage(IC(7)); stage(IC(8)); stage(IC(9)); stage(IC(10)); stage(IC(11));
  STAMP(16); CSTAMP(60);
  stage(IC(12)); stage(IC(13)); stage(IC(14)); stage(IC(15)); stage(IC(16)); stage(IC(17));
  STAMP(17); CSTAMP(61);
  stage(IC(18)); stage(IC(19)); stage(IC(20)); stage(IC(21)); stage(IC(22)); stage(IC(23));
  nops<8>();
  STAMP(20); PUT(42, wacc); CSTAMP(62);

  const int grp = (c.br * 4 + c.bp) * 4 + hq, k8 = c.h >> 2;
  {
    const unsigned so = (unsigned)WS_P + (unsigned)(grp * 8 + k8) * PBLK + (unsigned)wid * 10240u;
#pragma unroll
    for (int i = 0; i < 2; ++i) {
      bstore_sc1<0>(pack2(y[0][0][i], y[0][1][i]), vl, c.srdWs, so + i * 5120); bstore_sc1<1024>(pack2(y[1][0][i], y[1][1][i]), vl, c.srdWs, so + i * 5120);
      bstore_sc1<2048>(pack2(y[2][0][i], y[2][1][i]), vl, c.srdWs, so + i * 5120); bstore_sc1<3072>(pack2(y[3][0][i], y[3][1][i]), vl, c.srdWs, so + i * 5120);
      bstore_sc1<0>(pack2(y[4][0][i], y[4][1][i]), vl, c.srdWs, so + i * 5120 + 4096);
    }
  }
  relane();
  arrive(c, 2 + team, team ? 3 : 2, flagt + F_P + (unsigned)(hq * 8 + k8) * FS, tag);
  STAMP(21);
  poll_flags(c.srdWs, flagt + F_P + (unsigned)(hq * 8 + (lane & 7)) * FS, tag);
  STAMP(22);
#pragma unroll 1
  for (int u = k8;; u = 8 + (k8 & 1)) {
    bf16x8 pr[8];
    const unsigned so = (unsigned)WS_P + (unsigned)(grp * 8) * PBLK + (unsigned)wid * 10240u + (unsigned)u * 1024u;
#pragma unroll
    for (int k = 0; k < 8; ++k) bload_sc1<0>(pr[k], vl, c.srdWs, so + k * PBLK);
    waitvm<0>();
#pragma unroll
    for (int k = 0; k < 8; ++k) pin(pr[k]);
    const int i = u >= 5, cp = u - 5 * i;
    reduce_store(pr, c.out + (size_t)(2 * c.bp * SQ + row0 + i * drow + l15) * (2 * DM) + c.br * DM + hq * 160 + cp * 32 + 8 * g, g & 1);
    if (u >= 8 || team || (k8 >> 1) != wid) break;
  }
  asm volatile(".Lcode_end:");
}

typedef unsigned long long u64;
typedef u64 u64x2 __attribute__((ext_vector_type(2)));
typedef u64 u64x4 __attribute__((ext_vector_type(4)));
__global__ __launch_bounds__(512, 2) void fused_kernel(const u16* xA, const u16* kA, const u16* vA, const u16* wqA, const u16* woA,
                                                       const u16* xB, const u16* kB, const u16* vB, const u16* wqB, const u16* woB,
                                                       u16* out_, char* ws_) {
  extern __shared__ __attribute__((aligned(16))) char lds[];
  Ctx c;
  const int xcd = blockIdx.x & 7, idx = blockIdx.x >> 3;
  c.h = 8 * (xcd >> 1) + (idx & 7); c.bp = idx >> 3; c.br = xcd & 1;
  u64x4 p4; u64 p1; u64x2 p2;
  asm volatile("s_load_dwordx8 %0, %3, %4\n\ts_load_dwordx2 %1, %3, %4 offset:32\n\ts_load_dwordx4 %2, %3, 0x50"
               : "=&s"(p4), "=&s"(p1), "=&s"(p2) : "s"(__builtin_amdgcn_kernarg_segment_ptr()), "s"(c.br * 40) : "memory");
  c.lds0 = (unsigned)(uintptr_t)lds - CTL;
  c.ctl = c.lds0 + CTL;
  pins(c.ctl);
  c.wid = rfl(threadIdx.x >> 6);
  {
    unsigned long long pc; unsigned clen;
    asm volatile("s_getpc_b64 %0\n.Lcode_here:\n\ts_mov_b32 %1, .Lcode_end-.Lcode_here" : "=s"(pc), "=s"(clen));
    c.srdC = __builtin_amdgcn_make_buffer_rsrc((void*)pc, (short)0, (int)clen, 0x00020000);
    c.cshare = (unsigned)(idx & 31) * 128u;
    if (c.wid == 0) touch(c.lds0 + CTL + C_JUNK + 1024, threadIdx.x * 4096u, c.srdC, c.cshare);
  }
  cwrite(c.lds0 + CTL + threadIdx.x * 4, c.wid > 4 ? 32 * (c.lds0 + CTL + threadIdx.x * 4) : 0);
  {
    const int lane = threadIdx.x & 63;
#pragma unroll
    for (int k = 0; k < 3; ++k) {
      const int row = ((8 * k + (lane >> 3)) * 11) >> 5;
      c.vw[0][k] = (unsigned)(row * (HD * 2 - 384) + ((lane ^ ((row >> 1) & 1)) << 4));
      c.vw[1][k] = c.vw[0][k] ^ 64;
      c.vb[k] = (unsigned)((lane ^ (((row >> 1) & 3) << 1)) << 4);
      pin(c.vw[0][k]); pin(c.vw[1][k]); pin(c.vb[k]);
    }
  }
  asm volatile("s_waitcnt lgkmcnt(0)\n\ts_barrier" : "+s"(p4), "+s"(p1), "+s"(p2) :: "memory");
  const u16* x = (const u16*)p4[0]; const u16* kc = (const u16*)p4[1]; const u16* vc = (const u16*)p4[2]; const u16* wq = (const u16*)p4[3]; const u16* wo = (const u16*)p1;
  char* ws = (char*)p2[1];
  c.out = (GAS u16*)p2[0];
  const size_t kvoff = (size_t)(2 * c.bp * NH + c.h) * SK * DH;
  const u64 xb = (u64)(x + (size_t)c.bp * 2 * SQ * DM);
  c.srdX = u32x4{(unsigned)xb, (unsigned)(xb >> 32) & 0xFFFFu, 2 * SQ * DM * 2, 0x00020000};
  c.rot2 = ((c.bp + c.h) & 1) * 2;
  c.srdW = __builtin_amdgcn_make_buffer_rsrc((void*)(wq + c.h * DH), (short)0, (DM * HD - c.h * DH) * 2, 0x00020000);
  c.baseK = (u64)(kc + kvoff) & 0xFFFFFFFFFFFFull;
  c.baseV = (u64)(vc + kvoff) & 0xFFFFFFFFFFFFull;
  c.srdWo = __builtin_amdgcn_make_buffer_rsrc((void*)(wo + (size_t)(c.h >> 2) * 4 * DH * DM + (c.h & 3) * 160), (short)0, (4 * DH * DM - (c.h & 3) * 160) * 2, 0x00020000);
  c.baseT = (u64)(wo + (size_t)(c.h >> 2) * 4 * DH * DM + (c.bp * 4 + (c.h & 3)) * 30720) & 0xFFFFFFFFFFFFull;
  c.srdWs = __builtin_amdgcn_make_buffer_rsrc((void*)ws, (short)0, (int)WS_END, 0x00020000);
  if (__builtin_expect(c.wid == 7, 1)) {
    __builtin_amdgcn_s_setprio(3);
    loader(c, ws);
    asm volatile("s_endpgm" ::: "memory");
  }
  consumer(c, ws);
}

extern "C" void kernel_launch(void* const* d_in, const int* in_sizes, int n_in, void* d_out, int out_size, void* d_ws, size_t ws_size, hipStream_t stream) {
  (void)in_sizes; (void)n_in; (void)out_size; (void)ws_size;
  (void)hipFuncSetAttribute((const void*)fused_kernel, hipFuncAttributeMaxDynamicSharedMemorySize, LDS_BYTES);
  fused_kernel<<<256, 512, LDS_BYTES, stream>>>((const u16*)d_in[0], (const u16*)d_in[1], (const u16*)d_in[2], (const u16*)d_in[3], (const u16*)d_in[4],
                                                (const u16*)d_in[5], (const u16*)d_in[6], (const u16*)d_in[7], (const u16*)d_in[8], (const u16*)d_in[9],
                                                (u16*)d_out, (char*)d_ws);
}
```
